```python
import torch

def _softmax(x, axis=-1):
    return torch.softmax(x, dim=axis)

def _gelu(x):
    return torch.nn.functional.gelu(x, approximate='tanh')

def _silu(x):
    return torch.nn.functional.silu(x)

def _sigmoid(x):
    return torch.sigmoid(x)

def _layernorm(x, axis=-1, eps=1e-05):
    m = x.mean(dim=axis, keepdim=True)
    v = x.var(dim=axis, keepdim=True, unbiased=False)
    return (x - m) / torch.sqrt(v + eps)

def _scatter_add(tgt, idx, src, axis=0):
    return tgt.clone().index_add_(axis, idx, src)

def _segment_sum(src, seg, n):
    out = torch.zeros((n,) + src.shape[1:], dtype=src.dtype)
    return out.index_add_(0, seg, src)

def _pad(x, pw):
    flat = tuple((v for pair in reversed(pw) for v in pair))
    return torch.nn.functional.pad(x, flat)
kernel_name = 'gated_mix__memory_aug_recurrent_mem_tr__sliding_block__prompt'

def setup_inputs():

    _w = lambda a: (lambda t: t.to(torch.bfloat16) if t.is_floating_point() else t)(torch.as_tensor(a))

    def _A_setup():
        B, S, Dm = (1, 2048, 576)
        H, Dh = (32, 128)
        M, seg = (8, 32)
        seg = min(seg, S)
        S = S // seg * seg
        return {'x': _w((lambda *s: torch.randn(*s, dtype=torch.bfloat16) * 0.05)(B, S, Dm)), 'mem0': _w((lambda *s: torch.randn(*s, dtype=torch.bfloat16) * 1.6)(B, M, Dm)), 'Wq': _w((lambda *s: torch.randn(*s, dtype=torch.bfloat16) * 0.8)(Dm, H, Dh) * Dm ** (-0.5)), 'Wk': _w((lambda *s: torch.randn(*s, dtype=torch.bfloat16) * 0.8)(Dm, H, Dh) * Dm ** (-0.5)), 'Wv': _w((lambda *s: torch.randn(*s, dtype=torch.bfloat16) * 1.6)(Dm, H, Dh) * Dm ** (-0.5)), 'Wo': _w((lambda *s: torch.randn(*s, dtype=torch.bfloat16) * 1.6)(H, Dh, Dm) * (H * Dh) ** (-0.5)), 'seg_len': seg}
    _A_in = _A_setup()

    def _B_setup():
        B, S, D = (1, 2048, 576)
        return {'x': _w((lambda *s: torch.randn(*s, dtype=torch.bfloat16) * 0.05)(B, S, D)), 'wqkv': _w((lambda *s: torch.randn(*s, dtype=torch.bfloat16) * 1.6)(D, 3 * D)), 'wo': _w((lambda *s: torch.randn(*s, dtype=torch.bfloat16) * 0.05)(D, D))}
    _B_in = _B_setup()
    return {'A_x': _A_in['x'], 'A_mem0': _A_in['mem0'], 'A_Wq': _A_in['Wq'], 'A_Wk': _A_in['Wk'], 'A_Wv': _A_in['Wv'], 'A_Wo': _A_in['Wo'], 'A_seg_len': _A_in['seg_len'], 'B_x': _B_in['x'], 'B_wqkv': _B_in['wqkv'], 'B_wo': _B_in['wo'], 'gate_w': _w((lambda *s: torch.randn(*s, dtype=torch.bfloat16) * 0.05)(576) * 16.0)}

def reference(A_x, A_mem0, A_Wq, A_Wk, A_Wv, A_Wo, A_seg_len, B_x, B_wqkv, B_wo, gate_w):

    def _A_ref(x, mem0, Wq, Wk, Wv, Wo, seg_len):
        B, S, Dm = x.shape
        H, Dh = (Wq.shape[1], Wq.shape[2])
        M = mem0.shape[1]
        scale = Dh ** (-0.5)
        n_seg = S // seg_len
        L = M + seg_len
        idx = torch.arange(L)
        mask = (idx[:, None] < M) | (idx[:, None] >= idx[None, :])
        mem = mem0
        outs = []
        for s in range(n_seg):
            xs = x[:, s * seg_len:(s + 1) * seg_len, :].clone()
            h = torch.concatenate([mem, xs], axis=1)
            q = torch.einsum('btd,dhe->bhte', h, Wq)
            k = torch.einsum('btd,dhe->bhte', h, Wk)
            v = torch.einsum('btd,dhe->bhte', h, Wv)
            scores = torch.einsum('bhqe,bhke->bhqk', q, k) * scale
            scores = torch.where(mask[None, None], scores, -1000000000.0)
            p = _softmax(scores, axis=-1)
            o = torch.einsum('bhqk,bhke->bhqe', p, v)
            o = torch.einsum('bhte,hed->btd', o, Wo)
            mem = o[:, :M, :].clone()
            outs.append(o[:, M:, :])
        return torch.concatenate(outs, axis=1)

    def _B_ref(x, wqkv, wo):
        B, S, D = x.shape
        H = 32
        Dh = D // H
        W = 256
        qkv = (lambda x, *d: x.permute(*d))((x @ wqkv).reshape(B, S, 3, H, Dh), 2, 0, 3, 1, 4)
        q, k, v = (qkv[0], qkv[1], qkv[2])
        scores = torch.einsum('bhqd,bhkd->bhqk', q, k) * Dh ** (-0.5)
        idx = torch.arange(S)
        mask = (idx[:, None] - idx[None, :] >= 0) & (idx[:, None] - idx[None, :] < W)
        scores = torch.where(mask[None, None], scores, -1000000000.0)
        p = _softmax(scores, axis=-1)
        o = torch.einsum('bhqk,bhkd->bhqd', p, v)
        return (lambda x, *d: x.permute(*d))(o, 0, 2, 1, 3).reshape(B, S, D) @ wo
    x = A_x
    g = _sigmoid((x * gate_w).sum(-1, keepdims=True))
    a = _A_ref(x, A_mem0, A_Wq, A_Wk, A_Wv, A_Wo, A_seg_len)
    b = _B_ref(x, B_wqkv, B_wo)
    return g * a + (1 - g) * b
```

```cpp
#include <hip/hip_runtime.h>
#include <hip/hip_bf16.h>

typedef unsigned short u16;
typedef unsigned v4u __attribute__((ext_vector_type(4)));
typedef unsigned v2u __attribute__((ext_vector_type(2)));
typedef unsigned v8u __attribute__((ext_vector_type(8)));
typedef __attribute__((ext_vector_type(8))) __bf16 bf16x8;
typedef __attribute__((ext_vector_type(16))) __bf16 bf16x16;
typedef __attribute__((ext_vector_type(4))) float f32x4;
typedef __attribute__((ext_vector_type(4))) short s16x4;
#define LAS __attribute__((address_space(3)))
#define DEV __device__ __forceinline__

namespace {

constexpr int NSEG = 64;
constexpr int NH = 32, NC = 72, NSPARE = 24, NWORK = 128;
constexpr unsigned SENT = 0xFFFFFFFFu;
constexpr unsigned SPIN_CAP = 1u << 22;
constexpr int NREP = 4;
constexpr int MEM_SLOT = 10240, P_SLOT = 20480, STEP_SLOT = NREP * (MEM_SLOT + P_SLOT);
constexpr int NPAGES = 8;
constexpr unsigned MBOX_BYTES = NPAGES * (2u << 20);

constexpr size_t CTRL_BYTES = 65536;
constexpr size_t OFF_WVO = 0;
constexpr size_t SZ_WVO = (size_t)72 * 294912;
constexpr size_t OFF_KX = OFF_WVO + SZ_WVO;
constexpr size_t SZ_KX = (size_t)64 * 32 * 8192;
constexpr size_t OFF_QX = OFF_KX + SZ_KX;
constexpr size_t OFF_UX = OFF_QX + SZ_KX;
constexpr size_t SZ_UX = (size_t)64 * 72 * 16384;
constexpr size_t OFF_PX = OFF_UX + SZ_UX;
constexpr size_t SZ_PX = (size_t)64 * 81920;
constexpr size_t OFF_UM = OFF_PX + SZ_PX;
constexpr size_t SZ_UM = (size_t)64 * 72 * 4096;
constexpr size_t OFF_QKB = OFF_UM + SZ_UM;
constexpr size_t SZ_QKB = (size_t)2048 * 1152 * 2 + 4096;
constexpr size_t OFF_VTB = OFF_QKB + SZ_QKB;
constexpr size_t SZ_VTB = (size_t)608 * 2048 * 2;
constexpr size_t OFF_OB = OFF_VTB + SZ_VTB;
constexpr size_t SZ_OB = (size_t)2048 * 576 * 2;
constexpr size_t OFF_WOT = OFF_OB + SZ_OB;
constexpr size_t SZ_WOT = (size_t)36 * 18 * 1024;
constexpr size_t OFF_GATE = OFF_WOT + SZ_WOT;
constexpr size_t SZ_GATE = 8192;
constexpr size_t DATA_BYTES = OFF_GATE + SZ_GATE;

constexpr int C_TMO = 0;
constexpr int C_POL = 64;
constexpr int C_CLR = 128;
constexpr int C_WVO = 192;
constexpr int C_AUX = 256;
constexpr int C_B1 = 320;
constexpr int C_FQ = 384;
constexpr int C_UX = 1024;
constexpr int C_PX = 1024 + 2048;
constexpr int C_UM = 1024 + 4096;
constexpr int C_OB = 1024 + 6144;
constexpr int C_KQ = 8192;

struct Params {
  const u16 *x, *mem0, *Wq, *Wk, *Wv, *Wo, *wqkv, *wo, *gw;
  u16* out;
  unsigned* ctrl;
  char* mbox;
  char* data;
};

DEV float bf2f(unsigned v) { return __builtin_bit_cast(float, v << 16); }
DEV unsigned cvtpk(float lo, float hi) { unsigned r; asm("v_cvt_pk_bf16_f32 %0, %1, %2" : "=v"(r) : "v"(lo), "v"(hi)); return r; }
DEV unsigned long long clk() { unsigned long long t; asm volatile("s_memtime %0\n\ts_waitcnt lgkmcnt(0)" : "=s"(t) :: "memory"); return t; }
DEV unsigned rt() { unsigned long long t; asm volatile("s_memrealtime %0\n\ts_waitcnt lgkmcnt(0)" : "=s"(t) :: "memory"); return (unsigned)t; }
DEV unsigned phys(unsigned o, unsigned polmask) { return ((o >> 13) << 14) | (((polmask >> (o >> 20)) & 1u) << 13) | (o & 8191u); }
DEV __amdgpu_buffer_rsrc_t mkrsrc(const void* p, unsigned bytes) { return __builtin_amdgcn_make_buffer_rsrc((void*)p, 0, (int)bytes, 0x00020000); }
DEV v4u ld_sc1(__amdgpu_buffer_rsrc_t r, unsigned off) { return __builtin_amdgcn_raw_buffer_load_b128(r, off, 0, 16); }
DEV v2u ld2_sc1(__amdgpu_buffer_rsrc_t r, unsigned off) { return __builtin_amdgcn_raw_buffer_load_b64(r, off, 0, 16); }
DEV unsigned ld1_sc1(__amdgpu_buffer_rsrc_t r, unsigned off) { return __builtin_amdgcn_raw_buffer_load_b32(r, off, 0, 16); }
DEV void st_sc1(v4u v, __amdgpu_buffer_rsrc_t r, unsigned off) { __builtin_amdgcn_raw_buffer_store_b128(v, r, off, 0, 16); }
DEV void st2_sc1(v2u v, __amdgpu_buffer_rsrc_t r, unsigned off) { __builtin_amdgcn_raw_buffer_store_b64(v, r, off, 0, 16); }
DEV void st1_sc1(unsigned v, __amdgpu_buffer_rsrc_t r, unsigned off) { __builtin_amdgcn_raw_buffer_store_b32(v, r, off, 0, 16); }
DEV void drain() { asm volatile("s_waitcnt vmcnt(0)" ::: "memory"); }
DEV unsigned max4(v4u v) { return max(max(v.x, v.y), max(v.z, v.w)); }
DEV v2u tr_read(const char* smem, unsigned off) {
  s16x4 v = __builtin_amdgcn_ds_read_tr16_b64_v4i16((LAS s16x4*)(smem + off));
  return __builtin_bit_cast(v2u, v);
}
DEV v4u trfrag(const char* smem, int off) {
  v2u x0 = tr_read(smem, off), x1 = tr_read(smem, off + 512);
  return (v4u){x0.x, x0.y, x1.x, x1.y};
}
DEV f32x4 mfma16(v4u a, v4u b, f32x4 c) {
  return __builtin_amdgcn_mfma_f32_16x16x32_bf16(__builtin_bit_cast(bf16x8, a), __builtin_bit_cast(bf16x8, b), c, 0, 0, 0);
}
DEV f32x4 smfma(v4u a, v8u b, f32x4 c, int idx) {
  return __builtin_amdgcn_smfmac_f32_16x16x64_bf16(__builtin_bit_cast(bf16x8, a), __builtin_bit_cast(bf16x16, b), c, idx, 0, 0);
}
template <int CTRL> DEV float dpp(float x) {
  return __builtin_bit_cast(float, __builtin_amdgcn_mov_dpp(__builtin_bit_cast(int, x), CTRL, 0xf, 0xf, true));
}
constexpr int ROR8 = 0x128;
DEV float amax(float a, float b) { float r; asm("v_max_f32 %0, %1, %2" : "=v"(r) : "v"(a), "v"(b)); return r; }
DEV float amax3(float a, float b, float c) { float r; asm("v_max3_f32 %0, %1, %2, %3" : "=v"(r) : "v"(a), "v"(b), "v"(c)); return r; }
DEV float xmax(float x) {
  auto s = __builtin_amdgcn_permlane16_swap(__float_as_uint(x), __float_as_uint(x), false, false);
  x = amax(__uint_as_float(s[0]), __uint_as_float(s[1]));
  auto t = __builtin_amdgcn_permlane32_swap(__float_as_uint(x), __float_as_uint(x), false, false);
  return amax(__uint_as_float(t[0]), __uint_as_float(t[1]));
}
DEV float xsum(float x) {
  auto s = __builtin_amdgcn_permlane16_swap(__float_as_uint(x), __float_as_uint(x), false, false);
  x = __uint_as_float(s[0]) + __uint_as_float(s[1]);
  auto t = __builtin_amdgcn_permlane32_swap(__float_as_uint(x), __float_as_uint(x), false, false);
  return __uint_as_float(t[0]) + __uint_as_float(t[1]);
}
DEV unsigned aload(unsigned* p) { return __hip_atomic_load(p, __ATOMIC_RELAXED, __HIP_MEMORY_SCOPE_AGENT); }
DEV void aadd(unsigned* p, unsigned v) { __hip_atomic_fetch_add(p, v, __ATOMIC_RELAXED, __HIP_MEMORY_SCOPE_AGENT); }
DEV void astore(unsigned* p, unsigned v) { __hip_atomic_store(p, v, __ATOMIC_RELAXED, __HIP_MEMORY_SCOPE_AGENT); }

DEV bool wait_ge(unsigned* w, unsigned target, unsigned* tmo, unsigned code) {
  for (unsigned spins = 0;; ++spins) {
    if ((unsigned)__builtin_amdgcn_readfirstlane(aload(w)) >= target) { __builtin_amdgcn_fence(__ATOMIC_ACQUIRE, "wavefront"); return true; }
    __builtin_amdgcn_s_sleep(2);
    if (spins > SPIN_CAP) { astore(tmo, code); return false; }
    if ((spins & 255u) == 255u && __builtin_amdgcn_readfirstlane(aload(tmo))) return false;
  }
}
DEV void wg_wait_ge(unsigned* w, unsigned target, unsigned* tmo, unsigned code, int wave) {
  if (wave == 0) wait_ge(w, target, tmo, code);
  __syncthreads();
}
DEV void wg_signal(unsigned* w, int tid) {
  drain();
  __syncthreads();
  if (tid == 0) aadd(w, 1u);
}
DEV void wait_until(unsigned t) {
  for (int guard = 0; guard < 64; ++guard) {
    int rem = (int)(t - rt());
    if (rem <= 0 || rem > 1000) return;
    if (rem <= 16) { int n = (rem * 24) >> 6; for (int i = 0; i < n; ++i) __builtin_amdgcn_s_sleep(1); return; }
    int n = ((rem - 12) * 24) >> 6; if (n > 16) n = 16;
    for (int i = 0; i < n; ++i) __builtin_amdgcn_s_sleep(1);
  }
}

constexpr int L_MEM = 0;
constexpr int L_KQ = 10240;
constexpr int L_QD = 43008;
constexpr int L_KI = 47104;
constexpr int L_UIMG = 10240;
constexpr int L_RED = 14336;
constexpr int L_DEAD = 51200;
constexpr int L_STAGE = 53248;
constexpr int L_X = 0;
constexpr int XROW = 1168, XBUF = 32 * XROW;
constexpr int L_V = 74752;
constexpr int L_WST = 82944;
constexpr int LDS_BYTES = 160 * 1024;

struct MemAddr { unsigned o0, o1, o2, l2; };
DEV MemAddr mem_addr(int s, int rep, unsigned pol, int wave, int lane) {
  const unsigned base = s * STEP_SLOT + rep * MEM_SLOT;
  MemAddr a;
  a.o0 = phys(base + wave * 1024 + lane * 16, pol);
  a.o1 = phys(base + (wave + 4) * 1024 + lane * 16, pol);
  a.l2 = (lane < 16) ? (unsigned)(8192 + wave * 256 + lane * 16) : 9216u;
  a.o2 = phys(base + a.l2, pol);
  asm volatile("" : "+v"(a.o0), "+v"(a.o1), "+v"(a.o2), "+v"(a.l2));
  return a;
}
DEV unsigned gather_mem(const Params& p, __amdgpu_buffer_rsrc_t rm, char* smem, int wave, int lane, const MemAddr& a) {
  v4u v0 = ld_sc1(rm, a.o0), v1 = ld_sc1(rm, a.o1), v2 = ld_sc1(rm, a.o2);
  unsigned mx = max(max(max4(v0), max4(v1)), max4(v2));
  unsigned spins = 0;
  if (__builtin_expect(__builtin_amdgcn_ballot_w64(mx == SENT) != 0, 0)) {
    for (;;) {
      asm volatile("" ::: "memory");
      v0 = ld_sc1(rm, a.o0); v1 = ld_sc1(rm, a.o1); v2 = ld_sc1(rm, a.o2);
      mx = max(max(max4(v0), max4(v1)), max4(v2));
      ++spins;
      if (__builtin_amdgcn_ballot_w64(mx == SENT) == 0) break;
      if (spins > SPIN_CAP) { astore(p.ctrl + C_TMO, 1u); *(LAS unsigned*)(smem + L_DEAD) = 1; break; }
      if ((spins & 1023) == 0 && __builtin_amdgcn_readfirstlane(aload(p.ctrl + C_TMO))) { *(LAS unsigned*)(smem + L_DEAD) = 1; break; }
    }
  }
  *(v4u*)(smem + L_MEM + wave * 1024 + lane * 16) = v0;
  *(v4u*)(smem + L_MEM + (wave + 4) * 1024 + lane * 16) = v1;
  *(v4u*)(smem + L_MEM + a.l2) = v2;
  return spins;
}
DEV void zero_acc(f32x4 (&acc)[4]) {
#pragma unroll
  for (int i = 0; i < 4; ++i) acc[i] = (f32x4){0, 0, 0, 0};
  asm volatile("" : "+v"(acc[0]), "+v"(acc[1]), "+v"(acc[2]), "+v"(acc[3]));
}
DEV void proj36(const char* smem, const v8u (&w)[4][9], int lane, f32x4 (&acc)[4]) {
  const int idx = (lane & 8) ? 0xEEEEEEEE : 0x44444444;
  v4u a[9];
  __builtin_amdgcn_sched_barrier(0);
#pragma unroll
  for (int t = 0; t < 9; ++t) a[t] = *(const v4u*)(smem + L_MEM + t * 1024 + lane * 16);
  __builtin_amdgcn_sched_barrier(0);
#pragma unroll
  for (int t = 0; t < 9; ++t) {
#pragma unroll
    for (int i = 0; i < 4; ++i) acc[i] = smfma(a[t], w[i][t], acc[i], idx);
  }
  __builtin_amdgcn_sched_barrier(0);
}
DEV v2u pairsum(f32x4 a0, f32x4 a1) {
  float r[4];
#pragma unroll
  for (int i = 0; i < 4; ++i) {
    auto sw = __builtin_amdgcn_permlane32_swap(__float_as_uint(a0[i]), __float_as_uint(a1[i]), false, false);
    r[i] = __uint_as_float(sw[0]) + __uint_as_float(sw[1]);
  }
  return (v2u){cvtpk(r[0], r[1]), cvtpk(r[2], r[3])};
}
DEV unsigned read_pol(const Params& p) {
  unsigned m = 0;
#pragma unroll
  for (int i = 0; i < NPAGES; ++i) m |= ((aload(p.ctrl + C_POL + i) - 1u) & 1u) << i;
  return __builtin_amdgcn_readfirstlane(m);
}

template <int WV>
DEV void head_loop(const Params& p, char* smem, int h, int lane, const v8u (&w)[4][9], unsigned pol) {
  constexpr int wave = WV;
  __amdgpu_buffer_rsrc_t rm = mkrsrc(p.mbox, MBOX_BYTES);
  __amdgpu_buffer_rsrc_t rkx = mkrsrc(p.data + OFF_KX, SZ_KX);
  __amdgpu_buffer_rsrc_t rqx = mkrsrc(p.data + OFF_QX, SZ_KX);
  __amdgpu_buffer_rsrc_t rpx = mkrsrc(p.data + OFF_PX, SZ_PX);
  const float SC = 0.08838834764831845f * 1.4426950408889634f;
  const int g = lane >> 4, qi = lane & 15, hi = (lane >> 3) & 1;
  const float NEG = -1e30f;
  const int wr_e = 64 * (wave & 1) + 16 * (lane >> 5) + (lane & 15);
  const int wr_off = wr_e * 32 + ((lane >> 4) & 1) * 8;
  const float c0 = (g < 2) ? 0.f : NEG;
  const int rep = h & 3;
  unsigned tM = rt();
  for (int s = 0; s < NSEG; ++s) {
    v4u kx[2][4], qx[4];
    unsigned adx[NREP];
    if (wave < 3) {
#pragma unroll
      for (int tl = 0; tl < 2; ++tl)
#pragma unroll
        for (int u = 0; u < 4; ++u) kx[tl][u] = *(const v4u*)(smem + L_KQ + (s & 1) * 16384 + (tl * 4 + u) * 1024 + lane * 16);
      if (wave > 0) {
#pragma unroll
        for (int u = 0; u < 4; ++u) qx[u] = *(const v4u*)(smem + L_KQ + (s & 1) * 16384 + 8192 + ((wave - 1) * 4 + u) * 1024 + lane * 16);
      }
    }
    if (wave == 0) {
#pragma unroll
      for (int rp = 0; rp < NREP; ++rp) {
        const unsigned base = s * STEP_SLOT + NREP * MEM_SLOT + rp * P_SLOT;
        adx[rp] = hi ? phys(base + 16384 + (h >> 3) * 1024 + (h & 3) * 256 + ((h >> 2) & 1) * 128 + (qi & 7) * 16, pol)
                     : phys(base + (h >> 1) * 1024 + g * 256 + (h & 1) * 128 + (qi & 7) * 16, pol);
        if (hi && g != 0) adx[rp] = 0x7ffffff0u;
        asm volatile("" : "+v"(adx[rp]));
      }
    }
    MemAddr ma = mem_addr(s, rep, pol, wave, lane);
    f32x4 acc[4]; zero_acc(acc);
    f32x4 t0 = {c0, c0, c0, c0}, t1 = {0, 0, 0, 0}, t2 = {0, 0, 0, 0};
    if (wave == 1 || wave == 2) {
#pragma unroll
      for (int u = 0; u < 4; ++u) { t1 = mfma16(kx[0][u], qx[u], t1); t2 = mfma16(kx[1][u], qx[u], t2); }
    }
    asm volatile("" : "+v"(t0), "+v"(t1), "+v"(t2));
    wait_until(tM);
    gather_mem(p, rm, smem, wave, lane, ma);
    __syncthreads();
    proj36(smem, w, lane, acc);
    v2u pk0 = pairsum(acc[0], acc[1]), pk1 = pairsum(acc[2], acc[3]);
    if (wave < 2) {
      *(v2u*)(smem + L_QD + wr_off) = pk0; *(v2u*)(smem + L_QD + wr_off + 1024) = pk1;
      *(v2u*)(smem + L_QD + wr_off + 16) = pk0; *(v2u*)(smem + L_QD + wr_off + 1024 + 16) = pk1;
    } else {
      *(v2u*)(smem + L_KI + wr_off) = pk0; *(v2u*)(smem + L_KI + wr_off + 1024) = pk1;
    }
    __syncthreads();
    if (wave == 0) {
      v4u qd[4], km[4];
#pragma unroll
      for (int u = 0; u < 4; ++u) qd[u] = trfrag(smem, L_QD + u * 1024 + lane * 8);
#pragma unroll
      for (int u = 0; u < 4; ++u) km[u] = trfrag(smem, L_KI + u * 1024 + lane * 8);
      __builtin_amdgcn_sched_barrier(0);
#pragma unroll
      for (int u = 0; u < 4; ++u) { t1 = mfma16(kx[0][u], qd[u], t1); t2 = mfma16(kx[1][u], qd[u], t2); }
#pragma unroll
      for (int u = 0; u < 4; ++u) t0 = mfma16(km[u], qd[u], t0);
      float v0 = (hi ? t2[0] : t1[0]) * SC, v1 = (hi ? t2[1] : t1[1]) * SC, v2 = (hi ? t2[2] : t1[2]) * SC, v3 = (hi ? t2[3] : t1[3]) * SC;
      float v4 = (hi ? t0[2] : t0[0]) * SC, v5 = (hi ? t0[3] : t0[1]) * SC;
      float m = amax(amax3(v0, v1, v2), amax3(v3, v4, v5));
      m = amax(m, dpp<ROR8>(m));
      m = xmax(m);
      v0 = __builtin_amdgcn_exp2f(v0 - m); v1 = __builtin_amdgcn_exp2f(v1 - m); v2 = __builtin_amdgcn_exp2f(v2 - m);
      v3 = __builtin_amdgcn_exp2f(v3 - m); v4 = __builtin_amdgcn_exp2f(v4 - m); v5 = __builtin_amdgcn_exp2f(v5 - m);
      float l = ((v0 + v1) + (v2 + v3)) + (v4 + v5);
      l += dpp<ROR8>(l);
      l = xsum(l);
      float inv = __builtin_amdgcn_rcpf(l);
      v2u px = {min(cvtpk(v0 * inv, v1 * inv), 0xFFFFFFFEu), min(cvtpk(v2 * inv, v3 * inv), 0xFFFFFFFEu)};
      unsigned pm = min(cvtpk(v4 * inv, v5 * inv), 0xFFFFFFFEu);
      unsigned ox = __builtin_amdgcn_mov_dpp(px.x, ROR8, 0xf, 0xf, true), oy = __builtin_amdgcn_mov_dpp(px.y, ROR8, 0xf, 0xf, true);
      unsigned pmo = __builtin_amdgcn_mov_dpp(pm, ROR8, 0xf, 0xf, true);
      auto s1 = __builtin_amdgcn_permlane16_swap(pm, pm, false, false);
      auto s2 = __builtin_amdgcn_permlane16_swap(pmo, pmo, false, false);
      v4u o;
      o.x = hi ? pmo : px.x; o.y = hi ? pm : px.y; o.z = hi ? s2[1] : ox; o.w = hi ? s1[1] : oy;
#pragma unroll
      for (int rp = 0; rp < NREP; ++rp) st_sc1(o, rm, adx[rp]);
    } else if (wave < 3) {
      v4u km[4];
#pragma unroll
      for (int u = 0; u < 4; ++u) km[u] = trfrag(smem, L_KI + u * 1024 + lane * 8);
#pragma unroll
      for (int u = 0; u < 4; ++u) t0 = mfma16(km[u], qx[u], t0);
      float v[12];
      const int tq = 16 * (wave - 1) + qi;
#pragma unroll
      for (int r = 0; r < 4; ++r) {
        v[r] = t0[r] * SC;
        v[4 + r] = (4 * g + r <= tq) ? t1[r] * SC : NEG;
        v[8 + r] = (16 + 4 * g + r <= tq) ? t2[r] * SC : NEG;
      }
      float m = amax3(amax3(v[0], v[1], v[2]), amax3(v[3], v[4], v[5]), amax(amax3(v[6], v[7], v[8]), amax3(v[9], v[10], v[11])));
      m = xmax(m);
      float l = 0;
#pragma unroll
      for (int i = 0; i < 12; ++i) { v[i] = __builtin_amdgcn_exp2f(v[i] - m); l += v[i]; }
      l = xsum(l);
      float inv = __builtin_amdgcn_rcpf(l);
#pragma unroll
      for (int i = 0; i < 12; ++i) v[i] *= inv;
      v4u px = {cvtpk(v[4], v[5]), cvtpk(v[6], v[7]), cvtpk(v[8], v[9]), cvtpk(v[10], v[11])};
      v2u pm = {cvtpk(v[0], v[1]), cvtpk(v[2], v[3])};
      constexpr int qt = wave - 1;
      st_sc1(px, rpx, s * 81920 + (qt * 32 + h) * 1024 + lane * 16);
      if (g < 2) st2_sc1(pm, rpx, s * 81920 + 65536 + ((qt * 8 + (h >> 2)) * 64 + (h & 3) * 16 + qi) * 16 + g * 8);
      drain();
      if (lane == 0) aadd(p.ctrl + C_PX + s * 32, 1u);
    } else {
      for (int i = 0; i < 8; ++i) __builtin_amdgcn_s_sleep(1);
      if (s + 1 < NSEG) {
        if (!wait_ge(p.ctrl + C_KQ + (s + 1) * 32 + h, 1u, p.ctrl + C_TMO, 2u)) *(LAS unsigned*)(smem + L_DEAD) = 1;
        v4u v[16];
#pragma unroll
        for (int i = 0; i < 8; ++i) v[i] = ld_sc1(rkx, ((s + 1) * 32 + h) * 8192 + i * 1024 + lane * 16);
#pragma unroll
        for (int i = 0; i < 8; ++i) v[8 + i] = ld_sc1(rqx, ((s + 1) * 32 + h) * 8192 + i * 1024 + lane * 16);
#pragma unroll
        for (int i = 0; i < 16; ++i) *(v4u*)(smem + L_KQ + ((s + 1) & 1) * 16384 + i * 1024 + lane * 16) = v[i];
      }
    }
    tM = __builtin_amdgcn_readfirstlane(*(const LAS unsigned*)(smem + L_MEM + 9216 + 4));
    __syncthreads();
    if (*(volatile LAS unsigned*)(smem + L_DEAD)) break;
  }
}

DEV void head_role(const Params& p, char* smem, int h, int wave, int lane, int tid) {
  v8u w[4][9];
  {
    const int gB = lane >> 4, q4 = (lane & 15) >> 2, p4 = lane & 3;
    v4u st[8];
    auto ldstage = [&](int t) {
#pragma unroll
      for (int i = 0; i < 8; ++i) {
        int ch = tid + 256 * i; int row = ch >> 5, cc = ch & 31;
        const u16* src = (cc < 16 ? p.Wq : p.Wk) + (size_t)(64 * t + row) * 4096 + h * 128 + (cc & 15) * 8;
        st[i] = *(const v4u*)src;
      }
    };
    ldstage(0);
#pragma unroll
    for (int t = 0; t < 9; ++t) {
      __syncthreads();
#pragma unroll
      for (int i = 0; i < 8; ++i) { int ch = tid + 256 * i; int row = ch >> 5, cc = ch & 31; *(v4u*)(smem + L_STAGE + row * 528 + cc * 16) = st[i]; }
      if (t < 8) ldstage(t + 1);
      __syncthreads();
#pragma unroll
      for (int tl = 0; tl < 4; ++tl) {
        v8u ww;
#pragma unroll
        for (int X = 0; X < 2; ++X)
#pragma unroll
          for (int half = 0; half < 2; ++half) {
            int row0 = 8 * (4 * half + 2 * X + (gB >> 1)) + 4 * (gB & 1);
            v2u v = tr_read(smem, L_STAGE + (row0 + q4) * 528 + (64 * wave + 16 * tl + 4 * p4) * 2);
            ww[4 * X + half] = v.x; ww[4 * X + 2 + half] = v.y;
          }
        w[tl][t] = ww;
      }
    }
  }
  for (int i = tid; i < 2048; i += 256) *(unsigned*)(smem + L_QD + i * 4) = 0;
  if (tid == 0) *(unsigned*)(smem + L_DEAD) = 0;
  if (wave == 0) { wait_ge(p.ctrl + C_CLR, NSPARE, p.ctrl + C_TMO, 3u); wait_ge(p.ctrl + C_KQ + h, 1u, p.ctrl + C_TMO, 4u); }
  __syncthreads();
  const unsigned pol = read_pol(p);
  {
    __amdgpu_buffer_rsrc_t rkx = mkrsrc(p.data + OFF_KX, SZ_KX);
    __amdgpu_buffer_rsrc_t rqx = mkrsrc(p.data + OFF_QX, SZ_KX);
    v4u v[4];
#pragma unroll
    for (int i = 0; i < 4; ++i) { int f = wave * 4 + i; v[i] = ld_sc1(wave < 2 ? rkx : rqx, h * 8192 + (f & 7) * 1024 + lane * 16); }
#pragma unroll
    for (int i = 0; i < 4; ++i) *(v4u*)(smem + L_KQ + (wave * 4 + i) * 1024 + lane * 16) = v[i];
  }
  __syncthreads();
  if (wave == 0) head_loop<0>(p, smem, h, lane, w, pol);
  else if (wave == 1) head_loop<1>(p, smem, h, lane, w, pol);
  else if (wave == 2) head_loop<2>(p, smem, h, lane, w, pol);
  else head_loop<3>(p, smem, h, lane, w, pol);
}

#define SCHED_A0 130
#define SCHED_B0 72
DEV void col_role(const Params& p, char* smem, int c, int wave, int lane, int tid) {
  __amdgpu_buffer_rsrc_t rm = mkrsrc(p.mbox, MBOX_BYTES);
  __amdgpu_buffer_rsrc_t rux = mkrsrc(p.data + OFF_UX, SZ_UX);
  __amdgpu_buffer_rsrc_t rum = mkrsrc(p.data + OFF_UM, SZ_UM);
  __amdgpu_buffer_rsrc_t rwv = mkrsrc(p.data + OFF_WVO, SZ_WVO);
  if (tid == 0) *(unsigned*)(smem + L_DEAD) = 0;
  if (wave == 0) wait_ge(p.ctrl + C_CLR, NSPARE, p.ctrl + C_TMO, 5u);
  __syncthreads();
  const unsigned pol = read_pol(p);
  const int rep = c & 3;
  if (wave == 0) {
    if (lane < 8 * NREP) {
      int r = lane & 7, rp = lane >> 3;
      v4u v = *(const v4u*)(p.mem0 + r * 576 + 8 * c);
      v.x = min(v.x, 0xFFFFFFFEu); v.y = min(v.y, 0xFFFFFFFEu); v.z = min(v.z, 0xFFFFFFFEu); v.w = min(v.w, 0xFFFFFFFEu);
      st_sc1(v, rm, phys(rp * MEM_SLOT + (c >> 3) * 1024 + ((c & 3) * 16 + ((c >> 2) & 1) * 8 + r) * 16, pol));
    }
    if (c == 0) {
      unsigned now = rt() & 0x7fffffffu;
      if (lane < NREP) st_sc1((v4u){now, now, 0u, 0u}, rm, phys(lane * MEM_SLOT + 9216, pol));
    }
    wait_ge(p.ctrl + C_WVO, 64u, p.ctrl + C_TMO, 6u);
  }
  __syncthreads();
  v8u w[4][9];
#pragma unroll
  for (int tl = 0; tl < 4; ++tl)
#pragma unroll
    for (int t = 0; t < 9; ++t) {
      unsigned off = ((c * 16 + wave * 4 + tl) * 9 + t) * 2048 + lane * 32;
      v4u x0 = ld_sc1(rwv, off), x1 = ld_sc1(rwv, off + 16);
      v8u ww;
      ww[0] = x0.x; ww[2] = x0.y; ww[1] = x0.z; ww[3] = x0.w;
      ww[4] = x1.x; ww[6] = x1.y; ww[5] = x1.z; ww[7] = x1.w;
      w[tl][t] = ww;
    }
  const int g = lane >> 4, half = (lane >> 3) & 1, n7 = lane & 7;
  const int uw_off = L_UIMG + (((2 * (4 * wave + (lane >> 5)) + ((lane & 15) >> 3)) * 8 + (lane & 7)) * 8 + 4 * ((lane >> 4) & 1)) * 2;
  const int red_w = ((g >> 1) == half) ? L_RED + (((wave * 2 + half) * 8 + n7) * 8 + 4 * (g & 1)) * 4 : L_RED + 2048 + lane * 16;
  const int red_r = L_RED + (((2 * g) * 8 + ((lane >> 1) & 7)) * 8 + 4 * (lane & 1)) * 4;
  unsigned tM = rt();
  int schedA = SCHED_A0, schedB = SCHED_B0, streak = 0;
  for (int s = 0; s < NSEG; ++s) {
    MemAddr ma = mem_addr(s, rep, pol, wave, lane);
    f32x4 acc[4]; zero_acc(acc);
    wait_until(tM);
    unsigned mfail = gather_mem(p, rm, smem, wave, lane, ma);
    __syncthreads();
    if (s > 0 && tid == 0) aadd(p.ctrl + C_UM + (s - 1) * 32, 1u);
    const unsigned tP = __builtin_amdgcn_readfirstlane(*(const LAS unsigned*)(smem + L_MEM + 9216));
    tM = __builtin_amdgcn_readfirstlane(*(const LAS unsigned*)(smem + L_MEM + 9216 + 4));
    v4u ux[4];
    if (s < NSEG - 1) {
      if (!wait_ge(p.ctrl + C_UX + s * 32, 32u, p.ctrl + C_TMO, 7u)) *(LAS unsigned*)(smem + L_DEAD) = 1;
#pragma unroll
      for (int q = 0; q < 4; ++q) ux[q] = ld_sc1(rux, (s * 72 + c) * 16384 + (wave * 4 + q) * 1024 + lane * 16);
    }
    proj36(smem, w, lane, acc);
    v2u pk0 = pairsum(acc[0], acc[1]), pk1 = pairsum(acc[2], acc[3]);
    *(v2u*)(smem + uw_off) = pk0;
    *(v2u*)(smem + uw_off + 512) = pk1;
    v4u ub = *(const v4u*)(smem + L_UIMG + ((8 * wave + 4 * half + g) * 8 + n7) * 16);
    if (s < NSEG - 1) {
      const unsigned base = s * STEP_SLOT + NREP * MEM_SLOT + rep * P_SLOT;
      unsigned o[5];
#pragma unroll
      for (int q = 0; q < 4; ++q) o[q] = phys(base + (wave * 4 + q) * 1024 + lane * 16, pol);
      o[4] = phys(base + 16384 + wave * 1024 + lane * 16, pol);
      unsigned ado = phys((s + 1) * STEP_SLOT + (lane >> 4) * MEM_SLOT + (c >> 3) * 1024 + ((c & 3) * 16 + ((c >> 2) & 1) * 8 + ((lane >> 1) & 7)) * 16, pol);
      if (lane & 1) ado = 0x7ffffff0u;
      asm volatile("" : "+v"(ado), "+v"(o[0]), "+v"(o[1]), "+v"(o[2]), "+v"(o[3]), "+v"(o[4]));
      asm volatile("s_waitcnt vmcnt(0)" : "+v"(ux[0]), "+v"(ux[1]), "+v"(ux[2]), "+v"(ux[3]), "+v"(ub) :: "memory");
      wait_until(tP);
      f32x4 d = {0, 0, 0, 0};
      unsigned pfail = 0;
      {
        v4u v[5];
#pragma unroll
        for (int q = 0; q < 5; ++q) v[q] = ld_sc1(rm, o[q]);
        unsigned mx = 0;
#pragma unroll
        for (int q = 0; q < 4; ++q) { d = mfma16(ux[q], v[q], d); mx = max(mx, max4(v[q])); }
        d = mfma16(ub, v[4], d); mx = max(mx, max4(v[4]));
        if (__builtin_expect(__builtin_amdgcn_ballot_w64(mx == SENT) != 0, 0)) {
          for (;;) {
            asm volatile("" ::: "memory");
#pragma unroll
            for (int q = 0; q < 5; ++q) v[q] = ld_sc1(rm, o[q]);
            mx = 0;
#pragma unroll
            for (int q = 0; q < 5; ++q) mx = max(mx, max4(v[q]));
            ++pfail;
            if (__builtin_amdgcn_ballot_w64(mx == SENT) == 0) break;
            if (pfail > SPIN_CAP) { astore(p.ctrl + C_TMO, 8u); *(LAS unsigned*)(smem + L_DEAD) = 1; break; }
            if ((pfail & 1023) == 0 && __builtin_amdgcn_readfirstlane(aload(p.ctrl + C_TMO))) { *(LAS unsigned*)(smem + L_DEAD) = 1; break; }
          }
          d = (f32x4){0, 0, 0, 0};
#pragma unroll
          for (int q = 0; q < 4; ++q) d = mfma16(ux[q], v[q], d);
          d = mfma16(ub, v[4], d);
        }
      }
      *(f32x4*)(smem + red_w) = d;
      if (c == 0 && lane == 0) *(LAS unsigned*)(smem + L_DEAD + 16 + wave * 4) = (mfail ? 1u : 0u) | (pfail ? 2u : 0u);
      __syncthreads();
      if (wave == 0) {
        f32x4 x0 = *(const f32x4*)(smem + red_r), x1 = *(const f32x4*)(smem + red_r + 256);
        f32x4 x = x0 + x1;
        float r0 = xsum(x[0]), r1 = xsum(x[1]), r2 = xsum(x[2]), r3 = xsum(x[3]);
        v2u o2 = {min(cvtpk(r0, r1), 0xFFFFFFFEu), min(cvtpk(r2, r3), 0xFFFFFFFEu)};
        v4u o4 = {o2.x, o2.y, (unsigned)__builtin_amdgcn_mov_dpp(o2.x, 0xB1, 0xf, 0xf, true), (unsigned)__builtin_amdgcn_mov_dpp(o2.y, 0xB1, 0xf, 0xf, true)};
        st_sc1(o4, rm, ado);
      } else if (wave == 1 && c == 0) {
        v4u f = *(const v4u*)(smem + L_DEAD + 16);
        unsigned fl = __builtin_amdgcn_readfirstlane(f.x | f.y | f.z | f.w);
        unsigned now = rt();
        unsigned baseT = tM;
        if ((int)(now + 8u - tM) > 0) baseT = now + 30u;
        if (s >= 2) {
          if (fl & 1u) { schedB += 2; streak = 0; }
          else if (fl & 2u) { schedA += 2; streak = 0; }
          else if (++streak >= 6) { streak = 0; if (s & 1) schedA -= 1; else schedB -= 1; }
        }
        unsigned nP = (baseT + (unsigned)schedA) , nM = nP + (unsigned)schedB;
        if (nP == SENT) nP = 0; if (nM == SENT) nM = 0;
        if (lane < NREP) st_sc1((v4u){nP, nM, 0u, 0u}, rm, phys((s + 1) * STEP_SLOT + lane * MEM_SLOT + 9216, pol));
      }
    }
    st_sc1(ub, rum, ((s * 72 + c) * 4 + wave) * 1024 + lane * 16);
    if (*(volatile LAS unsigned*)(smem + L_DEAD)) break;
  }
  wg_signal(p.ctrl + C_UM + (NSEG - 1) * 32, tid);
}

DEV void spare_role(const Params& p, char* smem, int j, int wave, int lane, int tid) {
  __amdgpu_buffer_rsrc_t rm = mkrsrc(p.mbox, MBOX_BYTES);
  if (j < NPAGES) {
    if (wave < 2) {
      unsigned acc = 0, mn = 1u << 30;
      unsigned off = j * (2u << 20) + wave * 8192 + (1u << 20);
      acc += ld1_sc1(rm, off + 128);
      drain();
      asm volatile("" :: "v"(acc));
      for (int q = 0; q < 6; ++q) {
        unsigned long long t0 = clk();
        acc += ld1_sc1(rm, off + 512 + q * 256);
        drain();
        asm volatile("" :: "v"(acc));
        unsigned d = (unsigned)(clk() - t0); mn = min(mn, d);
      }
      if (lane == 0) *(LAS unsigned*)(smem + wave * 4) = mn;
    }
    __syncthreads();
    if (tid == 0) {
      unsigned t0 = *(LAS unsigned*)(smem), t1 = *(LAS unsigned*)(smem + 4);
      astore(p.ctrl + C_POL + j, 1u + (t1 < t0 ? 1u : 0u));
    }
  }
  if (wave == 0) {
    for (int i = 0; i < NPAGES; ++i) wait_ge(p.ctrl + C_POL + i, 1u, p.ctrl + C_TMO, 9u);
  }
  __syncthreads();
  const unsigned pol = read_pol(p);
  {
    const v4u sv = {SENT, SENT, SENT, SENT};
    constexpr unsigned TOT = (unsigned)(NSEG + 1) * STEP_SLOT / 16;
    for (unsigned i = j * 256 + tid; i < TOT; i += NSPARE * 256) st_sc1(sv, rm, phys(i * 16, pol));
  }
  wg_signal(p.ctrl + C_CLR, tid);
  {
    float* gate = (float*)(p.data + OFF_GATE);
    __amdgpu_buffer_rsrc_t rg = mkrsrc(gate, SZ_GATE);
    for (int tok = j * 4 + wave; tok < 2048; tok += NSPARE * 4) {
      float a = 0;
      for (int k = lane; k < 72; k += 64) {
        v4u xv = *(const v4u*)(p.x + (size_t)tok * 576 + k * 8), gv = *(const v4u*)(p.gw + k * 8);
#pragma unroll
        for (int e = 0; e < 4; ++e) {
          a += bf2f(xv[e] & 0xffffu) * bf2f(gv[e] & 0xffffu);
          a += bf2f(xv[e] >> 16) * bf2f(gv[e] >> 16);
        }
      }
#pragma unroll
      for (int o = 1; o < 64; o <<= 1) a += __shfl_xor(a, o);
      float gsig = 1.f / (1.f + __expf(-a));
      if (lane == 0) st1_sc1(__float_as_uint(gsig), rg, tok * 4);
    }
    __amdgpu_buffer_rsrc_t rw = mkrsrc(p.data + OFF_WOT, SZ_WOT);
    for (int f = j * 4 + wave; f < 36 * 18; f += NSPARE * 4) {
      int nt = f / 18, ks = f % 18;
      const u16* src = p.wo + (size_t)(32 * ks + 8 * (lane >> 4)) * 576 + 16 * nt + (lane & 15);
      unsigned e[8];
#pragma unroll
      for (int jj = 0; jj < 8; ++jj) e[jj] = src[jj * 576];
      st_sc1((v4u){e[0] | (e[1] << 16), e[2] | (e[3] << 16), e[4] | (e[5] << 16), e[6] | (e[7] << 16)}, rw, f * 1024 + lane * 16);
    }
  }
  wg_signal(p.ctrl + C_AUX, tid);
}

struct XStage { v4u v[9]; };
DEV void x_load(const u16* x, int s, int tid, XStage& st) {
#pragma unroll
  for (int i = 0; i < 9; ++i) st.v[i] = *(const v4u*)(x + (size_t)s * 32 * 576 + (size_t)(tid + 256 * i) * 8);
}
DEV void x_store(char* smem, int buf, int tid, const XStage& st) {
#pragma unroll
  for (int i = 0; i < 9; ++i) { int ch = tid + 256 * i; int row = ch / 72, cc = ch % 72; *(v4u*)(smem + L_X + buf * XBUF + row * XROW + cc * 16) = st.v[i]; }
}
template <int NTW, class F>
DEV void load_kfrags(char* smem, int wave, int lane, int tid, F srcf, v4u (&w)[NTW][18]) {
  constexpr int CH = NTW * 8;
  constexpr int RS = CH * 16 + 16;
  constexpr int PER = 64 * CH / 256;
  const int g = lane >> 4, q4 = (lane & 15) >> 2, p4 = lane & 3;
  v4u st[PER];
  auto ld = [&](int t) {
#pragma unroll
    for (int i = 0; i < PER; ++i) { int ch = tid + 256 * i; st[i] = srcf(64 * t + ch / CH, ch % CH); }
  };
  ld(0);
#pragma unroll
  for (int t = 0; t < 9; ++t) {
    __syncthreads();
#pragma unroll
    for (int i = 0; i < PER; ++i) { int ch = tid + 256 * i; *(v4u*)(smem + L_WST + (ch / CH) * RS + (ch % CH) * 16) = st[i]; }
    if (t < 8) ld(t + 1);
    __syncthreads();
#pragma unroll
    for (int tl = 0; tl < NTW; ++tl)
#pragma unroll
      for (int kk = 0; kk < 2; ++kk) {
        int a0 = L_WST + (32 * kk + 8 * g + q4) * RS + ((wave * NTW + tl) * 16 + 4 * p4) * 2;
        v2u x0 = tr_read(smem, a0), x1 = tr_read(smem, a0 + 4 * RS);
        w[tl][2 * t + kk] = (v4u){x0.x, x0.y, x1.x, x1.y};
      }
  }
  __syncthreads();
}

DEV void kq_worker(const Params& p, char* smem, int h, int par, int wave, int lane, int tid) {
  v4u w[4][18];
  XStage xs;
  x_load(p.x, par, tid, xs);
  load_kfrags<4>(smem, wave, lane, tid, [&](int row, int cc) {
    return *(const v4u*)((cc < 16 ? p.Wk : p.Wq) + (size_t)row * 4096 + h * 128 + (cc & 15) * 8); }, w);
  __amdgpu_buffer_rsrc_t ro = mkrsrc(p.data + (wave < 2 ? OFF_KX : OFF_QX), SZ_KX);
  const int xoff = (lane & 15) * XROW + (lane >> 4) * 16;
  int buf = 0;
  x_store(smem, 0, tid, xs);
  __syncthreads();
  for (int s = par; s < NSEG; s += 2) {
    if (s + 2 < NSEG) x_load(p.x, s + 2, tid, xs);
    f32x4 acc[2][4];
#pragma unroll
    for (int a = 0; a < 2; ++a)
#pragma unroll
      for (int b = 0; b < 4; ++b) acc[a][b] = (f32x4){0, 0, 0, 0};
#pragma unroll
    for (int ks = 0; ks < 18; ++ks) {
      v4u b0 = *(const v4u*)(smem + L_X + buf * XBUF + xoff + ks * 64);
      v4u b1 = *(const v4u*)(smem + L_X + buf * XBUF + 16 * XROW + xoff + ks * 64);
#pragma unroll
      for (int tl = 0; tl < 4; ++tl) { acc[0][tl] = mfma16(w[tl][ks], b0, acc[0][tl]); acc[1][tl] = mfma16(w[tl][ks], b1, acc[1][tl]); }
    }
#pragma unroll
    for (int tt = 0; tt < 2; ++tt)
#pragma unroll
      for (int uu = 0; uu < 2; ++uu) {
        f32x4 a = acc[tt][2 * uu], b = acc[tt][2 * uu + 1];
        v4u o = {cvtpk(a[0], a[1]), cvtpk(a[2], a[3]), cvtpk(b[0], b[1]), cvtpk(b[2], b[3])};
        st_sc1(o, ro, (s * 32 + h) * 8192 + (tt * 4 + (wave & 1) * 2 + uu) * 1024 + lane * 16);
      }
    if (s + 2 < NSEG) x_store(smem, buf ^ 1, tid, xs);
    drain();
    __syncthreads();
    if (tid == 0) astore(p.ctrl + C_KQ + s * 32 + h, 1u);
    buf ^= 1;
  }
}

DEV void vu_worker(const Params& p, char* smem, int h, int par, int wave, int lane, int tid) {
  const int g = lane >> 4, q4 = (lane & 15) >> 2, p4 = lane & 3;
  v4u wo[9][4];
  {
    constexpr int RS = 1152 + 16;
#pragma unroll
    for (int hf = 0; hf < 2; ++hf) {
      v4u st[18];
#pragma unroll
      for (int i = 0; i < 18; ++i) { int ch = tid + 256 * i; st[i] = *(const v4u*)(p.Wo + ((size_t)h * 128 + 64 * hf) * 576 + (size_t)ch * 8); }
      __syncthreads();
#pragma unroll
      for (int i = 0; i < 18; ++i) { int ch = tid + 256 * i; *(v4u*)(smem + L_WST + (ch / 72) * RS + (ch % 72) * 16) = st[i]; }
      __syncthreads();
#pragma unroll
      for (int tl = 0; tl < 9; ++tl)
#pragma unroll
        for (int uu = 0; uu < 2; ++uu) {
          int a0 = L_WST + (32 * uu + 4 * g + q4) * RS + ((wave * 9 + tl) * 16 + 4 * p4) * 2;
          v2u x0 = tr_read(smem, a0), x1 = tr_read(smem, a0 + 16 * RS);
          wo[tl][2 * hf + uu] = (v4u){x0.x, x0.y, x1.x, x1.y};
        }
    }
    __syncthreads();
  }
  {
    __amdgpu_buffer_rsrc_t rw = mkrsrc(p.data + OFF_WVO, SZ_WVO);
    for (int rt_ = par; rt_ < 18; rt_ += 2) {
      v4u a[2][4];
#pragma unroll
      for (int tt = 0; tt < 2; ++tt)
#pragma unroll
        for (int u = 0; u < 4; ++u) {
          const u16* src = p.Wv + (size_t)(32 * rt_ + 16 * tt + (lane & 15)) * 4096 + h * 128 + 32 * u + 4 * g;
          v2u x0 = *(const v2u*)src, x1 = *(const v2u*)(src + 16);
          a[tt][u] = (v4u){x0.x, x0.y, x1.x, x1.y};
        }
#pragma unroll
      for (int tl = 0; tl < 9; ++tl) {
        f32x4 d0 = {0, 0, 0, 0}, d1 = {0, 0, 0, 0};
#pragma unroll
        for (int u = 0; u < 4; ++u) { d0 = mfma16(a[0][u], wo[tl][u], d0); d1 = mfma16(a[1][u], wo[tl][u], d1); }
        const int n = (wave * 9 + tl) * 16 + (lane & 15), c = n >> 3;
#pragma unroll
        for (int tt = 0; tt < 2; ++tt) {
          const int k = 32 * rt_ + 16 * tt + 4 * g;
          const int t = k >> 6, i16 = (k >> 2) & 15, gB = i16 & 3, X = (i16 >> 2) & 1, hf = i16 >> 3;
          unsigned off = ((c * 16 + (h >> 1)) * 9 + t) * 2048 + (gB * 16 + (h & 1) * 8 + (n & 7)) * 32 + X * 16 + hf * 8;
          f32x4 d = tt ? d1 : d0;
          st2_sc1((v2u){cvtpk(d[0], d[1]), cvtpk(d[2], d[3])}, rw, off);
        }
      }
    }
    wg_signal(p.ctrl + C_WVO, tid);
  }
  v4u wv[2][18];
  XStage xs;
  x_load(p.x, par, tid, xs);
  load_kfrags<2>(smem, wave, lane, tid, [&](int row, int cc) {
    return *(const v4u*)(p.Wv + (size_t)row * 4096 + h * 128 + cc * 8); }, wv);
  __amdgpu_buffer_rsrc_t ro = mkrsrc(p.data + OFF_UX, SZ_UX);
  const int xoff = (lane & 15) * XROW + g * 16;
  int buf = 0;
  x_store(smem, 0, tid, xs);
  __syncthreads();
  for (int s = par; s < NSEG; s += 2) {
    if (s + 2 < NSEG) x_load(p.x, s + 2, tid, xs);
    f32x4 acc[2][2];
#pragma unroll
    for (int a = 0; a < 2; ++a)
#pragma unroll
      for (int b = 0; b < 2; ++b) acc[a][b] = (f32x4){0, 0, 0, 0};
#pragma unroll
    for (int ks = 0; ks < 18; ++ks) {
      v4u b0 = *(const v4u*)(smem + L_X + buf * XBUF + xoff + ks * 64);
      v4u b1 = *(const v4u*)(smem + L_X + buf * XBUF + 16 * XROW + xoff + ks * 64);
#pragma unroll
      for (int tl = 0; tl < 2; ++tl) { acc[0][tl] = mfma16(wv[tl][ks], b0, acc[0][tl]); acc[1][tl] = mfma16(wv[tl][ks], b1, acc[1][tl]); }
    }
#pragma unroll
    for (int tt = 0; tt < 2; ++tt) {
      f32x4 a = acc[tt][0], b = acc[tt][1];
      *(v4u*)(smem + L_V + (tt * 4 + wave) * 1024 + lane * 16) = (v4u){cvtpk(a[0], a[1]), cvtpk(a[2], a[3]), cvtpk(b[0], b[1]), cvtpk(b[2], b[3])};
    }
    __syncthreads();
    v4u a[2][4];
#pragma unroll
    for (int tt = 0; tt < 2; ++tt)
#pragma unroll
      for (int u = 0; u < 4; ++u) a[tt][u] = *(const v4u*)(smem + L_V + (tt * 4 + u) * 1024 + lane * 16);
#pragma unroll
    for (int tl = 0; tl < 9; ++tl) {
      f32x4 d0 = {0, 0, 0, 0}, d1 = {0, 0, 0, 0};
#pragma unroll
      for (int u = 0; u < 4; ++u) { d0 = mfma16(a[0][u], wo[tl][u], d0); d1 = mfma16(a[1][u], wo[tl][u], d1); }
      const int n = (wave * 9 + tl) * 16 + (lane & 15), c = n >> 3;
      v4u o = {cvtpk(d0[0], d0[1]), cvtpk(d0[2], d0[3]), cvtpk(d1[0], d1[1]), cvtpk(d1[2], d1[3])};
      st_sc1(o, ro, ((s * 72 + c) * 16 + (h >> 1)) * 1024 + (g * 16 + (h & 1) * 8 + (n & 7)) * 16);
    }
    if (s + 2 < NSEG) x_store(smem, buf ^ 1, tid, xs);
    drain();
    __syncthreads();
    if (tid == 0) aadd(p.ctrl + C_UX + s * 32, 1u);
    buf ^= 1;
  }
}

DEV void b1_worker(const Params& p, char* smem, int widx, int wave, int lane, int tid) {
  if (widx < 126) {
    const int slab = widx / 18, sub = widx % 18;
    v4u w[4][18];
    XStage xs;
    x_load(p.x, sub, tid, xs);
    load_kfrags<4>(smem, wave, lane, tid, [&](int row, int cc) {
      int col = slab * 256 + cc * 8; if (col > 1720) col = 1720;
      return *(const v4u*)(p.wqkv + (size_t)row * 1728 + col); }, w);
    __amdgpu_buffer_rsrc_t rqk = mkrsrc(p.data + OFF_QKB, (unsigned)(2048 * 1152 * 2));
    __amdgpu_buffer_rsrc_t rvt = mkrsrc(p.data + OFF_VTB, (unsigned)(576 * 2048 * 2));
    const int g = lane >> 4;
    const int xoff = (lane & 15) * XROW + g * 16;
    const int col0 = slab * 256 + wave * 64;
    const bool isv = col0 >= 1152, valid = col0 < 1728;
    int buf = 0;
    x_store(smem, 0, tid, xs);
    __syncthreads();
    for (int s = sub; s < NSEG; s += 18) {
      if (s + 18 < NSEG) x_load(p.x, s + 18, tid, xs);
      if (valid) {
        f32x4 acc[2][4];
#pragma unroll
        for (int a = 0; a < 2; ++a)
#pragma unroll
          for (int b = 0; b < 4; ++b) acc[a][b] = (f32x4){0, 0, 0, 0};
        if (!isv) {
#pragma unroll
          for (int ks = 0; ks < 18; ++ks) {
            v4u b0 = *(const v4u*)(smem + L_X + buf * XBUF + xoff + ks * 64);
            v4u b1 = *(const v4u*)(smem + L_X + buf * XBUF + 16 * XROW + xoff + ks * 64);
#pragma unroll
            for (int tl = 0; tl < 4; ++tl) { acc[0][tl] = mfma16(w[tl][ks], b0, acc[0][tl]); acc[1][tl] = mfma16(w[tl][ks], b1, acc[1][tl]); }
          }
#pragma unroll
          for (int tt = 0; tt < 2; ++tt)
#pragma unroll
            for (int tl = 0; tl < 4; ++tl) {
              f32x4 a = acc[tt][tl];
              st2_sc1((v2u){cvtpk(a[0], a[1]), cvtpk(a[2], a[3])}, rqk, ((s * 32 + tt * 16 + (lane & 15)) * 1152 + col0 + tl * 16 + 4 * g) * 2);
            }
        } else {
#pragma unroll
          for (int ks = 0; ks < 18; ++ks) {
            v4u b0 = *(const v4u*)(smem + L_X + buf * XBUF + xoff + ks * 64);
            v4u b1 = *(const v4u*)(smem + L_X + buf * XBUF + 16 * XROW + xoff + ks * 64);
#pragma unroll
            for (int tl = 0; tl < 4; ++tl) { acc[0][tl] = mfma16(b0, w[tl][ks], acc[0][tl]); acc[1][tl] = mfma16(b1, w[tl][ks], acc[1][tl]); }
          }
#pragma unroll
          for (int tt = 0; tt < 2; ++tt)
#pragma unroll
            for (int tl = 0; tl < 4; ++tl) {
              f32x4 a = acc[tt][tl];
              st2_sc1((v2u){cvtpk(a[0], a[1]), cvtpk(a[2], a[3])}, rvt, ((col0 - 1152 + tl * 16 + (lane & 15)) * 2048 + s * 32 + tt * 16 + 4 * g) * 2);
            }
        }
      }
      if (s + 18 < NSEG) x_store(smem, buf ^ 1, tid, xs);
      __syncthreads();
      buf ^= 1;
    }
  }
  wg_signal(p.ctrl + C_B1, tid);
}

DEV void b2_item(const Params& p, int hb, int qb, int lane) {
  __amdgpu_buffer_rsrc_t rqk = mkrsrc(p.data + OFF_QKB, (unsigned)(2048 * 1152 * 2));
  __amdgpu_buffer_rsrc_t rvt = mkrsrc(p.data + OFF_VTB, (unsigned)(576 * 2048 * 2));
  __amdgpu_buffer_rsrc_t rob = mkrsrc(p.data + OFF_OB, (unsigned)SZ_OB);
  const int g = lane >> 4, li = lane & 15;
  const int q0 = qb * 64, k0 = q0 - 256;
  const float SC = 0.23570226039551584f * 1.4426950408889634f;
  const float NEG = -1e30f;
  auto rowfrag = [&](int tok, int colbase) {
    v4u v = {0u, 0u, 0u, 0u};
    if (tok >= 0) {
      unsigned off = (tok * 1152 + colbase + 8 * g) * 2;
      if (g < 2) v = ld_sc1(rqk, off);
      else if (g == 2) v.x = ld1_sc1(rqk, off);
    }
    return v;
  };
  v4u kf[20], vf[10][2];
#pragma unroll
  for (int t = 0; t < 20; ++t) kf[t] = rowfrag(k0 + 16 * t + li, 576 + 18 * hb);
#pragma unroll
  for (int b = 0; b < 10; ++b)
#pragma unroll
    for (int dt = 0; dt < 2; ++dt) {
      int kb = k0 + 32 * b;
      v4u v = {0u, 0u, 0u, 0u};
      int d = 16 * dt + li;
      if (kb >= 0 && d < 18) {
        unsigned off = ((18 * hb + d) * 2048 + kb + 4 * g) * 2;
        v2u x0 = ld2_sc1(rvt, off), x1 = ld2_sc1(rvt, off + 32);
        v = (v4u){x0.x, x0.y, x1.x, x1.y};
      }
      vf[b][dt] = v;
    }
#pragma unroll
  for (int qt = 0; qt < 4; ++qt) {
    const int q = q0 + 16 * qt + li;
    v4u qf = rowfrag(q, 18 * hb);
    const int b0 = qt >> 1;
    float sc[18][4];
    float m = NEG;
#pragma unroll
    for (int t = 0; t < 18; ++t) {
      f32x4 s4 = mfma16(kf[2 * b0 + t], qf, (f32x4){0, 0, 0, 0});
#pragma unroll
      for (int r = 0; r < 4; ++r) {
        int key = k0 + 16 * (2 * b0 + t) + 4 * g + r;
        int dlt = q - key;
        float v = (dlt >= 0 && dlt < 256 && key >= 0) ? s4[r] * SC : NEG;
        sc[t][r] = v; m = fmaxf(m, v);
      }
    }
    m = xmax(m);
    float l = 0;
#pragma unroll
    for (int t = 0; t < 18; ++t)
#pragma unroll
      for (int r = 0; r < 4; ++r) { float e = __builtin_amdgcn_exp2f(sc[t][r] - m); sc[t][r] = e; l += e; }
    l = xsum(l);
    const float inv = __builtin_amdgcn_rcpf(l);
    f32x4 o0 = {0, 0, 0, 0}, o1 = {0, 0, 0, 0};
#pragma unroll
    for (int b = 0; b < 9; ++b) {
      v4u pf = {cvtpk(sc[2 * b][0] * inv, sc[2 * b][1] * inv), cvtpk(sc[2 * b][2] * inv, sc[2 * b][3] * inv),
                cvtpk(sc[2 * b + 1][0] * inv, sc[2 * b + 1][1] * inv), cvtpk(sc[2 * b + 1][2] * inv, sc[2 * b + 1][3] * inv)};
      o0 = mfma16(vf[b0 + b][0], pf, o0);
      o1 = mfma16(vf[b0 + b][1], pf, o1);
    }
    unsigned off = (q * 576 + 18 * hb + 4 * g) * 2;
    st1_sc1(cvtpk(o0[0], o0[1]), rob, off);
    st1_sc1(cvtpk(o0[2], o0[3]), rob, off + 4);
    if (g == 0) st1_sc1(cvtpk(o1[0], o1[1]), rob, (q * 576 + 18 * hb + 16) * 2);
  }
  drain();
  if (lane == 0) aadd(p.ctrl + C_OB + qb * 32, 1u);
}

DEV void final_item(const Params& p, char* smem, int s, int j, int wave, int lane, int tid) {
  __amdgpu_buffer_rsrc_t rpx = mkrsrc(p.data + OFF_PX, SZ_PX);
  __amdgpu_buffer_rsrc_t rux = mkrsrc(p.data + OFF_UX, SZ_UX);
  __amdgpu_buffer_rsrc_t rum = mkrsrc(p.data + OFF_UM, SZ_UM);
  __amdgpu_buffer_rsrc_t rob = mkrsrc(p.data + OFF_OB, (unsigned)SZ_OB);
  __amdgpu_buffer_rsrc_t rwt = mkrsrc(p.data + OFF_WOT, SZ_WOT);
  __amdgpu_buffer_rsrc_t rg = mkrsrc(p.data + OFF_GATE, SZ_GATE);
  const int g = lane >> 4, li = lane & 15;
  const int nt = j * 4 + wave;
  if (wave == 0) wait_ge(p.ctrl + C_OB + (s >> 1) * 32, 32u, p.ctrl + C_TMO, 10u);
  __syncthreads();
  f32x4 bacc[2] = {{0, 0, 0, 0}, {0, 0, 0, 0}};
#pragma unroll 6
  for (int ks = 0; ks < 18; ++ks) {
    v4u wf = ld_sc1(rwt, (nt * 18 + ks) * 1024 + lane * 16);
    v4u o0 = ld_sc1(rob, ((s * 32 + li) * 576 + 32 * ks + 8 * g) * 2);
    v4u o1 = ld_sc1(rob, ((s * 32 + 16 + li) * 576 + 32 * ks + 8 * g) * 2);
    bacc[0] = mfma16(wf, o0, bacc[0]); bacc[1] = mfma16(wf, o1, bacc[1]);
  }
  v4u ux[2][16];
#pragma unroll
  for (int sl = 0; sl < 2; ++sl)
#pragma unroll
    for (int pp = 0; pp < 16; ++pp) ux[sl][pp] = ld_sc1(rux, ((s * 72 + nt * 2 + sl) * 16 + pp) * 1024 + lane * 16);
  if (wave == 0) { wait_ge(p.ctrl + C_PX + s * 32, 64u, p.ctrl + C_TMO, 11u); wait_ge(p.ctrl + C_UM + s * 32, NC, p.ctrl + C_TMO, 12u); }
  __syncthreads();
  {
    v4u v[20];
#pragma unroll
    for (int i = 0; i < 20; ++i) v[i] = ld_sc1(rpx, s * 81920 + (i * 256 + tid) * 16);
#pragma unroll
    for (int i = 0; i < 20; ++i) *(v4u*)(smem + (i * 256 + tid) * 16) = v[i];
  }
  v4u um[2][4];
#pragma unroll
  for (int sl = 0; sl < 2; ++sl)
#pragma unroll
    for (int mm = 0; mm < 4; ++mm) um[sl][mm] = ld_sc1(rum, ((s * 72 + nt * 2 + sl) * 4 + mm) * 1024 + lane * 16);
  __syncthreads();
#pragma unroll
  for (int qt = 0; qt < 2; ++qt) {
    f32x4 aA[2] = {{0, 0, 0, 0}, {0, 0, 0, 0}}, aB[2] = {{0, 0, 0, 0}, {0, 0, 0, 0}};
#pragma unroll
    for (int pp = 0; pp < 16; ++pp) {
      v4u pe = *(const v4u*)(smem + (qt * 32 + 2 * pp) * 1024 + lane * 16);
      v4u po = *(const v4u*)(smem + (qt * 32 + 2 * pp + 1) * 1024 + lane * 16);
#pragma unroll
      for (int sl = 0; sl < 2; ++sl) { aA[sl] = mfma16(ux[sl][pp], pe, aA[sl]); aB[sl] = mfma16(ux[sl][pp], po, aB[sl]); }
    }
#pragma unroll
    for (int mm = 0; mm < 4; ++mm) {
      v4u pe = *(const v4u*)(smem + 65536 + (qt * 8 + 2 * mm) * 1024 + lane * 16);
      v4u po = *(const v4u*)(smem + 65536 + (qt * 8 + 2 * mm + 1) * 1024 + lane * 16);
#pragma unroll
      for (int sl = 0; sl < 2; ++sl) { aA[sl] = mfma16(um[sl][mm], pe, aA[sl]); aB[sl] = mfma16(um[sl][mm], po, aB[sl]); }
    }
    const int tok = s * 32 + qt * 16 + li;
    const float gt = __uint_as_float(ld1_sc1(rg, tok * 4));
    float o[4];
#pragma unroll
    for (int r = 0; r < 4; ++r) {
      auto sw = __builtin_amdgcn_permlane32_swap(__float_as_uint(aB[0][r]), __float_as_uint(aA[1][r]), false, false);
      float own = (lane < 32) ? aA[0][r] : aB[1][r];
      float oth = (lane < 32) ? __uint_as_float(sw[1]) : __uint_as_float(sw[0]);
      float a = own + oth;
      o[r] = gt * a + (1.f - gt) * bacc[qt][r];
    }
    *(v2u*)(p.out + (size_t)tok * 576 + nt * 16 + 4 * g) = (v2u){cvtpk(o[0], o[1]), cvtpk(o[2], o[3])};
  }
  __syncthreads();
}

DEV void worker_role(const Params& p, char* smem, int widx, int wave, int lane, int tid) {
  const int slab = widx >> 1, par = widx & 1;
  if (slab < 32) vu_worker(p, smem, slab, par, wave, lane, tid);
  else kq_worker(p, smem, slab - 32, par, wave, lane, tid);
  __syncthreads();
  b1_worker(p, smem, widx, wave, lane, tid);
  wg_wait_ge(p.ctrl + C_B1, NWORK, p.ctrl + C_TMO, 13u, wave);
  for (int it = 0; it < 2; ++it) {
    int item = (it * NWORK + widx) * 4 + wave;
    b2_item(p, item & 31, item >> 5, lane);
  }
  wg_wait_ge(p.ctrl + C_AUX, NSPARE, p.ctrl + C_TMO, 14u, wave);
  for (int item = widx; item < NSEG * 9; item += NWORK) final_item(p, smem, item / 9, item % 9, wave, lane, tid);
}

__global__ void __launch_bounds__(256, 1) fused_kernel(Params p) {
  extern __shared__ __attribute__((aligned(16))) char smem[];
  const int tid = threadIdx.x, lane = tid & 63, wave = __builtin_amdgcn_readfirstlane(tid >> 6);
  const int b = blockIdx.x;
  const int idx = (b >> 3) * 4 + (b & 3);
  if ((b & 7) < 4) {
    if (idx < NH) head_role(p, smem, idx, wave, lane, tid);
    else if (idx < NH + NC) col_role(p, smem, idx - NH, wave, lane, tid);
    else spare_role(p, smem, idx - NH - NC, wave, lane, tid);
  } else {
    worker_role(p, smem, idx, wave, lane, tid);
  }
}

}

extern "C" void kernel_launch(void* const* d_in, const int* in_sizes, int n_in, void* d_out, int out_size, void* d_ws,
                              size_t ws_size, hipStream_t stream) {
  (void)in_sizes; (void)n_in; (void)out_size; (void)ws_size;
  Params p;
  p.x = (const u16*)d_in[0]; p.mem0 = (const u16*)d_in[1];
  p.Wq = (const u16*)d_in[2]; p.Wk = (const u16*)d_in[3]; p.Wv = (const u16*)d_in[4]; p.Wo = (const u16*)d_in[5];
  p.wqkv = (const u16*)d_in[8]; p.wo = (const u16*)d_in[9]; p.gw = (const u16*)d_in[10];
  p.out = (u16*)d_out;
  char* ws = (char*)d_ws;
  p.ctrl = (unsigned*)ws;
  size_t a = ((size_t)ws + CTRL_BYTES + (2u << 20) - 1) & ~(size_t)((2u << 20) - 1);
  p.mbox = (char*)a;
  p.data = p.mbox + MBOX_BYTES;
  hipFuncSetAttribute((const void*)fused_kernel, hipFuncAttributeMaxDynamicSharedMemorySize, LDS_BYTES);
  hipMemsetAsync(ws, 0, CTRL_BYTES, stream);
  fused_kernel<<<256, 256, LDS_BYTES, stream>>>(p);
}
```

```cpp
#include <hip/hip_runtime.h>
#include <hip/hip_bf16.h>

typedef unsigned short u16;
typedef unsigned v4u __attribute__((ext_vector_type(4)));
typedef unsigned v2u __attribute__((ext_vector_type(2)));
typedef unsigned v8u __attribute__((ext_vector_type(8)));
typedef __attribute__((ext_vector_type(8))) __bf16 bf16x8;
typedef __attribute__((ext_vector_type(16))) __bf16 bf16x16;
typedef __attribute__((ext_vector_type(4))) float f32x4;
typedef __attribute__((ext_vector_type(4))) short s16x4;
#define LAS __attribute__((address_space(3)))
#define DEV __device__ __forceinline__

namespace {

constexpr int NSEG = 64;
constexpr int NH = 32, NC = 72, NSPARE = 24, NWORK = 128;
constexpr unsigned SENT = 0xFFFFFFFFu;
constexpr unsigned SPIN_CAP = 1u << 22;
constexpr int NREP = 4;
constexpr int MEM_SLOT = 10240, P_SLOT = 20480, STEP_SLOT = NREP * (MEM_SLOT + P_SLOT);
constexpr int NPAGES = 8;
constexpr unsigned MBOX_BYTES = NPAGES * (2u << 20);

constexpr size_t CTRL_BYTES = 98304 + 4096;
constexpr size_t OFF_WVO = 0;
constexpr size_t SZ_WVO = (size_t)72 * 294912;
constexpr size_t OFF_KX = OFF_WVO + SZ_WVO;
constexpr size_t SZ_KX = (size_t)64 * 32 * 8192;
constexpr size_t OFF_QX = OFF_KX + SZ_KX;
constexpr size_t OFF_UX = OFF_QX + SZ_KX;
constexpr size_t SZ_UX = (size_t)64 * 72 * 16384;
constexpr size_t OFF_PX = OFF_UX + SZ_UX;
constexpr size_t SZ_PX = (size_t)64 * 81920;
constexpr size_t OFF_UM = OFF_PX + SZ_PX;
constexpr size_t SZ_UM = (size_t)64 * 72 * 4096;
constexpr size_t OFF_QKB = OFF_UM + SZ_UM;
constexpr size_t SZ_QKB = (size_t)2048 * 1152 * 2 + 4096;
constexpr size_t OFF_VTB = OFF_QKB + SZ_QKB;
constexpr size_t SZ_VTB = (size_t)608 * 2048 * 2;
constexpr size_t OFF_OB = OFF_VTB + SZ_VTB;
constexpr size_t SZ_OB = (size_t)2048 * 576 * 2;
constexpr size_t OFF_AB = OFF_OB + SZ_OB;
constexpr size_t OFF_GATE = OFF_AB + SZ_OB;
constexpr size_t SZ_GATE = 8192;
constexpr size_t DATA_BYTES = OFF_GATE + SZ_GATE;

constexpr int C_TMO = 0;
constexpr int C_POL = 64;
constexpr int C_CLR = 128;
constexpr int C_WVO = 192;
constexpr int C_AUX = 256;
constexpr int C_B1 = 320;
constexpr int C_FQ = 384;
constexpr int C_FA = 1152;
constexpr int C_FB = 1280;
constexpr int C_PROG = 512;
constexpr int C_UX = 1024;
constexpr int C_OB = 2048;
constexpr int C_KQ = 4096;
constexpr int C_PX = 8192;
constexpr int C_DBG = 12288;
constexpr int C_UM = 16384;
constexpr int C_CAL = 24576;

struct Params {
  const u16 *x, *mem0, *Wq, *Wk, *Wv, *Wo, *wqkv, *wo, *gw;
  u16* out;
  unsigned* ctrl;
  char* mbox;
  char* data;
};

DEV float bf2f(unsigned v) { return __builtin_bit_cast(float, v << 16); }
typedef __attribute__((ext_vector_type(2))) float f32x2;
typedef __attribute__((ext_vector_type(2))) __bf16 bf16x2;
DEV unsigned cvtpk(float lo, float hi) { return __builtin_bit_cast(unsigned, __builtin_convertvector((f32x2){lo, hi}, bf16x2)); }
DEV unsigned long long clk() { unsigned long long t; asm volatile("s_memtime %0\n\ts_waitcnt lgkmcnt(0)" : "=s"(t) :: "memory"); return t; }
DEV unsigned rt() { unsigned long long t; asm volatile("s_memrealtime %0\n\ts_waitcnt lgkmcnt(0)" : "=s"(t) :: "memory"); return (unsigned)t; }
DEV unsigned phys(unsigned o, unsigned polmask) { return ((o >> 13) << 14) | (((polmask >> (o >> 20)) & 1u) << 13) | (o & 8191u); }
DEV __amdgpu_buffer_rsrc_t mkrsrc(const void* p, unsigned bytes) { return __builtin_amdgcn_make_buffer_rsrc((void*)p, 0, (int)bytes, 0x00020000); }
DEV v4u ld_sc1(__amdgpu_buffer_rsrc_t r, unsigned off) { return __builtin_amdgcn_raw_buffer_load_b128(r, off, 0, 16); }
DEV v2u ld2_sc1(__amdgpu_buffer_rsrc_t r, unsigned off) { return __builtin_amdgcn_raw_buffer_load_b64(r, off, 0, 16); }
DEV unsigned ld1_sc1(__amdgpu_buffer_rsrc_t r, unsigned off) { return __builtin_amdgcn_raw_buffer_load_b32(r, off, 0, 16); }
DEV void st_sc1(v4u v, __amdgpu_buffer_rsrc_t r, unsigned off) { __builtin_amdgcn_raw_buffer_store_b128(v, r, off, 0, 16); }
DEV void st2_sc1(v2u v, __amdgpu_buffer_rsrc_t r, unsigned off) { __builtin_amdgcn_raw_buffer_store_b64(v, r, off, 0, 16); }
DEV void st1_sc1(unsigned v, __amdgpu_buffer_rsrc_t r, unsigned off) { __builtin_amdgcn_raw_buffer_store_b32(v, r, off, 0, 16); }
DEV void drain() { asm volatile("s_waitcnt vmcnt(0)" ::: "memory"); }
DEV unsigned max4(v4u v) { return max(max(v.x, v.y), max(v.z, v.w)); }
DEV v2u tr_read(const char* smem, unsigned off) {
  s16x4 v = __builtin_amdgcn_ds_read_tr16_b64_v4i16((LAS s16x4*)(smem + off));
  return __builtin_bit_cast(v2u, v);
}
DEV v4u trfrag(const char* smem, int off) {
  v2u x0 = tr_read(smem, off), x1 = tr_read(smem, off + 512);
  return (v4u){x0.x, x0.y, x1.x, x1.y};
}
DEV f32x4 mfma16(v4u a, v4u b, f32x4 c) {
  return __builtin_amdgcn_mfma_f32_16x16x32_bf16(__builtin_bit_cast(bf16x8, a), __builtin_bit_cast(bf16x8, b), c, 0, 0, 0);
}
DEV f32x4 smfma(v4u a, v8u b, f32x4 c, int idx) {
  return __builtin_amdgcn_smfmac_f32_16x16x64_bf16(__builtin_bit_cast(bf16x8, a), __builtin_bit_cast(bf16x16, b), c, idx, 0, 0);
}
template <int CTRL> DEV float dpp(float x) {
  return __builtin_bit_cast(float, __builtin_amdgcn_mov_dpp(__builtin_bit_cast(int, x), CTRL, 0xf, 0xf, true));
}
constexpr int ROR8 = 0x128;
DEV float amax(float a, float b) { float r; asm("v_max_f32 %0, %1, %2\n\ts_nop 1" : "=v"(r) : "v"(a), "v"(b)); return r; }
DEV float amax3(float a, float b, float c) { return fmaxf(fmaxf(a, b), c); }
DEV float xmax(float x) {
  auto s = __builtin_amdgcn_permlane16_swap(__float_as_uint(x), __float_as_uint(x), false, false);
  x = amax(__uint_as_float(s[0]), __uint_as_float(s[1]));
  auto t = __builtin_amdgcn_permlane32_swap(__float_as_uint(x), __float_as_uint(x), false, false);
  return amax(__uint_as_float(t[0]), __uint_as_float(t[1]));
}
DEV float xsum(float x) {
  auto s = __builtin_amdgcn_permlane16_swap(__float_as_uint(x), __float_as_uint(x), false, false);
  x = __uint_as_float(s[0]) + __uint_as_float(s[1]);
  auto t = __builtin_amdgcn_permlane32_swap(__float_as_uint(x), __float_as_uint(x), false, false);
  return __uint_as_float(t[0]) + __uint_as_float(t[1]);
}
DEV unsigned aload(unsigned* p) { return __hip_atomic_load(p, __ATOMIC_RELAXED, __HIP_MEMORY_SCOPE_AGENT); }
DEV void aadd(unsigned* p, unsigned v) { __hip_atomic_fetch_add(p, v, __ATOMIC_RELAXED, __HIP_MEMORY_SCOPE_AGENT); }
DEV void astore(unsigned* p, unsigned v) { __hip_atomic_store(p, v, __ATOMIC_RELAXED, __HIP_MEMORY_SCOPE_AGENT); }

DEV bool wait_ge(unsigned* w, unsigned target, unsigned* tmo, unsigned code) {
  for (unsigned spins = 0;; ++spins) {
    if ((unsigned)__builtin_amdgcn_readfirstlane(aload(w)) >= target) { __builtin_amdgcn_fence(__ATOMIC_ACQUIRE, "wavefront"); return true; }
    __builtin_amdgcn_s_sleep(2);
    if (spins > SPIN_CAP) { astore(tmo, code); return false; }
    if ((spins & 255u) == 255u && __builtin_amdgcn_readfirstlane(aload(tmo))) return false;
  }
}
DEV bool wait_slow(unsigned* w, unsigned target, unsigned* tmo, unsigned code) {
  for (unsigned spins = 0;; ++spins) {
    if ((unsigned)__builtin_amdgcn_readfirstlane(aload(w)) >= target) { __builtin_amdgcn_fence(__ATOMIC_ACQUIRE, "wavefront"); return true; }
    __builtin_amdgcn_s_sleep(12);
    if (spins > SPIN_CAP) { astore(tmo, code); return false; }
    if ((spins & 63u) == 63u && __builtin_amdgcn_readfirstlane(aload(tmo))) return false;
  }
}
DEV bool wait_flags(unsigned* w, int n, int st, unsigned target, int lane, unsigned* tmo, unsigned code) {
  for (unsigned spins = 0;; ++spins) {
    unsigned a = aload(w + min(lane, n - 1) * st), b = aload(w + min(lane + 64, n - 1) * st);
    if (__builtin_amdgcn_ballot_w64(a < target || b < target) == 0) { __builtin_amdgcn_fence(__ATOMIC_ACQUIRE, "wavefront"); return true; }
    __builtin_amdgcn_s_sleep(6);
    if (spins > SPIN_CAP) { astore(tmo, code); return false; }
    if ((spins & 63u) == 63u && __builtin_amdgcn_readfirstlane(aload(tmo))) return false;
  }
}
DEV void wg_wait_ge(unsigned* w, unsigned target, unsigned* tmo, unsigned code, int wave) {
  if (wave == 0) wait_ge(w, target, tmo, code);
  __syncthreads();
}
DEV void wg_signal(unsigned* w, int tid) {
  drain();
  __syncthreads();
  if (tid == 0) aadd(w, 1u);
}
DEV void wait_until(unsigned t) {
  for (int guard = 0; guard < 64; ++guard) {
    int rem = (int)(t - rt());
    if (rem <= 0 || rem > 1000) return;
    if (rem <= 16) { int n = (rem * 24) >> 6; for (int i = 0; i < n; ++i) __builtin_amdgcn_s_sleep(1); return; }
    int n = ((rem - 12) * 24) >> 6; if (n > 16) n = 16;
    for (int i = 0; i < n; ++i) __builtin_amdgcn_s_sleep(1);
  }
}

#define DSTAMP(cond, slot)
#define DVAL(cond, slot, v)
#define DPT(k, ref)
#define DPDUMP(slot)
constexpr int L_MEM = 0;
constexpr int L_KQ = 10240;
constexpr int L_QD = 43008;
constexpr int L_KI = 47104;
constexpr int L_UIMG = 10240;
constexpr int L_RED = 14336;
constexpr int L_DEAD = 51200;
constexpr int L_PXS = 87040;
constexpr int L_STAGE = 53248;
constexpr int L_X = 0;
constexpr int XBUF = 36864;
constexpr int L_V = 110592;
constexpr int L_WST = 118784;
constexpr int LDS_BYTES = 160 * 1024;

struct MemAddr { unsigned o0, o1, o2, o3; };
DEV MemAddr mem_addr(int s, int rep, unsigned pol, int wave, int lane) {
  const unsigned base = s * STEP_SLOT + rep * MEM_SLOT;
  MemAddr a;
  a.o0 = phys(base + wave * 3072 + lane * 16, pol);
  a.o1 = phys(base + wave * 3072 + 1024 + lane * 16, pol);
  a.o2 = phys(base + wave * 3072 + 2048 + lane * 16, pol);
  a.o3 = phys(base + 9216, pol);
  asm volatile("" : "+v"(a.o0), "+v"(a.o1), "+v"(a.o2), "+v"(a.o3));
  return a;
}
DEV unsigned gather_mem(const Params& p, __amdgpu_buffer_rsrc_t rm, char* smem, int wave, int lane, const MemAddr& a) {
  v4u v0 = ld_sc1(rm, a.o0), v1 = ld_sc1(rm, a.o1), v2 = ld_sc1(rm, a.o2);
  unsigned mx = max(max(max4(v0), max4(v1)), max4(v2));
  unsigned spins = 0;
  if (__builtin_expect(__builtin_amdgcn_ballot_w64(mx == SENT) != 0, 0)) {
    for (;;) {
      asm volatile("" ::: "memory");
      v0 = ld_sc1(rm, a.o0); v1 = ld_sc1(rm, a.o1); v2 = ld_sc1(rm, a.o2);
      mx = max(max(max4(v0), max4(v1)), max4(v2));
      ++spins;
      if (__builtin_amdgcn_ballot_w64(mx == SENT) == 0) break;
      if (spins > SPIN_CAP) { astore(p.ctrl + C_TMO, 1u); *(LAS unsigned*)(smem + L_DEAD) = 1; break; }
      if ((spins & 1023) == 0 && __builtin_amdgcn_readfirstlane(aload(p.ctrl + C_TMO))) { *(LAS unsigned*)(smem + L_DEAD) = 1; break; }
    }
  }
  char* d = smem + L_MEM + wave * 3072 + lane * 16;
  *(v4u*)d = v0; *(v4u*)(d + 1024) = v1; *(v4u*)(d + 2048) = v2;
  return spins;
}
DEV v2u sched_fix(__amdgpu_buffer_rsrc_t rm, unsigned off, v2u v) {
  for (int i = 0; i < 8 && (unsigned)__builtin_amdgcn_readfirstlane(max(v.x, v.y)) == SENT; ++i) {
    asm volatile("" ::: "memory");
    v = ld2_sc1(rm, off);
  }
  if ((unsigned)__builtin_amdgcn_readfirstlane(max(v.x, v.y)) == SENT) { unsigned n = rt(); v = (v2u){n, n}; }
  return v;
}
DEV void zero_acc(f32x4 (&acc)[4]) {
#pragma unroll
  for (int i = 0; i < 4; ++i) acc[i] = (f32x4){0, 0, 0, 0};
  asm volatile("" : "+v"(acc[0]), "+v"(acc[1]), "+v"(acc[2]), "+v"(acc[3]));
}
DEV void proj36(const char* smem, const v8u (&w)[4][9], int lane, f32x4 (&acc)[4]) {
  const int idx = (lane & 8) ? 0xEEEEEEEE : 0x44444444;
  v4u a[9];
  __builtin_amdgcn_sched_barrier(0);
#pragma unroll
  for (int t = 0; t < 9; ++t) a[t] = *(const v4u*)(smem + L_MEM + t * 1024 + lane * 16);
  __builtin_amdgcn_sched_barrier(0);
#pragma unroll
  for (int t = 0; t < 9; ++t) {
#pragma unroll
    for (int i = 0; i < 4; ++i) acc[i] = smfma(a[t], w[i][t], acc[i], idx);
  }
  __builtin_amdgcn_sched_barrier(0);
}
DEV v2u pairsum(f32x4 a0, f32x4 a1) {
  float r[4];
#pragma unroll
  for (int i = 0; i < 4; ++i) {
    auto sw = __builtin_amdgcn_permlane32_swap(__float_as_uint(a0[i]), __float_as_uint(a1[i]), false, false);
    r[i] = __uint_as_float(sw[0]) + __uint_as_float(sw[1]);
  }
  return (v2u){cvtpk(r[0], r[1]), cvtpk(r[2], r[3])};
}
DEV unsigned read_pol(const Params& p) {
  return __builtin_amdgcn_readfirstlane(aload(p.ctrl + C_POL)) & 0xffu;
}

template <int WV>
DEV void head_loop(const Params& p, char* smem, int h, int lane, const v8u (&w)[4][9], unsigned pol) {
  constexpr int wave = WV;
  __amdgpu_buffer_rsrc_t rm = mkrsrc(p.mbox, MBOX_BYTES);
  __amdgpu_buffer_rsrc_t rkx = mkrsrc(p.data + OFF_KX, SZ_KX);
  __amdgpu_buffer_rsrc_t rqx = mkrsrc(p.data + OFF_QX, SZ_KX);
  __amdgpu_buffer_rsrc_t rpx = mkrsrc(p.data + OFF_PX, SZ_PX);
  const float SC = 0.08838834764831845f * 1.4426950408889634f;
  const int g = lane >> 4, qi = lane & 15, hi = (lane >> 3) & 1;
  const float NEG = -1e30f;
  const int wr_e = 64 * (wave & 1) + 16 * (lane >> 5) + (lane & 15);
  const int wr_off = wr_e * 32 + ((lane >> 4) & 1) * 8;
  const float c0 = (g < 2) ? 0.f : NEG;
  const int rep = h & 3;
  unsigned tM = rt();
  for (int s = 0; s < NSEG; ++s) {
    v4u kx[2][4], qx[4];
    unsigned adx[NREP];
    if (wave < 3) {
#pragma unroll
      for (int tl = 0; tl < 2; ++tl)
#pragma unroll
        for (int u = 0; u < 4; ++u) kx[tl][u] = *(const v4u*)(smem + L_KQ + (s & 1) * 16384 + (tl * 4 + u) * 1024 + lane * 16);
      if (wave > 0) {
#pragma unroll
        for (int u = 0; u < 4; ++u) qx[u] = *(const v4u*)(smem + L_KQ + (s & 1) * 16384 + 8192 + ((wave - 1) * 4 + u) * 1024 + lane * 16);
      }
    }
    if (wave == 0) {
#pragma unroll
      for (int rp = 0; rp < NREP; ++rp) {
        const unsigned base = s * STEP_SLOT + NREP * MEM_SLOT + rp * P_SLOT;
        adx[rp] = hi ? phys(base + 16384 + (h >> 3) * 1024 + (h & 3) * 256 + ((h >> 2) & 1) * 128 + (qi & 7) * 16, pol)
                     : phys(base + (h >> 1) * 1024 + g * 256 + (h & 1) * 128 + (qi & 7) * 16, pol);
        if (hi && g != 0) adx[rp] = 0x7ffffff0u;
        asm volatile("" : "+v"(adx[rp]));
      }
    }
    MemAddr ma = mem_addr(s, rep, pol, wave, lane);
    f32x4 acc[4]; zero_acc(acc);
    f32x4 t0 = {c0, c0, c0, c0}, t1 = {0, 0, 0, 0}, t2 = {0, 0, 0, 0};
    if (wave == 1 || wave == 2) {
#pragma unroll
      for (int u = 0; u < 4; ++u) { t1 = mfma16(kx[0][u], qx[u], t1); t2 = mfma16(kx[1][u], qx[u], t2); }
    }
    asm volatile("" : "+v"(t0), "+v"(t1), "+v"(t2));
    if (wave < 3) { wait_until(tM); gather_mem(p, rm, smem, wave, lane, ma); }
    __syncthreads();
    v2u sch = ld2_sc1(rm, ma.o3);
    proj36(smem, w, lane, acc);
    v2u pk0 = pairsum(acc[0], acc[1]), pk1 = pairsum(acc[2], acc[3]);
    if (wave < 2) {
      *(v2u*)(smem + L_QD + wr_off) = pk0; *(v2u*)(smem + L_QD + wr_off + 1024) = pk1;
      *(v2u*)(smem + L_QD + wr_off + 16) = pk0; *(v2u*)(smem + L_QD + wr_off + 1024 + 16) = pk1;
    } else {
      *(v2u*)(smem + L_KI + wr_off) = pk0; *(v2u*)(smem + L_KI + wr_off + 1024) = pk1;
    }
    __syncthreads();
    if (wave == 0) {
      v4u qd[4], km[4];
#pragma unroll
      for (int u = 0; u < 4; ++u) qd[u] = trfrag(smem, L_QD + u * 1024 + lane * 8);
#pragma unroll
      for (int u = 0; u < 4; ++u) km[u] = trfrag(smem, L_KI + u * 1024 + lane * 8);
      __builtin_amdgcn_sched_barrier(0);
#pragma unroll
      for (int u = 0; u < 4; ++u) { t1 = mfma16(kx[0][u], qd[u], t1); t2 = mfma16(kx[1][u], qd[u], t2); }
#pragma unroll
      for (int u = 0; u < 4; ++u) t0 = mfma16(km[u], qd[u], t0);
      float v0 = (hi ? t2[0] : t1[0]) * SC, v1 = (hi ? t2[1] : t1[1]) * SC, v2 = (hi ? t2[2] : t1[2]) * SC, v3 = (hi ? t2[3] : t1[3]) * SC;
      float v4 = (hi ? t0[2] : t0[0]) * SC, v5 = (hi ? t0[3] : t0[1]) * SC;
      float m = fmaxf(amax3(v0, v1, v2), amax3(v3, v4, v5));
      m = amax(m, dpp<ROR8>(m));
      m = xmax(m);
      v0 = __builtin_amdgcn_exp2f(v0 - m); v1 = __builtin_amdgcn_exp2f(v1 - m); v2 = __builtin_amdgcn_exp2f(v2 - m);
      v3 = __builtin_amdgcn_exp2f(v3 - m); v4 = __builtin_amdgcn_exp2f(v4 - m); v5 = __builtin_amdgcn_exp2f(v5 - m);
      float l = ((v0 + v1) + (v2 + v3)) + (v4 + v5);
      l += dpp<ROR8>(l);
      l = xsum(l);
      float inv = __builtin_amdgcn_rcpf(l);
      v2u px = {min(cvtpk(v0 * inv, v1 * inv), 0xFFFFFFFEu), min(cvtpk(v2 * inv, v3 * inv), 0xFFFFFFFEu)};
      unsigned pm = min(cvtpk(v4 * inv, v5 * inv), 0xFFFFFFFEu);
      unsigned ox = __builtin_amdgcn_mov_dpp(px.x, ROR8, 0xf, 0xf, true), oy = __builtin_amdgcn_mov_dpp(px.y, ROR8, 0xf, 0xf, true);
      unsigned pmo = __builtin_amdgcn_mov_dpp(pm, ROR8, 0xf, 0xf, true);
      auto s1 = __builtin_amdgcn_permlane16_swap(pm, pm, false, false);
      auto s2 = __builtin_amdgcn_permlane16_swap(pmo, pmo, false, false);
      v4u o;
      o.x = hi ? pmo : px.x; o.y = hi ? pm : px.y; o.z = hi ? s2[1] : ox; o.w = hi ? s1[1] : oy;
#pragma unroll
      for (int rp = 0; rp < NREP; ++rp) st_sc1(o, rm, adx[rp]);
    } else if (wave < 3) {
      v4u km[4];
#pragma unroll
      for (int u = 0; u < 4; ++u) km[u] = trfrag(smem, L_KI + u * 1024 + lane * 8);
#pragma unroll
      for (int u = 0; u < 4; ++u) t0 = mfma16(km[u], qx[u], t0);
      float v[12];
      const int tq = 16 * (wave - 1) + qi;
#pragma unroll
      for (int r = 0; r < 4; ++r) {
        v[r] = t0[r] * SC;
        v[4 + r] = (4 * g + r <= tq) ? t1[r] * SC : NEG;
        v[8 + r] = (16 + 4 * g + r <= tq) ? t2[r] * SC : NEG;
      }
      float m = amax3(amax3(v[0], v[1], v[2]), amax3(v[3], v[4], v[5]), fmaxf(amax3(v[6], v[7], v[8]), amax3(v[9], v[10], v[11])));
      m = xmax(m);
      float l = 0;
#pragma unroll
      for (int i = 0; i < 12; ++i) { v[i] = __builtin_amdgcn_exp2f(v[i] - m); l += v[i]; }
      l = xsum(l);
      float inv = __builtin_amdgcn_rcpf(l);
#pragma unroll
      for (int i = 0; i < 12; ++i) v[i] *= inv;
      v4u px = {cvtpk(v[4], v[5]), cvtpk(v[6], v[7]), cvtpk(v[8], v[9]), cvtpk(v[10], v[11])};
      v2u pm = {cvtpk(v[0], v[1]), cvtpk(v[2], v[3])};
      constexpr int qt = wave - 1;
      *(v4u*)(smem + L_PXS + qt * 1024 + lane * 16) = px;
      if (g < 2) *(v2u*)(smem + L_PXS + 2048 + qt * 256 + qi * 16 + g * 8) = pm;
    } else {
      for (int i = 0; i < 8; ++i) __builtin_amdgcn_s_sleep(1);
      if (s + 1 < NSEG) {
        if (!wait_ge(p.ctrl + C_KQ + (s + 1) * 32 + h, 1u, p.ctrl + C_TMO, 2u)) *(LAS unsigned*)(smem + L_DEAD) = 1;
        v4u v[16];
#pragma unroll
        for (int i = 0; i < 8; ++i) v[i] = ld_sc1(rkx, ((s + 1) * 32 + h) * 8192 + i * 1024 + lane * 16);
#pragma unroll
        for (int i = 0; i < 8; ++i) v[8 + i] = ld_sc1(rqx, ((s + 1) * 32 + h) * 8192 + i * 1024 + lane * 16);
#pragma unroll
        for (int i = 0; i < 16; ++i) *(v4u*)(smem + L_KQ + ((s + 1) & 1) * 16384 + i * 1024 + lane * 16) = v[i];
      }
    }
    sch = sched_fix(rm, ma.o3, sch);
    tM = __builtin_amdgcn_readfirstlane(sch.y);
    __syncthreads();
    if (wave == 3) {
      if (s + 1 >= NSEG) drain();
      if (s > 0 && lane == 0) astore(p.ctrl + C_PX + h * 32, (unsigned)s);
      v4u a0 = *(const v4u*)(smem + L_PXS + lane * 16), a1 = *(const v4u*)(smem + L_PXS + 1024 + lane * 16);
      v4u a2 = *(const v4u*)(smem + L_PXS + 2048 + (lane & 31) * 16);
      st_sc1(a0, rpx, s * 81920 + h * 1024 + lane * 16);
      st_sc1(a1, rpx, s * 81920 + (32 + h) * 1024 + lane * 16);
      if (lane < 32) st_sc1(a2, rpx, s * 81920 + 65536 + (((lane >> 4) * 8 + (h >> 2)) * 64 + (h & 3) * 16 + (lane & 15)) * 16);
      if (s + 1 >= NSEG) { drain(); if (lane == 0) astore(p.ctrl + C_PX + h * 32, (unsigned)s + 1u); }
    }
    if (*(volatile LAS unsigned*)(smem + L_DEAD)) break;
  }
}

DEV void head_role(const Params& p, char* smem, int h, int wave, int lane, int tid) {
  v8u w[4][9];
  {
    const int gB = lane >> 4, q4 = (lane & 15) >> 2, p4 = lane & 3;
    v4u st[3][8];
    auto ldstage = [&](int t) {
#pragma unroll
      for (int i = 0; i < 8; ++i) {
        int ch = tid + 256 * i; int row = ch >> 5, cc = ch & 31;
        const u16* src = (cc < 16 ? p.Wq : p.Wk) + (size_t)(64 * t + row) * 4096 + h * 128 + (cc & 15) * 8;
        st[t % 3][i] = *(const v4u*)src;
      }
    };
    ldstage(0); ldstage(1); ldstage(2);
#pragma unroll
    for (int t = 0; t < 9; ++t) {
      __syncthreads();
#pragma unroll
      for (int i = 0; i < 8; ++i) { int ch = tid + 256 * i; int row = ch >> 5, cc = ch & 31; *(v4u*)(smem + L_STAGE + row * 528 + cc * 16) = st[t % 3][i]; }
      if (t + 3 < 9) ldstage(t + 3);
      __syncthreads();
#pragma unroll
      for (int tl = 0; tl < 4; ++tl) {
        v8u ww;
#pragma unroll
        for (int X = 0; X < 2; ++X)
#pragma unroll
          for (int half = 0; half < 2; ++half) {
            int row0 = 8 * (4 * half + 2 * X + (gB >> 1)) + 4 * (gB & 1);
            v2u v = tr_read(smem, L_STAGE + (row0 + q4) * 528 + (64 * wave + 16 * tl + 4 * p4) * 2);
            ww[4 * X + half] = v.x; ww[4 * X + 2 + half] = v.y;
          }
        w[tl][t] = ww;
      }
    }
  }
  DSTAMP(h == 0 && wave == 0, 0);
  for (int i = tid; i < 2048; i += 256) *(unsigned*)(smem + L_QD + i * 4) = 0;
  if (tid == 0) *(unsigned*)(smem + L_DEAD) = 0;
  if (wave == 0) { wait_ge(p.ctrl + C_CLR, NSPARE, p.ctrl + C_TMO, 3u); wait_ge(p.ctrl + C_KQ + h, 1u, p.ctrl + C_TMO, 4u); }
  __syncthreads();
  const unsigned pol = read_pol(p);
  {
    __amdgpu_buffer_rsrc_t rkx = mkrsrc(p.data + OFF_KX, SZ_KX);
    __amdgpu_buffer_rsrc_t rqx = mkrsrc(p.data + OFF_QX, SZ_KX);
    v4u v[4];
#pragma unroll
    for (int i = 0; i < 4; ++i) { int f = wave * 4 + i; v[i] = ld_sc1(wave < 2 ? rkx : rqx, h * 8192 + (f & 7) * 1024 + lane * 16); }
#pragma unroll
    for (int i = 0; i < 4; ++i) *(v4u*)(smem + L_KQ + (wave * 4 + i) * 1024 + lane * 16) = v[i];
  }
  __syncthreads();
  DSTAMP(h == 0 && wave == 0, 1);
  if (wave == 0) head_loop<0>(p, smem, h, lane, w, pol);
  else if (wave == 1) head_loop<1>(p, smem, h, lane, w, pol);
  else if (wave == 2) head_loop<2>(p, smem, h, lane, w, pol);
  else head_loop<3>(p, smem, h, lane, w, pol);
}

#define RAW_BARRIER() do { asm volatile("s_waitcnt lgkmcnt(0)" ::: "memory"); __builtin_amdgcn_s_barrier(); } while (0)
constexpr int L_WOST = 0;
constexpr int L_AST = 118784;
DEV void load_wo(const Params& p, char* smem, int h, int wave, int lane, int tid, v4u (&wo)[9][4]) {
  constexpr int RS = 1152 + 16;
  const int g = lane >> 4, q4 = (lane & 15) >> 2, p4 = lane & 3;
  v4u st[2][18];
#pragma unroll
  for (int hf = 0; hf < 2; ++hf)
#pragma unroll
    for (int i = 0; i < 18; ++i) { int ch = tid + 256 * i; st[hf][i] = *(const v4u*)(p.Wo + ((size_t)h * 128 + 64 * hf) * 576 + (size_t)ch * 8); }
#pragma unroll
  for (int hf = 0; hf < 2; ++hf) {
    __syncthreads();
#pragma unroll
    for (int i = 0; i < 18; ++i) { int ch = tid + 256 * i; *(v4u*)(smem + L_WOST + (ch / 72) * RS + (ch % 72) * 16) = st[hf][i]; }
    __syncthreads();
#pragma unroll
    for (int tl = 0; tl < 9; ++tl)
#pragma unroll
      for (int uu = 0; uu < 2; ++uu) {
        int a0 = L_WOST + (32 * uu + 4 * g + q4) * RS + ((wave * 9 + tl) * 16 + 4 * p4) * 2;
        v2u x0 = tr_read(smem, a0), x1 = tr_read(smem, a0 + 16 * RS);
        wo[tl][2 * hf + uu] = (v4u){x0.x, x0.y, x1.x, x1.y};
      }
  }
  __syncthreads();
}
struct ABlk { v4u v[4]; };
DEV void wv_block_load(const Params& p, int h, int t, int tid, ABlk& b) {
#pragma unroll
  for (int i = 0; i < 4; ++i) { int ch = tid + 256 * i; b.v[i] = *(const v4u*)(p.Wv + (size_t)(64 * t + (ch >> 4)) * 4096 + h * 128 + (ch & 15) * 8); }
}
template <int TL0, int TL1>
DEV void wvo_unit(const Params& p, char* smem, int h, int t, int slot, int wave, int lane, int tid, const ABlk& blk, const v4u (&wo)[9][4]) {
  const int g = lane >> 4;
  __amdgpu_buffer_rsrc_t rw = mkrsrc(p.data + OFF_WVO, SZ_WVO);
  char* ab = smem + L_AST + slot * 17408;
#pragma unroll
  for (int i = 0; i < 4; ++i) { int ch = tid + 256 * i; *(v4u*)(ab + (ch >> 4) * 272 + (ch & 15) * 16) = blk.v[i]; }
  RAW_BARRIER();
  v4u a[4][4];
#pragma unroll
  for (int tt = 0; tt < 4; ++tt)
#pragma unroll
    for (int u = 0; u < 4; ++u) {
      const char* src = ab + (16 * tt + (lane & 15)) * 272 + (32 * u + 4 * g) * 2;
      v2u x0 = *(const v2u*)src, x1 = *(const v2u*)(src + 32);
      a[tt][u] = (v4u){x0.x, x0.y, x1.x, x1.y};
    }
#pragma unroll
  for (int tl = TL0; tl < TL1; ++tl) {
    f32x4 d[4];
#pragma unroll
    for (int tt = 0; tt < 4; ++tt) {
      d[tt] = (f32x4){0, 0, 0, 0};
#pragma unroll
      for (int u = 0; u < 4; ++u) d[tt] = mfma16(a[tt][u], wo[tl][u], d[tt]);
    }
    const int n = (wave * 9 + tl) * 16 + (lane & 15), c = n >> 3;
    unsigned off = ((c * 16 + (h >> 1)) * 9 + t) * 2048 + (g * 16 + (h & 1) * 8 + (n & 7)) * 16;
    st_sc1((v4u){cvtpk(d[0][0], d[0][1]), cvtpk(d[0][2], d[0][3]), cvtpk(d[2][0], d[2][1]), cvtpk(d[2][2], d[2][3])}, rw, off);
    st_sc1((v4u){cvtpk(d[1][0], d[1][1]), cvtpk(d[1][2], d[1][3]), cvtpk(d[3][0], d[3][1]), cvtpk(d[3][2], d[3][3])}, rw, off + 1024);
  }
}

#define SCHED_A0 130
#define SCHED_B0 72
DEV void col_role(const Params& p, char* smem, int c, int wave, int lane, int tid) {
  __amdgpu_buffer_rsrc_t rm = mkrsrc(p.mbox, MBOX_BYTES);
  __amdgpu_buffer_rsrc_t rux = mkrsrc(p.data + OFF_UX, SZ_UX);
  __amdgpu_buffer_rsrc_t rum = mkrsrc(p.data + OFF_UM, SZ_UM);
  __amdgpu_buffer_rsrc_t rwv = mkrsrc(p.data + OFF_WVO, SZ_WVO);
  if (c < 64) {
    const int hh = ((c >> 3) << 2) | (c & 3), par = (c >> 2) & 1;
    ABlk b0, b1, b2;
    v4u wo[9][4];
    wv_block_load(p, hh, par, tid, b0); wv_block_load(p, hh, par + 2, tid, b1); wv_block_load(p, hh, par + 4, tid, b2);
    load_wo(p, smem, hh, wave, lane, tid, wo);
    wvo_unit<0, 9>(p, smem, hh, par, 0, wave, lane, tid, b0, wo);
    wvo_unit<0, 9>(p, smem, hh, par + 2, 1, wave, lane, tid, b1, wo);
    wvo_unit<0, 9>(p, smem, hh, par + 4, 0, wave, lane, tid, b2, wo);
    wg_signal(p.ctrl + C_WVO, tid);
  }
  if (tid == 0) *(unsigned*)(smem + L_DEAD) = 0;
  if (wave == 0) wait_ge(p.ctrl + C_CLR, NSPARE, p.ctrl + C_TMO, 5u);
  __syncthreads();
  DSTAMP(c == 0 && wave == 0, 2);
  const unsigned pol = read_pol(p);
  const int rep = c & 3;
  if (wave == 0) {
    if (lane < 8 * NREP) {
      int r = lane & 7, rp = lane >> 3;
      v4u v = *(const v4u*)(p.mem0 + r * 576 + 8 * c);
      v.x = min(v.x, 0xFFFFFFFEu); v.y = min(v.y, 0xFFFFFFFEu); v.z = min(v.z, 0xFFFFFFFEu); v.w = min(v.w, 0xFFFFFFFEu);
      st_sc1(v, rm, phys(rp * MEM_SLOT + (c >> 3) * 1024 + ((c & 3) * 16 + ((c >> 2) & 1) * 8 + r) * 16, pol));
    }
    if (c == 0) {
      unsigned now = rt(); if (now == SENT) now = 0;
      if (lane < NREP) st_sc1((v4u){now, now, 0u, 0u}, rm, phys(lane * MEM_SLOT + 9216, pol));
    }
    wait_ge(p.ctrl + C_WVO, 128u, p.ctrl + C_TMO, 6u);
  }
  __syncthreads();
  DSTAMP(c == 0 && wave == 0, 3);
  v8u w[4][9];
#pragma unroll
  for (int tl = 0; tl < 4; ++tl)
#pragma unroll
    for (int t = 0; t < 9; ++t) {
      unsigned off = ((c * 16 + wave * 4 + tl) * 9 + t) * 2048 + lane * 16;
      v4u x0 = ld_sc1(rwv, off), x1 = ld_sc1(rwv, off + 1024);
      v8u ww;
      ww[0] = x0.x; ww[2] = x0.y; ww[1] = x0.z; ww[3] = x0.w;
      ww[4] = x1.x; ww[6] = x1.y; ww[5] = x1.z; ww[7] = x1.w;
      w[tl][t] = ww;
    }
  const int g = lane >> 4, half = (lane >> 3) & 1, n7 = lane & 7;
  const int uw_off = L_UIMG + (((2 * (4 * wave + (lane >> 5)) + ((lane & 15) >> 3)) * 8 + (lane & 7)) * 8 + 4 * ((lane >> 4) & 1)) * 2;
  const int red_w = ((g >> 1) == half) ? L_RED + (((wave * 2 + half) * 8 + n7) * 8 + 4 * (g & 1)) * 4 : L_RED + 2048 + lane * 16;
  const int red_r = L_RED + (((2 * g) * 8 + ((lane >> 1) & 7)) * 8 + 4 * (lane & 1)) * 4;
  asm volatile("" :: "v"(w[3][8]));
  DSTAMP(c == 0 && wave == 0, 20);
  v4u uxn[4];
  wait_ge(p.ctrl + C_UX, 32u, p.ctrl + C_TMO, 7u);
#pragma unroll
  for (int q = 0; q < 4; ++q) uxn[q] = ld_sc1(rux, c * 16384 + (wave * 4 + q) * 1024 + lane * 16);
  wait_ge(p.ctrl + C_UX + 1, 32u, p.ctrl + C_TMO, 7u);
  DSTAMP(c == 0 && wave == 0, 21);
  int rdy = 2;
  unsigned tM = rt();
  int schedA = SCHED_A0, schedB = SCHED_B0, streak = 0, floorA = 0, floorB = 0; bool prevLate = true;
  for (int s = 0; s < NSEG; ++s) {
    MemAddr ma = mem_addr(s, rep, pol, wave, lane);
    f32x4 acc[4]; zero_acc(acc);
    unsigned mfail = 0;
    const unsigned tM0 = tM; (void)tM0;
    DPT(0, tM0);
    if (wave < 3) { wait_until(tM); mfail = gather_mem(p, rm, smem, wave, lane, ma); }
    DPT(1, tM0);
    __syncthreads();
    DPT(2, tM0);
    v2u sch = ld2_sc1(rm, ma.o3);
    v4u ux[4];
#pragma unroll
    for (int q = 0; q < 4; ++q) ux[q] = uxn[q];
    unsigned fl2 = 32u;
    if (s + 1 < NSEG - 1) {
#pragma unroll
      for (int q = 0; q < 4; ++q) uxn[q] = ld_sc1(rux, ((s + 1) * 72 + c) * 16384 + (wave * 4 + q) * 1024 + lane * 16);
      if (rdy < s + 4 && rdy < NSEG) fl2 = aload(p.ctrl + C_UX + lane);
    }
    proj36(smem, w, lane, acc);
    v2u pk0 = pairsum(acc[0], acc[1]), pk1 = pairsum(acc[2], acc[3]);
    asm volatile("" : "+v"(pk0), "+v"(pk1));
    DPT(3, tM0);
    *(v2u*)(smem + uw_off) = pk0;
    *(v2u*)(smem + uw_off + 512) = pk1;
    v4u ub = *(const v4u*)(smem + L_UIMG + ((8 * wave + 4 * half + g) * 8 + n7) * 16);
    if (s < NSEG - 1) {
      const unsigned base = s * STEP_SLOT + NREP * MEM_SLOT + rep * P_SLOT;
      unsigned o[5];
#pragma unroll
      for (int q = 0; q < 4; ++q) o[q] = phys(base + (wave * 4 + q) * 1024 + lane * 16, pol);
      o[4] = phys(base + 16384 + wave * 1024 + lane * 16, pol);
      unsigned ado = phys((s + 1) * STEP_SLOT + (lane >> 4) * MEM_SLOT + (c >> 3) * 1024 + ((c & 3) * 16 + ((c >> 2) & 1) * 8 + ((lane >> 1) & 7)) * 16, pol);
      if (lane & 1) ado = 0x7ffffff0u;
      asm volatile("" : "+v"(ado), "+v"(o[0]), "+v"(o[1]), "+v"(o[2]), "+v"(o[3]), "+v"(o[4]));
      asm volatile("s_waitcnt vmcnt(0)" : "+v"(uxn[0]), "+v"(uxn[1]), "+v"(uxn[2]), "+v"(uxn[3]), "+v"(ub), "+v"(fl2) :: "memory");
      if (__builtin_expect(rdy < s + 4 && rdy < NSEG, 0)) {
        unsigned long long nb = __builtin_amdgcn_ballot_w64(fl2 < 32u);
        rdy = nb ? (int)__builtin_ctzll(nb) : NSEG;
        __builtin_amdgcn_fence(__ATOMIC_ACQUIRE, "wavefront");
        if (rdy < s + 3 && s + 2 < NSEG - 1) {
          if (!wait_ge(p.ctrl + C_UX + s + 2, 32u, p.ctrl + C_TMO, 7u)) *(LAS unsigned*)(smem + L_DEAD) = 1;
          rdy = s + 3;
        }
      }
      DPT(4, tM0);
      sch = sched_fix(rm, ma.o3, sch);
      const unsigned tP = __builtin_amdgcn_readfirstlane(sch.x);
      tM = __builtin_amdgcn_readfirstlane(sch.y);
      DPT(5, tM0);
      wait_until(tP);
      DPT(6, tM0);
      f32x4 d = {0, 0, 0, 0};
      unsigned pfail = 0;
      {
        v4u v[5];
#pragma unroll
        for (int q = 0; q < 5; ++q) v[q] = ld_sc1(rm, o[q]);
        unsigned mx = 0;
#pragma unroll
        for (int q = 0; q < 4; ++q) { d = mfma16(ux[q], v[q], d); mx = max(mx, max4(v[q])); }
        d = mfma16(ub, v[4], d); mx = max(mx, max4(v[4]));
        if (__builtin_expect(__builtin_amdgcn_ballot_w64(mx == SENT) != 0, 0)) {
          for (;;) {
            asm volatile("" ::: "memory");
#pragma unroll
            for (int q = 0; q < 5; ++q) v[q] = ld_sc1(rm, o[q]);
            mx = 0;
#pragma unroll
            for (int q = 0; q < 5; ++q) mx = max(mx, max4(v[q]));
            ++pfail;
            if (__builtin_amdgcn_ballot_w64(mx == SENT) == 0) break;
            if (pfail > SPIN_CAP) { astore(p.ctrl + C_TMO, 8u); *(LAS unsigned*)(smem + L_DEAD) = 1; break; }
            if ((pfail & 1023) == 0 && __builtin_amdgcn_readfirstlane(aload(p.ctrl + C_TMO))) { *(LAS unsigned*)(smem + L_DEAD) = 1; break; }
          }
          d = (f32x4){0, 0, 0, 0};
#pragma unroll
          for (int q = 0; q < 4; ++q) d = mfma16(ux[q], v[q], d);
          d = mfma16(ub, v[4], d);
        }
      }
      asm volatile("" : "+v"(d));
      DPT(7, tM0);
      *(f32x4*)(smem + red_w) = d;
      if (c == 0 && lane == 0) *(LAS unsigned*)(smem + L_DEAD + 16 + wave * 4) = (mfail ? 1u : 0u) | (pfail ? 2u : 0u);
      __syncthreads();
      if (wave == 0) {
        f32x4 x0 = *(const f32x4*)(smem + red_r), x1 = *(const f32x4*)(smem + red_r + 256);
        f32x4 x = x0 + x1;
        float r0 = xsum(x[0]), r1 = xsum(x[1]), r2 = xsum(x[2]), r3 = xsum(x[3]);
        v2u o2 = {min(cvtpk(r0, r1), 0xFFFFFFFEu), min(cvtpk(r2, r3), 0xFFFFFFFEu)};
        v4u o4 = {o2.x, o2.y, (unsigned)__builtin_amdgcn_mov_dpp(o2.x, 0xB1, 0xf, 0xf, true), (unsigned)__builtin_amdgcn_mov_dpp(o2.y, 0xB1, 0xf, 0xf, true)};
        st_sc1(o4, rm, ado);
      } else if (wave == 3 && c == 0) {
        v4u f = *(const v4u*)(smem + L_DEAD + 16);
        unsigned fl = __builtin_amdgcn_readfirstlane(f.x | f.y | f.z | f.w);
        unsigned now = rt();
        unsigned baseT = tM;
        const bool late = (int)(now - tM) > 2 || (int)(tM - now) > 600;
        if (late) baseT = now + 70u;
        if (!prevLate) {
          if (fl & 1u) { schedB += 2; floorB = schedB; streak = 0; }
          else if (fl & 2u) { schedA += 2; floorA = schedA; streak = 0; }
          else if (late) { schedB += 2; floorB = schedB; streak = 0; }
          else if (++streak >= 3) { streak = 0; if ((s & 1) && schedA > floorA) schedA -= 1; else if (schedB > floorB) schedB -= 1; else if (schedA > floorA) schedA -= 1; }
        }
        prevLate = late || (fl & 2u);
        unsigned nP = (baseT + (unsigned)schedA) , nM = nP + (unsigned)schedB;
        if (nP == SENT) nP = 0; if (nM == SENT) nM = 0;
        if (lane < NREP) st_sc1((v4u){nP, nM, 0u, 0u}, rm, phys((s + 1) * STEP_SLOT + lane * MEM_SLOT + 9216, pol));
        if (lane < 16) astore(p.ctrl + C_PROG + lane * 32, (unsigned)s + 1u);
        DVAL(true, 64 + s, now); DVAL(true, 128 + s, fl | (schedA << 8) | (schedB << 20));
      }
    }
    else __syncthreads();
    if (wave == 3) {
      if (s + 1 >= NSEG) drain();
      if (s > 0 && lane == 0) astore(p.ctrl + C_UM + c * 32, (unsigned)s);
#pragma unroll
      for (int w4 = 0; w4 < 4; ++w4) {
        v4u u4 = *(const v4u*)(smem + L_UIMG + ((8 * w4 + 4 * half + g) * 8 + n7) * 16);
        st_sc1(u4, rum, ((s * 72 + c) * 4 + w4) * 1024 + lane * 16);
      }
      if (s + 1 >= NSEG) { drain(); if (lane == 0) astore(p.ctrl + C_UM + c * 32, (unsigned)s + 1u); }
    }
    if (*(volatile LAS unsigned*)(smem + L_DEAD)) break;
  }
  __syncthreads();
  if (c == 0 && tid < 16) astore(p.ctrl + C_PROG + tid * 32, (unsigned)NSEG + 1u);
  if (c == 5 || c == 10) DPDUMP(c == 5 ? 0 : 1);
}

DEV void spare_role(const Params& p, char* smem, int j, int wave, int lane, int tid) {
  __amdgpu_buffer_rsrc_t rm = mkrsrc(p.mbox, MBOX_BYTES);
  if (j == 0 && tid == 0) {
    const unsigned grp = (__builtin_amdgcn_s_getreg(20 | (3 << 11)) >> 2) & 1u;
    unsigned m = 0;
    for (int i = 0; i < NPAGES; ++i) { unsigned r = p.ctrl[C_CAL + i]; m |= ((r ^ (r >> 1) ^ grp) & 1u) << i; }
    astore(p.ctrl + C_POL, 0x100u | m);
  }
  wg_wait_ge(p.ctrl + C_POL, 0x100u, p.ctrl + C_TMO, 9u, wave);
  const unsigned pol = read_pol(p);
  DSTAMP(j == 0 && wave == 0, 4); DVAL(j == 0 && wave == 0, 5, pol);
  {
    const v4u sv = {SENT, SENT, SENT, SENT};
    constexpr unsigned TOT = (unsigned)(NSEG + 1) * STEP_SLOT / 16;
    for (unsigned i = j * 256 + tid; i < TOT; i += NSPARE * 256) st_sc1(sv, rm, phys(i * 16, pol));
  }
  wg_signal(p.ctrl + C_CLR, tid);
  DSTAMP(j == 0 && wave == 0, 6);
  {
    __amdgpu_buffer_rsrc_t rux = mkrsrc(p.data + OFF_UX, SZ_UX);
    __amdgpu_buffer_rsrc_t rkq = mkrsrc(p.data + OFF_KX, 2 * SZ_KX);
    const int xg = j & 3, rk = j >> 2;
    for (int s = 1; s < NSEG; ++s) {
      if (wave == 0) {
        if (s >= 5) wait_slow(p.ctrl + C_PROG + xg * 32, (unsigned)(s - 4), p.ctrl + C_TMO, 16u);
        wait_slow(p.ctrl + C_UX + s, 32u, p.ctrl + C_TMO, 15u);
        wait_flags(p.ctrl + C_KQ + s * 32, 32, 1, 1u, lane, p.ctrl + C_TMO, 18u);
      }
      __syncthreads();
      v4u v[12];
#pragma unroll
      for (int i = 0; i < 12; ++i) v[i] = ld_sc1(rux, (s * 72 + xg + 4 * (3 * rk + i / 4)) * 16384 + ((i & 3) * 256 + tid) * 16);
      v4u k6[6];
#pragma unroll
      for (int i = 0; i < 6; ++i) {
        int ch = rk * 6 + i;
        unsigned off = (ch < 32) ? (unsigned)(((ch >> 1) & 1) * SZ_KX + (s * 32 + xg + 4 * (ch >> 2)) * 8192 + (ch & 1) * 4096 + tid * 16) : 0x7ffffff0u;
        k6[i] = ld_sc1(rkq, off);
      }
#pragma unroll
      for (int i = 0; i < 12; ++i) asm volatile("" :: "v"(v[i]));
#pragma unroll
      for (int i = 0; i < 6; ++i) asm volatile("" :: "v"(k6[i]));
    }
  }
}

template <int N> DEV void wait_vm() { asm volatile("s_waitcnt vmcnt(%0)" :: "n"(N) : "memory"); }

struct XDma { const u16* src; char* dst; bool on; };
DEV XDma x_dma_setup(const u16* x, char* smem, int s, int buf, int wave, int lane, bool on) {
  XDma d;
  d.src = x + (size_t)(32 * s + (lane & 15)) * 576 + 8 * (lane >> 4);
  d.dst = smem + L_X + buf * XBUF + wave * 9216;
  d.on = on;
  return d;
}
DEV void x_dma_piece(const XDma& d, int wave, int i) {
  const int f = wave * 9 + i;
  if (d.on) __builtin_amdgcn_global_load_lds((const unsigned*)(d.src + (f & 1) * 16 * 576 + (f >> 1) * 32), (LAS unsigned*)(d.dst + i * 1024), 16, 0, 0);
}
template <int DEPTH, class F, class G>
DEV void xloop(const char* xb, F body, G side) {
  v4u f0[DEPTH + 1], f1[DEPTH + 1];
#pragma unroll
  for (int i = 0; i < DEPTH; ++i) { f0[i] = *(const v4u*)(xb + i * 2048); f1[i] = *(const v4u*)(xb + i * 2048 + 1024); }
#pragma unroll
  for (int ks = 0; ks < 18; ++ks) {
    if (ks + DEPTH < 18) {
      f0[(ks + DEPTH) % (DEPTH + 1)] = *(const v4u*)(xb + (ks + DEPTH) * 2048);
      f1[(ks + DEPTH) % (DEPTH + 1)] = *(const v4u*)(xb + (ks + DEPTH) * 2048 + 1024);
    }
    __builtin_amdgcn_sched_barrier(0);
    body(ks, f0[ks % (DEPTH + 1)], f1[ks % (DEPTH + 1)]);
    __builtin_amdgcn_sched_barrier(0);
    side(ks);
  }
}
template <int NST, class FC, class FS>
DEV void stream(const u16* x, char* smem, int s0, int st, int wave, int lane, int tid, FC compute, FS signal) {
  const int n = (NSEG - s0 + st - 1) / st;
  {
    XDma d0 = x_dma_setup(x, smem, s0, 0, wave, lane, true), d1 = x_dma_setup(x, smem, min(s0 + st, NSEG - 1), 1, wave, lane, n > 1);
#pragma unroll
    for (int i = 0; i < 9; ++i) x_dma_piece(d0, wave, i);
#pragma unroll
    for (int i = 0; i < 9; ++i) x_dma_piece(d1, wave, i);
  }
  int buf = 0;
  for (int i = 0; i < n; ++i) {
    const int s = s0 + i * st;
    if (i < 2 || i + 1 >= n) wait_vm<0>(); else wait_vm<9 + NST>();
    RAW_BARRIER();
    if (tid == 0) {
      if (i == 1) signal(s - st);
      else if (i >= 3) signal(s - 2 * st);
      if (i >= 2 && i + 1 >= n) signal(s - st);
    }
    XDma d = x_dma_setup(x, smem, min(s + 2 * st, NSEG - 1), buf == 0 ? 2 : buf - 1, wave, lane, i + 2 < n);
    compute(s, smem + L_X + buf * XBUF + lane * 16, d);
    buf = buf == 2 ? 0 : buf + 1;
  }
  wait_vm<0>();
  RAW_BARRIER();
  if (tid == 0) signal(s0 + (n - 1) * st);
}
template <int NTW, class F>
DEV void load_kfrags(char* smem, int wave, int lane, int tid, F srcf, v4u (&w)[NTW][18]) {
  constexpr int CH = NTW * 8;
  constexpr int RS = CH * 16 + 16;
  constexpr int PER = 64 * CH / 256;
  const int g = lane >> 4, q4 = (lane & 15) >> 2, p4 = lane & 3;
  v4u st[3][PER];
  auto ld = [&](int t) {
#pragma unroll
    for (int i = 0; i < PER; ++i) { int ch = tid + 256 * i; st[t % 3][i] = srcf(64 * t + ch / CH, ch % CH); }
  };
  ld(0); ld(1); ld(2);
#pragma unroll
  for (int t = 0; t < 9; ++t) {
    __syncthreads();
#pragma unroll
    for (int i = 0; i < PER; ++i) { int ch = tid + 256 * i; *(v4u*)(smem + L_WST + (ch / CH) * RS + (ch % CH) * 16) = st[t % 3][i]; }
    if (t + 3 < 9) ld(t + 3);
    __syncthreads();
#pragma unroll
    for (int tl = 0; tl < NTW; ++tl)
#pragma unroll
      for (int kk = 0; kk < 2; ++kk) {
        int a0 = L_WST + (32 * kk + 8 * g + q4) * RS + ((wave * NTW + tl) * 16 + 4 * p4) * 2;
        v2u x0 = tr_read(smem, a0), x1 = tr_read(smem, a0 + 4 * RS);
        w[tl][2 * t + kk] = (v4u){x0.x, x0.y, x1.x, x1.y};
      }
  }
  __syncthreads();
}

DEV void kq_worker(const Params& p, char* smem, int h, int par, int wave, int lane, int tid) {
  v4u w[4][18];
  load_kfrags<4>(smem, wave, lane, tid, [&](int row, int cc) {
    return *(const v4u*)((cc < 16 ? p.Wk : p.Wq) + (size_t)row * 4096 + h * 128 + (cc & 15) * 8); }, w);
  __amdgpu_buffer_rsrc_t ro = mkrsrc(p.data + (wave < 2 ? OFF_KX : OFF_QX), SZ_KX);
  stream<4>(p.x, smem, par, 2, wave, lane, tid,
    [&](int s, const char* xb, const XDma& dm) {
      f32x4 acc[2][4];
#pragma unroll
      for (int a = 0; a < 2; ++a)
#pragma unroll
        for (int b = 0; b < 4; ++b) acc[a][b] = (f32x4){0, 0, 0, 0};
      xloop<2>(xb, [&](int ks, v4u b0, v4u b1) {
#pragma unroll
        for (int tl = 0; tl < 4; ++tl) { acc[0][tl] = mfma16(w[tl][ks], b0, acc[0][tl]); acc[1][tl] = mfma16(w[tl][ks], b1, acc[1][tl]); }
      }, [&](int ks) { if (!(ks & 1)) x_dma_piece(dm, wave, ks >> 1); });
#pragma unroll
      for (int tt = 0; tt < 2; ++tt)
#pragma unroll
        for (int uu = 0; uu < 2; ++uu) {
          f32x4 a = acc[tt][2 * uu], b = acc[tt][2 * uu + 1];
          v4u o = {cvtpk(a[0], a[1]), cvtpk(a[2], a[3]), cvtpk(b[0], b[1]), cvtpk(b[2], b[3])};
          st_sc1(o, ro, (s * 32 + h) * 8192 + (tt * 4 + (wave & 1) * 2 + uu) * 1024 + lane * 16);
        }
      DSTAMP(h == 0 && wave == 1, 256 + s); DVAL(h == 0 && wave == 1, 320 + s, (unsigned)clk());
    },
    [&](int s) { astore(p.ctrl + C_KQ + s * 32 + h, 1u); });
}

DEV void vu_worker(const Params& p, char* smem, int h, int par, int wave, int lane, int tid) {
  const int g = lane >> 4;
  v4u wo[9][4];
  ABlk b0, b1;
  wv_block_load(p, h, 6 + par, tid, b0); wv_block_load(p, h, 8, tid, b1);
  load_wo(p, smem, h, wave, lane, tid, wo);
  DSTAMP(h == 0 && par == 0 && wave == 0, 8);
  wvo_unit<0, 9>(p, smem, h, 6 + par, 0, wave, lane, tid, b0, wo);
  if (par == 0) wvo_unit<0, 5>(p, smem, h, 8, 1, wave, lane, tid, b1, wo);
  else wvo_unit<5, 9>(p, smem, h, 8, 1, wave, lane, tid, b1, wo);
  wg_signal(p.ctrl + C_WVO, tid);
  DSTAMP(h == 0 && par == 0 && wave == 0, 9);
  v4u wv[2][18];
  load_kfrags<2>(smem, wave, lane, tid, [&](int row, int cc) {
    return *(const v4u*)(p.Wv + (size_t)row * 4096 + h * 128 + cc * 8); }, wv);
  __amdgpu_buffer_rsrc_t ro = mkrsrc(p.data + OFF_UX, SZ_UX);
  stream<9>(p.x, smem, par, 2, wave, lane, tid,
    [&](int s, const char* xb, const XDma& dm) {
      f32x4 acc[2][2];
#pragma unroll
      for (int a = 0; a < 2; ++a)
#pragma unroll
        for (int b = 0; b < 2; ++b) acc[a][b] = (f32x4){0, 0, 0, 0};
      xloop<3>(xb, [&](int ks, v4u b0, v4u b1) {
#pragma unroll
        for (int tl = 0; tl < 2; ++tl) { acc[0][tl] = mfma16(wv[tl][ks], b0, acc[0][tl]); acc[1][tl] = mfma16(wv[tl][ks], b1, acc[1][tl]); }
      }, [&](int ks) { if (!(ks & 1)) x_dma_piece(dm, wave, ks >> 1); });
#pragma unroll
      for (int tt = 0; tt < 2; ++tt) {
        f32x4 a = acc[tt][0], b = acc[tt][1];
        *(v4u*)(smem + L_V + (tt * 4 + wave) * 1024 + lane * 16) = (v4u){cvtpk(a[0], a[1]), cvtpk(a[2], a[3]), cvtpk(b[0], b[1]), cvtpk(b[2], b[3])};
      }
      RAW_BARRIER();
      v4u a[2][4];
#pragma unroll
      for (int tt = 0; tt < 2; ++tt)
#pragma unroll
        for (int u = 0; u < 4; ++u) a[tt][u] = *(const v4u*)(smem + L_V + (tt * 4 + u) * 1024 + lane * 16);
#pragma unroll
      for (int tl = 0; tl < 9; ++tl) {
        f32x4 d0 = {0, 0, 0, 0}, d1 = {0, 0, 0, 0};
#pragma unroll
        for (int u = 0; u < 4; ++u) { d0 = mfma16(a[0][u], wo[tl][u], d0); d1 = mfma16(a[1][u], wo[tl][u], d1); }
        const int n = (wave * 9 + tl) * 16 + (lane & 15), c = n >> 3;
        v4u o = {cvtpk(d0[0], d0[1]), cvtpk(d0[2], d0[3]), cvtpk(d1[0], d1[1]), cvtpk(d1[2], d1[3])};
        st_sc1(o, ro, ((s * 72 + c) * 16 + (h >> 1)) * 1024 + (g * 16 + (h & 1) * 8 + (n & 7)) * 16);
      }
      DSTAMP(h == 0 && wave == 1, 192 + s); DVAL(h == 0 && wave == 1, 384 + s, (unsigned)clk());
    },
    [&](int s) { aadd(p.ctrl + C_UX + s, 1u); });
}

DEV void b1_worker(const Params& p, char* smem, int widx, int wave, int lane, int tid) {
  if (widx >= 64 && widx < 127) {
    const int slab = (widx - 64) / 9, sub = (widx - 64) % 9;
    DSTAMP(widx == 64 && wave == 0, 30);
    v4u w[4][18];
    load_kfrags<4>(smem, wave, lane, tid, [&](int row, int cc) {
      int col = slab * 256 + cc * 8; if (col > 1720) col = 1720;
      return *(const v4u*)(p.wqkv + (size_t)row * 1728 + col); }, w);
    DSTAMP(widx == 64 && wave == 0, 31);
    __amdgpu_buffer_rsrc_t rqk = mkrsrc(p.data + OFF_QKB, (unsigned)(2048 * 1152 * 2));
    __amdgpu_buffer_rsrc_t rvt = mkrsrc(p.data + OFF_VTB, (unsigned)(576 * 2048 * 2));
    const int g = lane >> 4;
    const int col0 = slab * 256 + wave * 64;
    const bool isv = col0 >= 1152;
    stream<4>(p.x, smem, sub, 9, wave, lane, tid,
      [&](int s, const char* xb, const XDma& dm) {
        f32x4 acc[2][4];
#pragma unroll
        for (int a = 0; a < 2; ++a)
#pragma unroll
          for (int b = 0; b < 4; ++b) acc[a][b] = (f32x4){0, 0, 0, 0};
        if (!isv) {
          xloop<2>(xb, [&](int ks, v4u b0, v4u b1) {
#pragma unroll
            for (int tl = 0; tl < 4; ++tl) { acc[0][tl] = mfma16(w[tl][ks], b0, acc[0][tl]); acc[1][tl] = mfma16(w[tl][ks], b1, acc[1][tl]); }
          }, [&](int ks) { if (!(ks & 1)) x_dma_piece(dm, wave, ks >> 1); });
#pragma unroll
          for (int tt = 0; tt < 2; ++tt)
#pragma unroll
            for (int tp = 0; tp < 2; ++tp) {
              f32x4 a = acc[tt][2 * tp], b = acc[tt][2 * tp + 1];
              auto r0 = __builtin_amdgcn_permlane16_swap(cvtpk(a[0], a[1]), cvtpk(b[0], b[1]), false, false);
              auto r1 = __builtin_amdgcn_permlane16_swap(cvtpk(a[2], a[3]), cvtpk(b[2], b[3]), false, false);
              st_sc1((v4u){r0[0], r1[0], r0[1], r1[1]}, rqk,
                     ((s * 32 + tt * 16 + (lane & 15)) * 1152 + col0 + 16 * (2 * tp + (g & 1)) + 8 * (g >> 1)) * 2);
            }
        } else {
          xloop<2>(xb, [&](int ks, v4u b0, v4u b1) {
#pragma unroll
            for (int tl = 0; tl < 4; ++tl) { acc[0][tl] = mfma16(b0, w[tl][ks], acc[0][tl]); acc[1][tl] = mfma16(b1, w[tl][ks], acc[1][tl]); }
          }, [&](int ks) { if (!(ks & 1)) x_dma_piece(dm, wave, ks >> 1); });
#pragma unroll
          for (int tl = 0; tl < 4; ++tl) {
            f32x4 a = acc[0][tl], b = acc[1][tl];
            st_sc1((v4u){cvtpk(a[0], a[1]), cvtpk(a[2], a[3]), cvtpk(b[0], b[1]), cvtpk(b[2], b[3])}, rvt,
                   (((col0 - 1152 + tl * 16 + (lane & 15)) * 64 + s) * 4 + g) * 16);
          }
        }
      },
      [&](int) {});
    DSTAMP(widx == 64 && wave == 0, 32);
  }
  wg_signal(p.ctrl + C_B1, tid);
}

DEV void b2_item(const Params& p, char* smem, int hg, int qb, int wave, int lane, int tid) {
  __amdgpu_buffer_rsrc_t rqk = mkrsrc(p.data + OFF_QKB, (unsigned)(2048 * 1152 * 2));
  __amdgpu_buffer_rsrc_t rvt = mkrsrc(p.data + OFF_VTB, (unsigned)(576 * 2048 * 2));
  __amdgpu_buffer_rsrc_t rob = mkrsrc(p.data + OFF_OB, (unsigned)SZ_OB);
  const int g = lane >> 4, li = lane & 15;
  const int hb = hg * 4 + wave;
  const int q0 = qb * 128, k0 = q0 - 256;
  const float SC = 0.23570226039551584f * 1.4426950408889634f;
  const float NEG = -1e30f;
  const unsigned OOB = 0x7ffffff0u;
  auto rowfrag = [&](int tok, int colbase) {
    unsigned off = (tok >= 0 && g < 3) ? (unsigned)((tok * 1152 + colbase + 8 * g) * 2) : OOB;
    v4u v = ld_sc1(rqk, off);
    if (g == 2) { v.y = 0u; v.z = 0u; v.w = 0u; }
    return v;
  };
  v4u kf[24], vf[12][2], qf[8];
#pragma unroll
  for (int qt = 0; qt < 8; ++qt) qf[qt] = rowfrag(q0 + 16 * qt + li, 18 * hb);
#pragma unroll
  for (int t = 0; t < 24; ++t) kf[t] = rowfrag(k0 + 16 * t + li, 576 + 18 * hb);
#pragma unroll
  for (int b = 0; b < 12; ++b)
#pragma unroll
    for (int dt = 0; dt < 2; ++dt) {
      const int blk = 4 * qb - 8 + b, d = 16 * dt + li;
      v4u v = ld_sc1(rvt, (blk >= 0 && d < 18) ? (unsigned)((((18 * hb + d) * 64 + blk) * 4 + g) * 16) : OOB);
      if (dt == 1 && li == 2) v = (v4u){0x3F803F80u, 0x3F803F80u, 0x3F803F80u, 0x3F803F80u};
      vf[b][dt] = v;
    }
#pragma unroll
  for (int qt = 0; qt < 8; ++qt) {
    f32x4 sc[17];
#pragma unroll
    for (int tau = 0; tau < 17; ++tau) {
      const float c = (k0 + 16 * (qt + tau)) >= 0 ? 0.f : NEG;
      f32x4 ci = {c, c, c, c};
      if (tau == 0) {
#pragma unroll
        for (int r = 0; r < 4; ++r) ci[r] = (4 * g + r > li) ? c : NEG;
      }
      if (tau == 16) {
#pragma unroll
        for (int r = 0; r < 4; ++r) ci[r] = (4 * g + r <= li) ? c : NEG;
      }
      sc[tau] = mfma16(kf[qt + tau], qf[qt], ci);
    }
    float m = fmaxf(fmaxf(sc[0][0], sc[0][1]), fmaxf(sc[0][2], sc[0][3]));
#pragma unroll
    for (int tau = 1; tau < 17; ++tau) m = fmaxf(fmaxf(fmaxf(m, sc[tau][0]), sc[tau][1]), fmaxf(sc[tau][2], sc[tau][3]));
    m = xmax(m);
    const float ms = -m * SC;
    unsigned pk[18][2];
#pragma unroll
    for (int t = 0; t < 17; ++t) {
      float e0 = __builtin_amdgcn_exp2f(fmaf(sc[t][0], SC, ms)), e1 = __builtin_amdgcn_exp2f(fmaf(sc[t][1], SC, ms));
      float e2 = __builtin_amdgcn_exp2f(fmaf(sc[t][2], SC, ms)), e3 = __builtin_amdgcn_exp2f(fmaf(sc[t][3], SC, ms));
      pk[t + (qt & 1)][0] = cvtpk(e0, e1); pk[t + (qt & 1)][1] = cvtpk(e2, e3);
    }
    pk[(qt & 1) ? 0 : 17][0] = 0u; pk[(qt & 1) ? 0 : 17][1] = 0u;
    f32x4 o0 = {0, 0, 0, 0}, o1 = {0, 0, 0, 0};
#pragma unroll
    for (int b = 0; b < 9; ++b) {
      v4u pf = {pk[2 * b][0], pk[2 * b][1], pk[2 * b + 1][0], pk[2 * b + 1][1]};
      o0 = mfma16(vf[(qt >> 1) + b][0], pf, o0);
      o1 = mfma16(vf[(qt >> 1) + b][1], pf, o1);
    }
    const float inv = __builtin_amdgcn_rcpf(__shfl(o1[2], li));
    char* orow = smem + (16 * qt + li) * 144 + wave * 36;
    *(unsigned*)(orow + 8 * g) = cvtpk(o0[0] * inv, o0[1] * inv);
    *(unsigned*)(orow + 8 * g + 4) = cvtpk(o0[2] * inv, o0[3] * inv);
    if (g == 0) *(unsigned*)(orow + 32) = cvtpk(o1[0] * inv, o1[1] * inv);
  }
  __syncthreads();
#pragma unroll
  for (int i = 0; i < 5; ++i) {
    int ch = tid + 256 * i;
    if (ch < 1152) { int row = ch / 9, cc = ch % 9; st_sc1(*(const v4u*)(smem + ch * 16), rob, ((q0 + row) * 576 + hg * 72) * 2 + cc * 16); }
  }
  wg_signal(p.ctrl + C_OB + qb * 32, tid);
}

constexpr int S0 = 36;
DEV void fa_item(const Params& p, char* smem, int s, int j, int widx, int wave, int lane, int tid) {
  __amdgpu_buffer_rsrc_t rpx = mkrsrc(p.data + OFF_PX, SZ_PX);
  __amdgpu_buffer_rsrc_t rux = mkrsrc(p.data + OFF_UX, SZ_UX);
  __amdgpu_buffer_rsrc_t rum = mkrsrc(p.data + OFF_UM, SZ_UM);
  __amdgpu_buffer_rsrc_t rab = mkrsrc(p.data + OFF_AB, (unsigned)SZ_OB);
  __amdgpu_buffer_rsrc_t rg = mkrsrc(p.data + OFF_GATE, SZ_GATE);
  const int g = lane >> 4, li = lane & 15;
  const int nt = j * 4 + wave;
  if (wave == 0) {
    wait_ge(p.ctrl + C_UX + s, 32u, p.ctrl + C_TMO, 19u);
    if (s >= S0) wait_ge(p.ctrl + C_FB + s, 9u, p.ctrl + C_TMO, 20u);
  }
  __syncthreads();
  v4u ux[2][16];
#pragma unroll
  for (int sl = 0; sl < 2; ++sl)
#pragma unroll
    for (int pp = 0; pp < 16; ++pp) ux[sl][pp] = ld_sc1(rux, ((s * 72 + nt * 2 + sl) * 16 + pp) * 1024 + lane * 16);
  v2u bp[2] = {{0u, 0u}, {0u, 0u}};
  float gt[2] = {1.f, 1.f};
  if (s >= S0) {
#pragma unroll
    for (int qt = 0; qt < 2; ++qt) {
      bp[qt] = ld2_sc1(rab, ((s * 32 + qt * 16 + li) * 576 + nt * 16 + 4 * g) * 2);
      gt[qt] = __uint_as_float(ld1_sc1(rg, (s * 32 + qt * 16 + li) * 4));
    }
  }
  if (wave == 1) {
    wait_slow(p.ctrl + C_PROG + (4 + widx % 12) * 32, (unsigned)s + 2u, p.ctrl + C_TMO, 17u);
    wait_flags(p.ctrl + C_PX, 32, 32, (unsigned)s + 1u, lane, p.ctrl + C_TMO, 11u);
    wait_flags(p.ctrl + C_UM, NC, 32, (unsigned)s + 1u, lane, p.ctrl + C_TMO, 12u);
  }
  __syncthreads();
  v4u um[2][4];
  {
    v4u v[20];
#pragma unroll
    for (int i = 0; i < 20; ++i) v[i] = ld_sc1(rpx, s * 81920 + (i * 256 + tid) * 16);
#pragma unroll
    for (int sl = 0; sl < 2; ++sl)
#pragma unroll
      for (int mm = 0; mm < 4; ++mm) um[sl][mm] = ld_sc1(rum, ((s * 72 + nt * 2 + sl) * 4 + mm) * 1024 + lane * 16);
#pragma unroll
    for (int i = 0; i < 20; ++i) *(v4u*)(smem + (i * 256 + tid) * 16) = v[i];
  }
  __syncthreads();
#pragma unroll
  for (int qt = 0; qt < 2; ++qt) {
    f32x4 aA[2] = {{0, 0, 0, 0}, {0, 0, 0, 0}}, aB[2] = {{0, 0, 0, 0}, {0, 0, 0, 0}};
    v4u pe[3], po[3];
    auto ldp = [&](int i) {
      const char* b = (i < 16) ? smem + (qt * 32 + 2 * i) * 1024 + lane * 16 : smem + 65536 + (qt * 8 + 2 * (i - 16)) * 1024 + lane * 16;
      pe[i % 3] = *(const v4u*)b; po[i % 3] = *(const v4u*)(b + 1024);
    };
    ldp(0); ldp(1);
#pragma unroll
    for (int i = 0; i < 20; ++i) {
      if (i + 2 < 20) ldp(i + 2);
      __builtin_amdgcn_sched_barrier(0);
#pragma unroll
      for (int sl = 0; sl < 2; ++sl) {
        v4u u = (i < 16) ? ux[sl][i & 15] : um[sl][i & 3];
        aA[sl] = mfma16(u, pe[i % 3], aA[sl]); aB[sl] = mfma16(u, po[i % 3], aB[sl]);
      }
      __builtin_amdgcn_sched_barrier(0);
    }
    const int tok = s * 32 + qt * 16 + li;
    float o[4];
#pragma unroll
    for (int r = 0; r < 4; ++r) {
      auto sw = __builtin_amdgcn_permlane32_swap(__float_as_uint(aB[0][r]), __float_as_uint(aA[1][r]), false, false);
      float own = (lane < 32) ? aA[0][r] : aB[1][r];
      float oth = (lane < 32) ? __uint_as_float(sw[1]) : __uint_as_float(sw[0]);
      unsigned bw = (r < 2) ? bp[qt].x : bp[qt].y;
      o[r] = gt[qt] * (own + oth) + bf2f((r & 1) ? (bw >> 16) : (bw & 0xffffu));
    }
    v2u ov = {cvtpk(o[0], o[1]), cvtpk(o[2], o[3])};
    if (s >= S0) *(v2u*)(p.out + (size_t)tok * 576 + nt * 16 + 4 * g) = ov;
    else st2_sc1(ov, rab, (tok * 576 + nt * 16 + 4 * g) * 2);
  }
  if (s < S0) wg_signal(p.ctrl + C_FA + s, tid); else __syncthreads();
}

DEV void fb_stage(const Params& p, char* smem, int widx, int wave, int lane, int tid) {
  if ((widx >> 2) >= 27) return;
  __amdgpu_buffer_rsrc_t rob = mkrsrc(p.data + OFF_OB, (unsigned)SZ_OB);
  __amdgpu_buffer_rsrc_t rab = mkrsrc(p.data + OFF_AB, (unsigned)SZ_OB);
  __amdgpu_buffer_rsrc_t rg = mkrsrc(p.data + OFF_GATE, SZ_GATE);
  const int g = lane >> 4, li = lane & 15;
  const int j = (widx >> 2) % 9, grp = ((widx >> 2) / 9) * 4 + (widx & 3);
  const int nt = j * 4 + wave;
  v4u wf[1][18];
  load_kfrags<1>(smem, wave, lane, tid, [&](int row, int cc) { return *(const v4u*)(p.wo + (size_t)row * 576 + j * 64 + cc * 8); }, wf);
  v4u gv[9];
#pragma unroll
  for (int i = 0; i < 9; ++i) gv[i] = *(const v4u*)(p.gw + (i * 8 + (lane & 7)) * 8);
  for (int it = 0; it < 6; ++it) {
    const int s = grp + 12 * ((it + 3) % 6);
    if (s >= NSEG) continue;
    if (wave == 0) {
      wait_ge(p.ctrl + C_OB + (s >> 2) * 32, 8u, p.ctrl + C_TMO, 10u);
      if (s < S0) wait_ge(p.ctrl + C_FA + s, 9u, p.ctrl + C_TMO, 21u);
    }
    __syncthreads();
    v4u v[9], xv[9];
#pragma unroll
    for (int i = 0; i < 9; ++i) { int f = wave * 9 + i; v[i] = ld_sc1(rob, ((s * 32 + (f & 1) * 16 + li) * 576 + 32 * (f >> 1) + 8 * g) * 2); }
    const int gtok = s * 32 + wave * 8 + (lane >> 3);
#pragma unroll
    for (int i = 0; i < 9; ++i) xv[i] = *(const v4u*)(p.x + (size_t)gtok * 576 + (i * 8 + (lane & 7)) * 8);
    v2u ap[2] = {{0u, 0u}, {0u, 0u}};
    if (s < S0) {
#pragma unroll
      for (int qt = 0; qt < 2; ++qt) ap[qt] = ld2_sc1(rab, ((s * 32 + qt * 16 + li) * 576 + nt * 16 + 4 * g) * 2);
    }
#pragma unroll
    for (int i = 0; i < 9; ++i) *(v4u*)(smem + (wave * 9 + i) * 1024 + lane * 16) = v[i];
    {
      float a = 0;
#pragma unroll
      for (int i = 0; i < 9; ++i)
#pragma unroll
        for (int e = 0; e < 4; ++e) {
          a += bf2f(xv[i][e] & 0xffffu) * bf2f(gv[i][e] & 0xffffu);
          a += bf2f(xv[i][e] >> 16) * bf2f(gv[i][e] >> 16);
        }
      a += __shfl_xor(a, 1); a += __shfl_xor(a, 2); a += __shfl_xor(a, 4);
      const float gs = 1.f / (1.f + __expf(-a));
      if ((lane & 7) == 0) {
        *(float*)(smem + 40960 + (wave * 8 + (lane >> 3)) * 4) = gs;
        if (j == 0 && s >= S0) st1_sc1(__float_as_uint(gs), rg, gtok * 4);
      }
    }
    __syncthreads();
    f32x4 bacc[2] = {{0, 0, 0, 0}, {0, 0, 0, 0}};
    xloop<3>(smem + lane * 16, [&](int ks, v4u b0, v4u b1) { bacc[0] = mfma16(wf[0][ks], b0, bacc[0]); bacc[1] = mfma16(wf[0][ks], b1, bacc[1]); }, [&](int) {});
#pragma unroll
    for (int qt = 0; qt < 2; ++qt) {
      const float gt = *(const float*)(smem + 40960 + (qt * 16 + li) * 4);
      const int tok = s * 32 + qt * 16 + li;
      float o[4];
#pragma unroll
      for (int r = 0; r < 4; ++r) {
        unsigned aw = (r < 2) ? ap[qt].x : ap[qt].y;
        o[r] = (1.f - gt) * bacc[qt][r] + gt * bf2f((r & 1) ? (aw >> 16) : (aw & 0xffffu));
      }
      v2u ov = {cvtpk(o[0], o[1]), cvtpk(o[2], o[3])};
      if (s < S0) *(v2u*)(p.out + (size_t)tok * 576 + nt * 16 + 4 * g) = ov;
      else st2_sc1(ov, rab, (tok * 576 + nt * 16 + 4 * g) * 2);
    }
    if (s >= S0) wg_signal(p.ctrl + C_FB + s, tid); else __syncthreads();
  }
}

template <int STAGE>
DEV void fa_loop(const Params& p, char* smem, int widx, int wave, int lane, int tid) {
  LAS int* cmd = (LAS int*)(smem + 163824);
  for (unsigned spins = 0;; ++spins) {
    if (tid == 0) {
      int c = -1;
      if (STAGE == 2) {
        unsigned t = __hip_atomic_fetch_add(p.ctrl + C_FQ + 32, 1u, __ATOMIC_RELAXED, __HIP_MEMORY_SCOPE_AGENT);
        c = t < (unsigned)((NSEG - S0) * 9) ? (int)t + S0 * 9 : -4;
      } else {
        const unsigned head = aload(p.ctrl + C_FQ);
        if (STAGE == 0 && aload(p.ctrl + C_B1) >= 64u) c = -4;
        else if (STAGE == 1 && head >= (unsigned)(S0 * 9)) c = -4;
        else if (head < (unsigned)(S0 * 9) && aload(p.ctrl + C_PROG + (4 + widx % 12) * 32) >= head / 9u + 2u) {
          unsigned t = __hip_atomic_fetch_add(p.ctrl + C_FQ, 1u, __ATOMIC_RELAXED, __HIP_MEMORY_SCOPE_AGENT);
          c = t < (unsigned)(S0 * 9) ? (int)t : -5;
        }
      }
      if (spins > SPIN_CAP || ((spins & 63u) == 63u && aload(p.ctrl + C_TMO))) { astore(p.ctrl + C_TMO, 22u); c = -4; }
      *cmd = c;
    }
    __syncthreads();
    const int c = __builtin_amdgcn_readfirstlane(*(volatile LAS int*)cmd);
    __builtin_amdgcn_fence(__ATOMIC_ACQUIRE, "wavefront");
    __syncthreads();
    if (c == -4) break;
    if (c == -1) { __builtin_amdgcn_s_sleep(16); continue; }
    if (c == -5) continue;
    fa_item(p, smem, c / 9, c % 9, widx, wave, lane, tid);
    DSTAMP(wave == 0 && (c % 9) == 0, 448 + c / 9);
  }
}

DEV void worker_role(const Params& p, char* smem, int widx, int wave, int lane, int tid) {
  const int slab = ((widx >> 3) << 2) | (widx & 3), par = (widx >> 2) & 1;
  if (slab < 32) vu_worker(p, smem, slab, par, wave, lane, tid);
  else kq_worker(p, smem, slab - 32, par, wave, lane, tid);
  __syncthreads();
  DSTAMP(widx == 0 && wave == 0, 10);
  if (widx >= 64) b1_worker(p, smem, widx, wave, lane, tid);
  fa_loop<0>(p, smem, widx, wave, lane, tid);
  DSTAMP(widx == 0 && wave == 0, 12);
  b2_item(p, smem, widx & 7, widx >> 3, wave, lane, tid);
  DSTAMP(widx == 0 && wave == 0, 13);
  fa_loop<1>(p, smem, widx, wave, lane, tid);
  DSTAMP(widx == 0 && wave == 0, 14);
  fb_stage(p, smem, widx, wave, lane, tid);
  DSTAMP(widx == 0 && wave == 0, 15);
  fa_loop<2>(p, smem, widx, wave, lane, tid);
}

__global__ void __launch_bounds__(256, 1) fused_kernel(Params p) {
  extern __shared__ __attribute__((aligned(16))) char smem[];
  const int tid = threadIdx.x, lane = tid & 63, wave = __builtin_amdgcn_readfirstlane(tid >> 6);
  const int b = blockIdx.x;
  const int idx = (b >> 3) * 4 + (b & 3);
  DSTAMP(wave == 0, 1024 + b);
  if ((b & 7) < 4) {
    if (idx < NH) head_role(p, smem, idx, wave, lane, tid);
    else if (idx < NH + NC) col_role(p, smem, idx - NH, wave, lane, tid);
    else spare_role(p, smem, idx - NH - NC, wave, lane, tid);
  } else {
    worker_role(p, smem, idx, wave, lane, tid);
  }
  DSTAMP(wave == 0, 1280 + b);
}

__global__ void __launch_bounds__(256) prep_kernel(Params p) {
  __shared__ unsigned sm[8];
  const int tid = threadIdx.x, lane = tid & 63, wave = __builtin_amdgcn_readfirstlane(tid >> 6), b = blockIdx.x;
  if (b >= 8) {
    ((v4u*)p.ctrl)[(b - 8) * 256 + tid] = (v4u){0u, 0u, 0u, 0u};
    return;
  }
  __amdgpu_buffer_rsrc_t rm = mkrsrc(p.mbox, MBOX_BYTES);
  unsigned acc = 0, mn0 = 1u << 30, mn1 = 1u << 30;
  const unsigned off = b * (2u << 20) + (1u << 20) + wave * 16384;
  acc += ld1_sc1(rm, off + 128);
  drain();
  asm volatile("" :: "v"(acc));
#pragma unroll 1
  for (int q = 0; q < 6; ++q) {
    unsigned long long t0 = clk();
    acc += ld1_sc1(rm, off + (q & 1) * 8192 + 512 + (q >> 1) * 256);
    drain();
    asm volatile("" :: "v"(acc));
    unsigned d = (unsigned)(clk() - t0);
    if (q & 1) mn1 = min(mn1, d); else mn0 = min(mn0, d);
  }
  if (lane == 0) { sm[wave * 2] = mn0; sm[wave * 2 + 1] = mn1; }
  __syncthreads();
  if (tid == 0) {
    unsigned a0 = min(min(sm[0], sm[2]), min(sm[4], sm[6])), a1 = min(min(sm[1], sm[3]), min(sm[5], sm[7]));
    const unsigned grp = (__builtin_amdgcn_s_getreg(20 | (3 << 11)) >> 2) & 1u;
    p.ctrl[C_CAL + b] = (a1 < a0 ? 1u : 0u) | (grp << 1);
  }
}

}

extern "C" void kernel_launch(void* const* d_in, const int* in_sizes, int n_in, void* d_out, int out_size, void* d_ws,
                              size_t ws_size, hipStream_t stream) {
  (void)in_sizes; (void)n_in; (void)out_size; (void)ws_size;
  Params p;
  p.x = (const u16*)d_in[0]; p.mem0 = (const u16*)d_in[1];
  p.Wq = (const u16*)d_in[2]; p.Wk = (const u16*)d_in[3]; p.Wv = (const u16*)d_in[4]; p.Wo = (const u16*)d_in[5];
  p.wqkv = (const u16*)d_in[8]; p.wo = (const u16*)d_in[9]; p.gw = (const u16*)d_in[10];
  p.out = (u16*)d_out;
  char* ws = (char*)d_ws;
  p.ctrl = (unsigned*)ws;
  size_t a = ((size_t)ws + CTRL_BYTES + (2u << 20) - 1) & ~(size_t)((2u << 20) - 1);
  p.mbox = (char*)a;
  p.data = p.mbox + MBOX_BYTES;
  hipFuncSetAttribute((const void*)fused_kernel, hipFuncAttributeMaxDynamicSharedMemorySize, LDS_BYTES);
  prep_kernel<<<32, 256, 0, stream>>>(p);
  fused_kernel<<<256, 256, LDS_BYTES, stream>>>(p);
}
```

```cpp
#include <hip/hip_runtime.h>
#include <hip/hip_bf16.h>

typedef unsigned short u16;
typedef unsigned v4u __attribute__((ext_vector_type(4)));
typedef unsigned v2u __attribute__((ext_vector_type(2)));
typedef unsigned v8u __attribute__((ext_vector_type(8)));
typedef __attribute__((ext_vector_type(8))) __bf16 bf16x8;
typedef __attribute__((ext_vector_type(16))) __bf16 bf16x16;
typedef __attribute__((ext_vector_type(4))) float f32x4;
typedef __attribute__((ext_vector_type(4))) short s16x4;
#define LAS __attribute__((address_space(3)))
#define DEV __device__ __forceinline__

namespace {

constexpr int NSEG = 64;
constexpr int NH = 32, NC = 72, NSPARE = 24, NWORK = 128;
constexpr unsigned SENT = 0xFFFFFFFFu;
constexpr unsigned SPIN_CAP = 1u << 22;
constexpr int NREP = 4;
constexpr int STEP_SPAN = NREP * 5 * 16384;
constexpr int NPAGES = 11;
constexpr unsigned MBOX_BYTES = NPAGES * (2u << 20);

constexpr size_t CTRL_BYTES = 98304 + 4096;
constexpr size_t OFF_WVO = 0;
constexpr size_t SZ_WVO = (size_t)72 * 294912;
constexpr size_t OFF_KX = OFF_WVO + SZ_WVO;
constexpr size_t SZ_KX = (size_t)64 * 32 * 8192;
constexpr size_t OFF_QX = OFF_KX + SZ_KX;
constexpr size_t OFF_UX = OFF_QX + SZ_KX;
constexpr size_t SZ_UX = (size_t)64 * 72 * 16384;
constexpr size_t OFF_PX = OFF_UX + SZ_UX;
constexpr size_t SZ_PX = (size_t)64 * 81920;
constexpr size_t OFF_UM = OFF_PX + SZ_PX;
constexpr size_t SZ_UM = (size_t)64 * 72 * 4096;
constexpr size_t OFF_QKB = OFF_UM + SZ_UM;
constexpr size_t SZ_QKB = (size_t)2048 * 1152 * 2 + 4096;
constexpr size_t OFF_VTB = OFF_QKB + SZ_QKB;
constexpr size_t SZ_VTB = (size_t)608 * 2048 * 2;
constexpr size_t OFF_OB = OFF_VTB + SZ_VTB;
constexpr size_t SZ_OB = (size_t)2048 * 576 * 2;
constexpr size_t OFF_AB = OFF_OB + SZ_OB;
constexpr size_t OFF_GATE = OFF_AB + SZ_OB;
constexpr size_t SZ_GATE = 8192;
constexpr size_t DATA_BYTES = OFF_GATE + SZ_GATE;

constexpr int C_TMO = 0;
constexpr int C_POL = 64;
constexpr int C_CLR = 128;
constexpr int C_WVO = 192;
constexpr int C_AUX = 256;
constexpr int C_B1 = 320;
constexpr int C_FQ = 384;
constexpr int C_FA = 1152;
constexpr int C_FB = 1280;
constexpr int C_PROG = 512;
constexpr int C_UX = 1024;
constexpr int C_OB = 2048;
constexpr int C_KQ = 4096;
constexpr int C_PX = 8192;
constexpr int C_DBG = 12288;
constexpr int C_UM = 16384;
constexpr int C_CAL = 24576;

struct Params {
  const u16 *x, *mem0, *Wq, *Wk, *Wv, *Wo, *wqkv, *wo, *gw;
  u16* out;
  unsigned* ctrl;
  char* mbox;
  char* data;
};

template <int N> struct IC { static constexpr int value = N; };
DEV unsigned umin(unsigned a, unsigned b) { return a < b ? a : b; }
DEV unsigned umax(unsigned a, unsigned b) { return a > b ? a : b; }
DEV float bf2f(unsigned v) { return __builtin_bit_cast(float, v << 16); }
typedef __attribute__((ext_vector_type(2))) float f32x2;
typedef __attribute__((ext_vector_type(2))) __bf16 bf16x2;
DEV unsigned cvtpk(float lo, float hi) { return __builtin_bit_cast(unsigned, __builtin_convertvector((f32x2){lo, hi}, bf16x2)); }
DEV unsigned long long clk() { unsigned long long t; asm volatile("s_memtime %0\n\ts_waitcnt lgkmcnt(0)" : "=s"(t) :: "memory"); return t; }
DEV unsigned rt() { unsigned long long t; asm volatile("s_memrealtime %0\n\ts_waitcnt lgkmcnt(0)" : "=s"(t) :: "memory"); return (unsigned)t; }
DEV unsigned vo_mem(int rep, unsigned o) { return (unsigned)(rep * 5 + (o >> 13)) * 16384u + (o & 8191u); }
DEV unsigned vo_p(int rep, unsigned o) { return (unsigned)(rep * 5 + 2 + (o >> 13)) * 16384u + (o & 8191u); }
DEV unsigned sbase(int s, unsigned pol) { int pg = s / 6; return (unsigned)pg * (2u << 20) + (unsigned)(s - 6 * pg) * STEP_SPAN + (((pol >> pg) & 1u) << 13); }
DEV __amdgpu_buffer_rsrc_t mkrsrc(const void* p, unsigned bytes) { return __builtin_amdgcn_make_buffer_rsrc((void*)p, 0, (int)bytes, 0x00020000); }
DEV v4u ld_sc1(__amdgpu_buffer_rsrc_t r, unsigned off) { return __builtin_amdgcn_raw_buffer_load_b128(r, off, 0, 16); }
DEV v2u ld2_sc1(__amdgpu_buffer_rsrc_t r, unsigned off) { return __builtin_amdgcn_raw_buffer_load_b64(r, off, 0, 16); }
DEV unsigned ld1_sc1(__amdgpu_buffer_rsrc_t r, unsigned off) { return __builtin_amdgcn_raw_buffer_load_b32(r, off, 0, 16); }
DEV void st_sc1(v4u v, __amdgpu_buffer_rsrc_t r, unsigned off) { __builtin_amdgcn_raw_buffer_store_b128(v, r, off, 0, 16); }
DEV void st2_sc1(v2u v, __amdgpu_buffer_rsrc_t r, unsigned off) { __builtin_amdgcn_raw_buffer_store_b64(v, r, off, 0, 16); }
DEV void st1_sc1(unsigned v, __amdgpu_buffer_rsrc_t r, unsigned off) { __builtin_amdgcn_raw_buffer_store_b32(v, r, off, 0, 16); }
DEV v4u ld_mb(__amdgpu_buffer_rsrc_t r, unsigned vo, unsigned so) { return __builtin_amdgcn_raw_buffer_load_b128(r, vo, so, 16); }
DEV v2u ld2_mb(__amdgpu_buffer_rsrc_t r, unsigned vo, unsigned so) { return __builtin_amdgcn_raw_buffer_load_b64(r, vo, so, 16); }
DEV void st_mb(v4u v, __amdgpu_buffer_rsrc_t r, unsigned vo, unsigned so) { __builtin_amdgcn_raw_buffer_store_b128(v, r, vo, so, 16); }
DEV void drain() { asm volatile("s_waitcnt vmcnt(0)" ::: "memory"); }
DEV unsigned max4(v4u v) { return umax(umax(v.x, v.y), umax(v.z, v.w)); }
DEV v2u tr_read(const char* smem, unsigned off) {
  s16x4 v = __builtin_amdgcn_ds_read_tr16_b64_v4i16((LAS s16x4*)(smem + off));
  return __builtin_bit_cast(v2u, v);
}
DEV v4u trfrag(const char* smem, int off) {
  v2u x0 = tr_read(smem, off), x1 = tr_read(smem, off + 512);
  return (v4u){x0.x, x0.y, x1.x, x1.y};
}
DEV f32x4 mfma16(v4u a, v4u b, f32x4 c) {
  return __builtin_amdgcn_mfma_f32_16x16x32_bf16(__builtin_bit_cast(bf16x8, a), __builtin_bit_cast(bf16x8, b), c, 0, 0, 0);
}
DEV f32x4 smfma(v4u a, v8u b, f32x4 c, int idx) {
  return __builtin_amdgcn_smfmac_f32_16x16x64_bf16(__builtin_bit_cast(bf16x8, a), __builtin_bit_cast(bf16x16, b), c, idx, 0, 0);
}
template <int CTRL> DEV float dpp(float x) {
  return __builtin_bit_cast(float, __builtin_amdgcn_mov_dpp(__builtin_bit_cast(int, x), CTRL, 0xf, 0xf, true));
}
constexpr int ROR8 = 0x128;
DEV float amax(float a, float b) { float r; asm("v_max_f32 %0, %1, %2\n\ts_nop 1" : "=v"(r) : "v"(a), "v"(b)); return r; }
DEV float amax3(float a, float b, float c) { return fmaxf(fmaxf(a, b), c); }
DEV float xmax(float x) {
  auto s = __builtin_amdgcn_permlane16_swap(__float_as_uint(x), __float_as_uint(x), false, false);
  x = amax(__uint_as_float(s[0]), __uint_as_float(s[1]));
  auto t = __builtin_amdgcn_permlane32_swap(__float_as_uint(x), __float_as_uint(x), false, false);
  return amax(__uint_as_float(t[0]), __uint_as_float(t[1]));
}
DEV float xmax8(float x) {
  float t;
  asm("s_nop 1\n\tv_max_f32_dpp %0, %0, %0 row_ror:8 row_mask:0xf bank_mask:0xf\n\tv_mov_b32 %1, %0\n\ts_nop 1\n\t"
      "v_permlane16_swap_b32 %0, %1\n\tv_max_f32 %0, %0, %1\n\tv_mov_b32 %1, %0\n\ts_nop 1\n\t"
      "v_permlane32_swap_b32 %0, %1\n\tv_max_f32 %0, %0, %1" : "+v"(x), "=&v"(t));
  return x;
}
DEV float xsum(float x) {
  auto s = __builtin_amdgcn_permlane16_swap(__float_as_uint(x), __float_as_uint(x), false, false);
  x = __uint_as_float(s[0]) + __uint_as_float(s[1]);
  auto t = __builtin_amdgcn_permlane32_swap(__float_as_uint(x), __float_as_uint(x), false, false);
  return __uint_as_float(t[0]) + __uint_as_float(t[1]);
}
DEV unsigned aload(unsigned* p) { return __hip_atomic_load(p, __ATOMIC_RELAXED, __HIP_MEMORY_SCOPE_AGENT); }
DEV void aadd(unsigned* p, unsigned v) { __hip_atomic_fetch_add(p, v, __ATOMIC_RELAXED, __HIP_MEMORY_SCOPE_AGENT); }
DEV void astore(unsigned* p, unsigned v) { __hip_atomic_store(p, v, __ATOMIC_RELAXED, __HIP_MEMORY_SCOPE_AGENT); }

DEV bool wait_ge(unsigned* w, unsigned target, unsigned* tmo, unsigned code) {
  for (unsigned spins = 0;; ++spins) {
    if ((unsigned)__builtin_amdgcn_readfirstlane(aload(w)) >= target) { __builtin_amdgcn_fence(__ATOMIC_ACQUIRE, "wavefront"); return true; }
    __builtin_amdgcn_s_sleep(2);
    if (spins > SPIN_CAP) { astore(tmo, code); return false; }
    if ((spins & 255u) == 255u && __builtin_amdgcn_readfirstlane(aload(tmo))) return false;
  }
}
DEV bool wait_slow(unsigned* w, unsigned target, unsigned* tmo, unsigned code) {
  for (unsigned spins = 0;; ++spins) {
    if ((unsigned)__builtin_amdgcn_readfirstlane(aload(w)) >= target) { __builtin_amdgcn_fence(__ATOMIC_ACQUIRE, "wavefront"); return true; }
    __builtin_amdgcn_s_sleep(12);
    if (spins > SPIN_CAP) { astore(tmo, code); return false; }
    if ((spins & 63u) == 63u && __builtin_amdgcn_readfirstlane(aload(tmo))) return false;
  }
}
DEV bool wait_flags(unsigned* w, int n, int st, unsigned target, int lane, unsigned* tmo, unsigned code) {
  for (unsigned spins = 0;; ++spins) {
    unsigned a = aload(w + min(lane, n - 1) * st), b = aload(w + min(lane + 64, n - 1) * st);
    if (__builtin_amdgcn_ballot_w64(a < target || b < target) == 0) { __builtin_amdgcn_fence(__ATOMIC_ACQUIRE, "wavefront"); return true; }
    __builtin_amdgcn_s_sleep(6);
    if (spins > SPIN_CAP) { astore(tmo, code); return false; }
    if ((spins & 63u) == 63u && __builtin_amdgcn_readfirstlane(aload(tmo))) return false;
  }
}
DEV void wg_wait_ge(unsigned* w, unsigned target, unsigned* tmo, unsigned code, int wave) {
  if (wave == 0) wait_ge(w, target, tmo, code);
  __syncthreads();
}
DEV void wg_signal(unsigned* w, int tid) {
  drain();
  __syncthreads();
  if (tid == 0) aadd(w, 1u);
}
DEV void wait_until(unsigned t) {
  for (int guard = 0; guard < 64; ++guard) {
    int rem = (int)(t - rt());
    if (rem <= 0 || rem > 1000) return;
    if (rem <= 16) { int n = (rem * 24) >> 6; for (int i = 0; i < n; ++i) __builtin_amdgcn_s_sleep(1); return; }
    int n = ((rem - 12) * 24) >> 6; if (n > 16) n = 16;
    for (int i = 0; i < n; ++i) __builtin_amdgcn_s_sleep(1);
  }
}

#define DSTAMP(cond, slot)
#define DVAL(cond, slot, v)
#define DPT(k, ref)
#define DPDUMP(slot)
#define RAW_BARRIER() do { asm volatile("s_waitcnt lgkmcnt(0)" ::: "memory"); __builtin_amdgcn_s_barrier(); asm volatile("" ::: "memory"); } while (0)
constexpr int L_MEM = 0;
constexpr int L_KQ = 10240;
constexpr int L_SP = 43008;
constexpr int L_YP = 90112;
constexpr int L_UIMG = 10240;
constexpr int L_RED = 14336;
constexpr int L_DEAD = 51200;
constexpr int L_PXS = 87040;
constexpr int L_STAGE = 53248;
constexpr int L_X = 0;
constexpr int XBUF = 36864;
constexpr int L_V = 110592;
constexpr int L_WST = 118784;
constexpr int LDS_BYTES = 160 * 1024;

struct MemAddr { unsigned o0, o1, o2, o3; };
DEV MemAddr mem_addr(int rep, int wave, int lane) {
  MemAddr a;
  a.o0 = vo_mem(rep, wave * 3072 + lane * 16);
  a.o1 = vo_mem(rep, wave * 3072 + 1024 + lane * 16);
  a.o2 = vo_mem(rep, wave * 3072 + 2048 + lane * 16);
  a.o3 = vo_mem(rep, 9216);
  return a;
}
DEV unsigned gather_mem(const Params& p, __amdgpu_buffer_rsrc_t rm, char* smem, int wave, int lane, const MemAddr& a, unsigned sb, v4u& v0, v4u& v1, v4u& v2) {
  v0 = ld_mb(rm, a.o0, sb); v1 = ld_mb(rm, a.o1, sb); v2 = ld_mb(rm, a.o2, sb);
  unsigned mx = umax(umax(max4(v0), max4(v1)), max4(v2));
  unsigned spins = 0;
  if (__builtin_expect(__builtin_amdgcn_ballot_w64(mx == SENT) != 0, 0)) {
    for (;;) {
      asm volatile("" ::: "memory");
      v0 = ld_mb(rm, a.o0, sb); v1 = ld_mb(rm, a.o1, sb); v2 = ld_mb(rm, a.o2, sb);
      mx = umax(umax(max4(v0), max4(v1)), max4(v2));
      ++spins;
      if (__builtin_amdgcn_ballot_w64(mx == SENT) == 0) break;
      if (spins > SPIN_CAP) { astore(p.ctrl + C_TMO, 1u); *(LAS unsigned*)(smem + L_DEAD) = 1; break; }
      if ((spins & 1023) == 0 && __builtin_amdgcn_readfirstlane(aload(p.ctrl + C_TMO))) { *(LAS unsigned*)(smem + L_DEAD) = 1; break; }
    }
  }
  if (wave < 3) {
    char* d = smem + L_MEM + wave * 3072 + lane * 16;
    *(v4u*)d = v0; *(v4u*)(d + 1024) = v1; *(v4u*)(d + 2048) = v2;
  }
  return spins;
}
DEV v2u sched_fix(__amdgpu_buffer_rsrc_t rm, unsigned off, unsigned sb, v2u v) {
  if (__builtin_expect((unsigned)__builtin_amdgcn_readfirstlane(umax(v.x, v.y)) == SENT, 0)) {
    for (int i = 0; i < 8 && (unsigned)__builtin_amdgcn_readfirstlane(umax(v.x, v.y)) == SENT; ++i) {
      asm volatile("" ::: "memory");
      v = ld2_mb(rm, off, sb);
    }
    if ((unsigned)__builtin_amdgcn_readfirstlane(umax(v.x, v.y)) == SENT) { unsigned n = rt(); v = (v2u){n, n}; }
  }
  return v;
}
DEV void zero_acc(f32x4 (&acc)[4]) {
#pragma unroll
  for (int i = 0; i < 4; ++i) acc[i] = (f32x4){0, 0, 0, 0};
  asm volatile("" : "+v"(acc[0]), "+v"(acc[1]), "+v"(acc[2]), "+v"(acc[3]));
}
DEV void proj36(const char* smem, const v8u (&w)[4][9], int lane, f32x4 (&acc)[4]) {
  const int idx = (lane & 8) ? 0xEEEEEEEE : 0x44444444;
  v4u a[9];
  __builtin_amdgcn_sched_barrier(0);
#pragma unroll
  for (int t = 0; t < 9; ++t) a[t] = *(const v4u*)(smem + L_MEM + t * 1024 + lane * 16);
  __builtin_amdgcn_sched_barrier(0);
#pragma unroll
  for (int t = 0; t < 9; ++t) {
#pragma unroll
    for (int i = 0; i < 4; ++i) acc[i] = smfma(a[t], w[i][t], acc[i], idx);
  }
  __builtin_amdgcn_sched_barrier(0);
}
DEV v2u pairsum(f32x4 a0, f32x4 a1) {
  float r[4];
#pragma unroll
  for (int i = 0; i < 4; ++i) {
    auto sw = __builtin_amdgcn_permlane32_swap(__float_as_uint(a0[i]), __float_as_uint(a1[i]), false, false);
    r[i] = __uint_as_float(sw[0]) + __uint_as_float(sw[1]);
  }
  return (v2u){cvtpk(r[0], r[1]), cvtpk(r[2], r[3])};
}
DEV unsigned read_pol(const Params& p) {
  return __builtin_amdgcn_readfirstlane(aload(p.ctrl + C_POL)) & 0xfffu;
}

DEV f32x4 mfma16k16(v2u a, v2u b, f32x4 c) {
  return __builtin_amdgcn_mfma_f32_16x16x16bf16_1k(__builtin_bit_cast(s16x4, a), __builtin_bit_cast(s16x4, b), c, 0, 0, 0);
}
template <int WV>
DEV void head_loop(const Params& p, char* smem, int h, int lane, const v8u (&w)[4][9], unsigned pol) {
  constexpr int wave = WV;
  __amdgpu_buffer_rsrc_t rm = mkrsrc(p.mbox, MBOX_BYTES);
  __amdgpu_buffer_rsrc_t rkx = mkrsrc(p.data + OFF_KX, SZ_KX);
  __amdgpu_buffer_rsrc_t rqx = mkrsrc(p.data + OFF_QX, SZ_KX);
  __amdgpu_buffer_rsrc_t rpx = mkrsrc(p.data + OFF_PX, SZ_PX);
  const float SC = 0.08838834764831845f * 1.4426950408889634f;
  const int g = lane >> 4, qi = lane & 15, hi = (lane >> 3) & 1;
  const float NEG = -1e30f;
  const float c0 = (wave == 0 && g >= 2) ? NEG : 0.f;
  const int rep = h & 3;
  v2u selq[2], selk[2];
#pragma unroll
  for (int tl = 0; tl < 2; ++tl) {
    unsigned e[4];
#pragma unroll
    for (int jj = 0; jj < 4; ++jj) e[jj] = (4 * g + jj == 8 * tl + (qi & 7)) ? 0x3F80u : 0u;
    selq[tl] = (v2u){e[0] | (e[1] << 16), e[2] | (e[3] << 16)};
    selk[tl] = hi ? (v2u){0u, 0u} : selq[tl];
  }
  const f32x4 zero4 = {0, 0, 0, 0}, c04 = {c0, c0, c0, c0};
  const MemAddr ma = mem_addr(rep, wave == 3 ? 0 : wave, lane);
  unsigned adx = hi ? vo_p(0, 16384 + (h >> 3) * 1024 + (h & 3) * 256 + ((h >> 2) & 1) * 128 + (qi & 7) * 16)
                    : vo_p(0, (h >> 1) * 1024 + g * 256 + (h & 1) * 128 + (qi & 7) * 16);
  if (hi && g != 0) adx = 0x70000000u;
  unsigned tM = rt();
  for (int s = 0; s < NSEG; ++s) {
    const char* kq = smem + L_KQ + (s & 1) * 16384 + lane * 16;
    v4u kxw[2];
    kxw[0] = *(const v4u*)(kq + wave * 1024); kxw[1] = *(const v4u*)(kq + (4 + wave) * 1024);
    const unsigned sb = __builtin_amdgcn_readfirstlane(sbase(s, pol));
    f32x4 acc[4]; zero_acc(acc);
    f32x4 t0, t1, t2, dq0, dq1, dk0, dk1;
    asm volatile("" : "+v"(kxw[0]), "+v"(kxw[1]));
    const unsigned tM0 = tM; (void)tM0;
    DPT(10, tM0);
    constexpr int T0 = (wave == 3) ? 0 : 3 * wave;
    const int sidx = (lane & 8) ? 0xEEEEEEEE : 0x44444444;
    {
      v4u g3[3];
      wait_until(tM);
      gather_mem(p, rm, smem, wave, lane, ma, sb, g3[0], g3[1], g3[2]);
      __builtin_amdgcn_sched_barrier(0);
#pragma unroll
      for (int i = 0; i < 4; ++i) acc[i] = smfma(g3[0], w[i][T0], acc[i], sidx);
      __builtin_amdgcn_sched_barrier(0);
      DPT(11, tM0);
      RAW_BARRIER();
      v4u a[6];
#pragma unroll
      for (int j = 0; j < 6; ++j) a[j] = *(const v4u*)(smem + L_MEM + ((T0 + 3 + j) % 9) * 1024 + lane * 16);
      __builtin_amdgcn_sched_barrier(0);
#pragma unroll
      for (int j = 1; j < 3; ++j)
#pragma unroll
        for (int i = 0; i < 4; ++i) acc[i] = smfma(g3[j], w[i][T0 + j], acc[i], sidx);
#pragma unroll
      for (int j = 0; j < 6; ++j)
#pragma unroll
        for (int i = 0; i < 4; ++i) acc[i] = smfma(a[j], w[i][(T0 + 3 + j) % 9], acc[i], sidx);
      __builtin_amdgcn_sched_barrier(0);
    }
    DPT(12, tM0);
    v2u sch = ld2_mb(rm, ma.o3, sb);
    v2u pkq = pairsum(acc[0], acc[1]), pkk = pairsum(acc[2], acc[3]);
    asm volatile("" : "+v"(pkq), "+v"(pkk));
    DPT(13, tM0);
    asm("s_nop 1\n\tv_mfma_f32_16x16x16_bf16 %0, %4, %6, 0\n\tv_mfma_f32_16x16x16_bf16 %1, %4, %7, 0\n\t"
        "v_mfma_f32_16x16x16_bf16 %2, %5, %8, 0\n\tv_mfma_f32_16x16x16_bf16 %3, %5, %9, 0\n\ts_nop 7"
        : "=&v"(dq0), "=&v"(dq1), "=&v"(dk0), "=&v"(dk1) : "v"(pkq), "v"(pkk), "v"(selq[0]), "v"(selq[1]), "v"(selk[0]), "v"(selk[1]));
    v4u qf = {cvtpk(dq0[0], dq0[1]), cvtpk(dq0[2], dq0[3]), cvtpk(dq1[0], dq1[1]), cvtpk(dq1[2], dq1[3])};
    v4u kf = {cvtpk(dk0[0], dk0[1]), cvtpk(dk0[2], dk0[3]), cvtpk(dk1[0], dk1[1]), cvtpk(dk1[2], dk1[3])};
    asm("s_nop 1\n\tv_mfma_f32_16x16x32_bf16 %0, %3, %6, 0\n\tv_mfma_f32_16x16x32_bf16 %1, %4, %6, 0\n\t"
        "v_mfma_f32_16x16x32_bf16 %2, %5, %6, %7\n\ts_nop 7"
        : "=&v"(t1), "=&v"(t2), "=&v"(t0) : "v"(kxw[0]), "v"(kxw[1]), "v"(kf), "v"(qf), "v"(c04));
    f32x4 va = {hi ? t2[0] : t1[0], hi ? t2[1] : t1[1], hi ? t2[2] : t1[2], hi ? t2[3] : t1[3]};
    f32x2 vb = {hi ? t0[2] : t0[0], hi ? t0[3] : t0[1]};
    asm volatile("" : "+v"(va), "+v"(vb));
    DPT(14, tM0);
    *(v4u*)(smem + L_YP + wave * 1024 + lane * 16) = kf;
    if (wave > 0) {
      *(f32x4*)(smem + L_SP + (wave - 1) * 1536 + lane * 16) = va;
      *(f32x2*)(smem + L_SP + (wave - 1) * 1536 + 1024 + lane * 8) = vb;
    }
    RAW_BARRIER();
    DPT(15, tM0);
    if (wave == 0) {
      f32x4 pa[3]; f32x2 pb[3];
#pragma unroll
      for (int i = 0; i < 3; ++i) {
        pa[i] = *(const f32x4*)(smem + L_SP + i * 1536 + lane * 16);
        pb[i] = *(const f32x2*)(smem + L_SP + i * 1536 + 1024 + lane * 8);
      }
      __builtin_amdgcn_sched_barrier(0);
      va = (va + pa[0]) + (pa[1] + pa[2]);
      vb = (vb + pb[0]) + (pb[1] + pb[2]);
      float m = fmaxf(fmaxf(fmaxf(va[0], va[1]), va[2]), fmaxf(fmaxf(va[3], vb[0]), vb[1]));
      m = xmax8(m);
      const float ms = -m * SC;
      float v0 = __builtin_amdgcn_exp2f(fmaf(va[0], SC, ms)), v1 = __builtin_amdgcn_exp2f(fmaf(va[1], SC, ms)), v2 = __builtin_amdgcn_exp2f(fmaf(va[2], SC, ms));
      float v3 = __builtin_amdgcn_exp2f(fmaf(va[3], SC, ms)), v4 = __builtin_amdgcn_exp2f(fmaf(vb[0], SC, ms)), v5 = __builtin_amdgcn_exp2f(fmaf(vb[1], SC, ms));
      float l = ((v0 + v1) + (v2 + v3)) + (v4 + v5);
      l += dpp<ROR8>(l);
      l = xsum(l);
      float inv = __builtin_amdgcn_rcpf(l);
      v2u px = {umin(cvtpk(v0 * inv, v1 * inv), 0xFFFFFFFEu), umin(cvtpk(v2 * inv, v3 * inv), 0xFFFFFFFEu)};
      unsigned pm = umin(cvtpk(v4 * inv, v5 * inv), 0xFFFFFFFEu);
      unsigned ox = __builtin_amdgcn_mov_dpp(px.x, ROR8, 0xf, 0xf, true), oy = __builtin_amdgcn_mov_dpp(px.y, ROR8, 0xf, 0xf, true);
      unsigned pmo = __builtin_amdgcn_mov_dpp(pm, ROR8, 0xf, 0xf, true);
      auto s1 = __builtin_amdgcn_permlane16_swap(pm, pm, false, false);
      auto s2 = __builtin_amdgcn_permlane16_swap(pmo, pmo, false, false);
      v4u o;
      o.x = hi ? pmo : px.x; o.y = hi ? pm : px.y; o.z = hi ? s2[1] : ox; o.w = hi ? s1[1] : oy;
#pragma unroll
      for (int rp = 0; rp < NREP; ++rp) st_mb(o, rm, adx, sb + rp * 81920);
    }
    DPT(16, tM0);
    if (wave == 3) {
      const int sn = min(s + 1, NSEG - 1);
      const char* src = p.data + OFF_KX + (size_t)(sn * 32 + h) * 8192 + lane * 16;
      char* dst = smem + L_KQ + ((s + 1) & 1) * 16384;
      const unsigned ldst = __builtin_amdgcn_readfirstlane((unsigned)(size_t)(LAS char*)dst);
#pragma unroll
      for (int i = 0; i < 16; ++i) {
        unsigned keep;
        const char* gp = src + (i >> 3) * SZ_KX + (i & 7) * 1024;
        asm volatile("s_mov_b32 %0, m0\n\ts_mov_b32 m0, %2\n\ts_nop 0\n\tglobal_load_lds_dwordx4 %1, off sc1\n\ts_mov_b32 m0, %0"
                     : "=&s"(keep) : "v"(gp), "s"(ldst + i * 1024) : "memory");
      }
      if (!wait_ge(p.ctrl + C_KQ + min(s + 2, NSEG - 1) * 32 + h, 1u, p.ctrl + C_TMO, 2u)) *(LAS unsigned*)(smem + L_DEAD) = 1;
      drain();
    }
    if (wave == 1 || wave == 2) {
      constexpr int qt = (wave == 2) ? 1 : 0;
      f32x4 x1 = zero4, x2 = zero4, y = zero4;
#pragma unroll
      for (int u = 0; u < 4; ++u) {
        v4u q = *(const v4u*)(kq + 8192 + (qt * 4 + u) * 1024);
        x1 = mfma16(*(const v4u*)(kq + u * 1024), q, x1); x2 = mfma16(*(const v4u*)(kq + (4 + u) * 1024), q, x2);
        y = mfma16(*(const v4u*)(smem + L_YP + u * 1024 + lane * 16), q, y);
      }
      float v[12];
      const int tq = 16 * qt + qi;
#pragma unroll
      for (int r = 0; r < 4; ++r) {
        v[r] = (g < 2) ? y[r] * SC : NEG;
        v[4 + r] = (4 * g + r <= tq) ? x1[r] * SC : NEG;
        v[8 + r] = (16 + 4 * g + r <= tq) ? x2[r] * SC : NEG;
      }
      float m = amax3(amax3(v[0], v[1], v[2]), amax3(v[3], v[4], v[5]), fmaxf(amax3(v[6], v[7], v[8]), amax3(v[9], v[10], v[11])));
      m = xmax(m);
      float l = 0;
#pragma unroll
      for (int i = 0; i < 12; ++i) { v[i] = __builtin_amdgcn_exp2f(v[i] - m); l += v[i]; }
      l = xsum(l);
      float inv = __builtin_amdgcn_rcpf(l);
#pragma unroll
      for (int i = 0; i < 12; ++i) v[i] *= inv;
      v4u px = {cvtpk(v[4], v[5]), cvtpk(v[6], v[7]), cvtpk(v[8], v[9]), cvtpk(v[10], v[11])};
      v2u pm = {cvtpk(v[0], v[1]), cvtpk(v[2], v[3])};
      *(v4u*)(smem + L_PXS + qt * 1024 + lane * 16) = px;
      if (g < 2) *(v2u*)(smem + L_PXS + 2048 + qt * 256 + qi * 16 + g * 8) = pm;
    }
    sch = sched_fix(rm, ma.o3, sb, sch);
    tM = __builtin_amdgcn_readfirstlane(sch.y);
    RAW_BARRIER();
    if (wave == 3) {
      if (s > 0 && lane == 0) astore(p.ctrl + C_PX + h * 32, (unsigned)s);
      v4u a0 = *(const v4u*)(smem + L_PXS + lane * 16), a1 = *(const v4u*)(smem + L_PXS + 1024 + lane * 16);
      v4u a2 = *(const v4u*)(smem + L_PXS + 2048 + (lane & 31) * 16);
      st_sc1(a0, rpx, s * 81920 + h * 1024 + lane * 16);
      st_sc1(a1, rpx, s * 81920 + (32 + h) * 1024 + lane * 16);
      if (lane < 32) st_sc1(a2, rpx, s * 81920 + 65536 + (((lane >> 4) * 8 + (h >> 2)) * 64 + (h & 3) * 16 + (lane & 15)) * 16);
      if (s + 1 >= NSEG) { drain(); if (lane == 0) astore(p.ctrl + C_PX + h * 32, (unsigned)s + 1u); }
    }
    if (*(volatile LAS unsigned*)(smem + L_DEAD)) break;
  }
}

DEV void head_role(const Params& p, char* smem, int h, int wave, int lane, int tid) {
  v8u w[4][9];
  {
    const int gB = lane >> 4, q4 = (lane & 15) >> 2, p4 = lane & 3;
    v4u st[6][8];
    auto ldstage = [&](int t) {
#pragma unroll
      for (int i = 0; i < 8; ++i) {
        int ch = tid + 256 * i; int row = ch >> 5, cc = ch & 31;
        const u16* src = (cc < 16 ? p.Wq : p.Wk) + (size_t)(64 * t + row) * 4096 + h * 128 + (cc & 15) * 8;
        st[t % 6][i] = *(const v4u*)src;
      }
    };
#pragma unroll
    for (int t = 0; t < 6; ++t) ldstage(t);
#pragma unroll
    for (int t = 0; t < 9; ++t) {
      __syncthreads();
#pragma unroll
      for (int i = 0; i < 8; ++i) { int ch = tid + 256 * i; int row = ch >> 5, cc = ch & 31; *(v4u*)(smem + L_STAGE + row * 528 + cc * 16) = st[t % 6][i]; }
      if (t + 6 < 9) ldstage(t + 6);
      __syncthreads();
#pragma unroll
      for (int tl = 0; tl < 4; ++tl) {
        v8u ww;
#pragma unroll
        for (int X = 0; X < 2; ++X)
#pragma unroll
          for (int half = 0; half < 2; ++half) {
            int row0 = 8 * (4 * half + 2 * X + (gB >> 1)) + 4 * (gB & 1);
            v2u v = tr_read(smem, L_STAGE + (row0 + q4) * 528 + ((tl >> 1) * 128 + 32 * wave + 16 * (tl & 1) + 4 * p4) * 2);
            ww[4 * X + half] = v.x; ww[4 * X + 2 + half] = v.y;
          }
        if (tl >= 2 || (tl == 1 && t < 3)) asm volatile("" : "+a"(ww));
        w[tl][t] = ww;
      }
    }
  }
  DSTAMP(h == 0 && wave == 0, 0);
  if (tid == 0) *(unsigned*)(smem + L_DEAD) = 0;
  if (wave == 0) { wait_ge(p.ctrl + C_CLR, NSPARE, p.ctrl + C_TMO, 3u); wait_ge(p.ctrl + C_KQ + h, 1u, p.ctrl + C_TMO, 4u); wait_ge(p.ctrl + C_KQ + 32 + h, 1u, p.ctrl + C_TMO, 4u); }
  __syncthreads();
  const unsigned pol = read_pol(p);
  {
    __amdgpu_buffer_rsrc_t rkx = mkrsrc(p.data + OFF_KX, SZ_KX);
    __amdgpu_buffer_rsrc_t rqx = mkrsrc(p.data + OFF_QX, SZ_KX);
    v4u v[4];
#pragma unroll
    for (int i = 0; i < 4; ++i) { int f = wave * 4 + i; v[i] = ld_sc1(wave < 2 ? rkx : rqx, h * 8192 + (f & 7) * 1024 + lane * 16); }
#pragma unroll
    for (int i = 0; i < 4; ++i) *(v4u*)(smem + L_KQ + (wave * 4 + i) * 1024 + lane * 16) = v[i];
  }
  __syncthreads();
  DSTAMP(h == 0 && wave == 0, 1);
  if (wave == 0) head_loop<0>(p, smem, h, lane, w, pol);
  else if (wave == 1) head_loop<1>(p, smem, h, lane, w, pol);
  else if (wave == 2) head_loop<2>(p, smem, h, lane, w, pol);
  else head_loop<3>(p, smem, h, lane, w, pol);
}

constexpr int L_WOST = 0;
constexpr int L_AST = 118784;
DEV void load_wo(const Params& p, char* smem, int h, int wave, int lane, int tid, v4u (&wo)[9][4]) {
  constexpr int RS = 1152 + 16;
  const int g = lane >> 4, q4 = (lane & 15) >> 2, p4 = lane & 3;
  v4u st[2][18];
#pragma unroll
  for (int hf = 0; hf < 2; ++hf)
#pragma unroll
    for (int i = 0; i < 18; ++i) { int ch = tid + 256 * i; st[hf][i] = *(const v4u*)(p.Wo + ((size_t)h * 128 + 64 * hf) * 576 + (size_t)ch * 8); }
#pragma unroll
  for (int hf = 0; hf < 2; ++hf) {
    __syncthreads();
#pragma unroll
    for (int i = 0; i < 18; ++i) { int ch = tid + 256 * i; *(v4u*)(smem + L_WOST + (ch / 72) * RS + (ch % 72) * 16) = st[hf][i]; }
    __syncthreads();
#pragma unroll
    for (int tl = 0; tl < 9; ++tl)
#pragma unroll
      for (int uu = 0; uu < 2; ++uu) {
        int a0 = L_WOST + (32 * uu + 4 * g + q4) * RS + ((wave * 9 + tl) * 16 + 4 * p4) * 2;
        v2u x0 = tr_read(smem, a0), x1 = tr_read(smem, a0 + 16 * RS);
        wo[tl][2 * hf + uu] = (v4u){x0.x, x0.y, x1.x, x1.y};
      }
  }
  __syncthreads();
}
struct ABlk { v4u v[4]; };
DEV void wv_block_load(const Params& p, int h, int t, int tid, ABlk& b) {
#pragma unroll
  for (int i = 0; i < 4; ++i) { int ch = tid + 256 * i; b.v[i] = *(const v4u*)(p.Wv + (size_t)(64 * t + (ch >> 4)) * 4096 + h * 128 + (ch & 15) * 8); }
}
template <int TL0, int TL1>
DEV void wvo_unit(const Params& p, char* smem, int h, int t, int slot, int wave, int lane, int tid, const ABlk& blk, const v4u (&wo)[9][4]) {
  const int g = lane >> 4;
  __amdgpu_buffer_rsrc_t rw = mkrsrc(p.data + OFF_WVO, SZ_WVO);
  char* ab = smem + L_AST + slot * 17408;
#pragma unroll
  for (int i = 0; i < 4; ++i) { int ch = tid + 256 * i; *(v4u*)(ab + (ch >> 4) * 272 + (ch & 15) * 16) = blk.v[i]; }
  RAW_BARRIER();
  v4u a[4][4];
#pragma unroll
  for (int tt = 0; tt < 4; ++tt)
#pragma unroll
    for (int u = 0; u < 4; ++u) {
      const char* src = ab + (16 * tt + (lane & 15)) * 272 + (32 * u + 4 * g) * 2;
      v2u x0 = *(const v2u*)src, x1 = *(const v2u*)(src + 32);
      a[tt][u] = (v4u){x0.x, x0.y, x1.x, x1.y};
    }
#pragma unroll
  for (int tl = TL0; tl < TL1; ++tl) {
    f32x4 d[4];
#pragma unroll
    for (int tt = 0; tt < 4; ++tt) {
      d[tt] = (f32x4){0, 0, 0, 0};
#pragma unroll
      for (int u = 0; u < 4; ++u) d[tt] = mfma16(a[tt][u], wo[tl][u], d[tt]);
    }
    const int n = (wave * 9 + tl) * 16 + (lane & 15), c = n >> 3;
    unsigned off = ((c * 16 + (h >> 1)) * 9 + t) * 2048 + (g * 16 + (h & 1) * 8 + (n & 7)) * 16;
    st_sc1((v4u){cvtpk(d[0][0], d[0][1]), cvtpk(d[2][0], d[2][1]), cvtpk(d[0][2], d[0][3]), cvtpk(d[2][2], d[2][3])}, rw, off);
    st_sc1((v4u){cvtpk(d[1][0], d[1][1]), cvtpk(d[3][0], d[3][1]), cvtpk(d[1][2], d[1][3]), cvtpk(d[3][2], d[3][3])}, rw, off + 1024);
  }
}

#define SCHED_A0 124
#define SCHED_B0 70
#define LATE0 56
#define DEC_EVERY 5
DEV void col_role(const Params& p, char* smem, int c, int wave, int lane, int tid) {
  __amdgpu_buffer_rsrc_t rm = mkrsrc(p.mbox, MBOX_BYTES);
  __amdgpu_buffer_rsrc_t rux = mkrsrc(p.data + OFF_UX, SZ_UX);
  __amdgpu_buffer_rsrc_t rum = mkrsrc(p.data + OFF_UM, SZ_UM);
  __amdgpu_buffer_rsrc_t rwv = mkrsrc(p.data + OFF_WVO, SZ_WVO);
  if (c < 64) {
    const int hh = ((c >> 3) << 2) | (c & 3), par = (c >> 2) & 1;
    ABlk b0, b1, b2;
    v4u wo[9][4];
    wv_block_load(p, hh, par, tid, b0); wv_block_load(p, hh, par + 2, tid, b1); wv_block_load(p, hh, par + 4, tid, b2);
    load_wo(p, smem, hh, wave, lane, tid, wo);
    wvo_unit<0, 9>(p, smem, hh, par, 0, wave, lane, tid, b0, wo);
    wvo_unit<0, 9>(p, smem, hh, par + 2, 1, wave, lane, tid, b1, wo);
    wvo_unit<0, 9>(p, smem, hh, par + 4, 0, wave, lane, tid, b2, wo);
    wg_signal(p.ctrl + C_WVO, tid);
  }
  if (tid == 0) *(unsigned*)(smem + L_DEAD) = 0;
  if (wave == 0) wait_ge(p.ctrl + C_CLR, NSPARE, p.ctrl + C_TMO, 5u);
  __syncthreads();
  DSTAMP(c == 0 && wave == 0, 2);
  const unsigned pol = read_pol(p);
  const int rep = c & 3;
  const unsigned slice = (c >> 3) * 1024 + ((c & 3) * 16 + ((c >> 2) & 1) * 8) * 16;
  if (wave == 0) {
    const unsigned sb0 = __builtin_amdgcn_readfirstlane(sbase(0, pol));
    if (lane < 8 * NREP) {
      int r = lane & 7, rp = lane >> 3;
      v4u v = *(const v4u*)(p.mem0 + r * 576 + 8 * c);
      v.x = umin(v.x, 0xFFFFFFFEu); v.y = umin(v.y, 0xFFFFFFFEu); v.z = umin(v.z, 0xFFFFFFFEu); v.w = umin(v.w, 0xFFFFFFFEu);
      st_mb(v, rm, vo_mem(rp, slice + r * 16), sb0);
    }
    if (c == 0) {
      unsigned now = rt(); if (now == SENT) now = 0;
      if (lane < NREP) st_mb((v4u){now, now, 0u, 0u}, rm, vo_mem(lane, 9216), sb0);
    }
    wait_ge(p.ctrl + C_WVO, 128u, p.ctrl + C_TMO, 6u);
  }
  __syncthreads();
  DSTAMP(c == 0 && wave == 0, 3);
  v8u w[4][9];
#pragma unroll
  for (int tl = 0; tl < 4; ++tl)
#pragma unroll
    for (int t = 0; t < 9; ++t) {
      unsigned off = ((c * 16 + wave * 4 + tl) * 9 + t) * 2048 + lane * 16;
      v4u x0 = ld_sc1(rwv, off), x1 = ld_sc1(rwv, off + 1024);
      w[tl][t] = __builtin_shufflevector(x0, x1, 0, 1, 2, 3, 4, 5, 6, 7);
    }
#pragma unroll
  for (int tl = 1; tl < 4; ++tl)
#pragma unroll
    for (int t = 0; t < 9; ++t)
      if (tl >= 2 || t < 3) asm volatile("" : "+a"(w[tl][t]));
  auto loop = [&](auto WV) {
  constexpr int wave = decltype(WV)::value;
  const int g = lane >> 4, half = (lane >> 3) & 1, n7 = lane & 7;
  const int uw_off = L_UIMG + (((2 * (4 * wave + (lane >> 5)) + ((lane & 15) >> 3)) * 8 + (lane & 7)) * 8 + 4 * ((lane >> 4) & 1)) * 2;
  const int red_w = ((g >> 1) == half) ? L_RED + (((wave * 2 + half) * 8 + n7) * 8 + 4 * (g & 1)) * 4 : L_RED + 2048 + lane * 16;
  const int red_r = L_RED + (((lane >> 1) & 7) * 8 + 4 * (lane & 1)) * 4;
  const MemAddr ma = mem_addr(rep, wave, lane);
  const unsigned po = vo_p(rep, wave * 4096 + lane * 16), pm_o = vo_p(rep, 16384 + wave * 1024 + lane * 16);
  const unsigned ado = (lane & 1) ? 0x70000000u : vo_mem(lane >> 4, slice + ((lane >> 1) & 7) * 16);
  const unsigned uxo = c * 16384 + wave * 4096 + lane * 16;
  DSTAMP(c == 0 && wave == 0, 20);
  wait_ge(p.ctrl + C_UX, 32u, p.ctrl + C_TMO, 7u);
  DSTAMP(c == 0 && wave == 0, 21);
  int rdy = 1;
  unsigned tM = rt();
  int schedA = SCHED_A0, schedB = SCHED_B0, streak = 0, flip = 0; bool prevLate = true; int lateM = LATE0;
  unsigned sb = __builtin_amdgcn_readfirstlane(sbase(0, pol));
  for (int s = 0; s < NSEG; ++s) {
    f32x4 acc[4]; zero_acc(acc);
    unsigned mfail = 0;
    const unsigned tM0 = tM; (void)tM0;
    DPT(0, tM0);
    if (wave < 3) { wait_until(tM); v4u g0, g1, g2; mfail = gather_mem(p, rm, smem, wave, lane, ma, sb, g0, g1, g2); }
    else if (s > 0) {
      v4u u4[4];
#pragma unroll
      for (int w4 = 0; w4 < 4; ++w4) u4[w4] = *(const v4u*)(smem + L_UIMG + ((8 * w4 + 4 * half + g) * 8 + n7) * 16);
      wait_until(tM + 3u);
#pragma unroll
      for (int w4 = 0; w4 < 4; ++w4) __builtin_amdgcn_raw_buffer_store_b128(u4[w4], rum, c * 4096 + w4 * 1024 + lane * 16, (unsigned)(s - 1) * (72u * 4096u), 16);
      if (lane == 0) astore(p.ctrl + C_UM + c * 32, (unsigned)(s - 1));
    }
    DPT(1, tM0);
    __syncthreads();
    DPT(2, tM0);
    v2u sch = ld2_mb(rm, ma.o3, sb);
    unsigned ofail = SENT;
    if (wave == 3 && c == 0) ofail = __builtin_amdgcn_raw_buffer_load_b32(rm, vo_mem(0, 9216 + 128 * (1 + (lane % 3))), sb, 16);
    v4u ux[4];
    const unsigned uso = (unsigned)s * (72u * 16384u);
#pragma unroll
    for (int q = 0; q < 4; ++q) ux[q] = __builtin_amdgcn_raw_buffer_load_b128(rux, uxo + q * 1024, uso, 16);
    unsigned fl2 = 32u;
    const bool chk = rdy < s + 3 && rdy < NSEG;
    if (__builtin_expect(chk, 0)) fl2 = aload(p.ctrl + C_UX + lane);
    proj36(smem, w, lane, acc);
    v2u pk0 = pairsum(acc[0], acc[1]), pk1 = pairsum(acc[2], acc[3]);
    asm volatile("" : "+v"(pk0), "+v"(pk1));
    DPT(3, tM0);
    *(v2u*)(smem + uw_off) = pk0;
    *(v2u*)(smem + uw_off + 512) = pk1;
    v4u ub = *(const v4u*)(smem + L_UIMG + ((8 * wave + 4 * half + g) * 8 + n7) * 16);
    const unsigned sbn = __builtin_amdgcn_readfirstlane(sbase(s + 1, pol));
    if (s < NSEG - 1) {
      asm volatile("s_waitcnt vmcnt(0)" : "+v"(ux[0]), "+v"(ux[1]), "+v"(ux[2]), "+v"(ux[3]), "+v"(ub), "+v"(fl2) :: "memory");
      if (__builtin_expect(chk, 0)) {
        unsigned long long nb = __builtin_amdgcn_ballot_w64(fl2 < 32u);
        rdy = nb ? (int)__builtin_ctzll(nb) : NSEG;
        __builtin_amdgcn_fence(__ATOMIC_ACQUIRE, "wavefront");
        if (rdy < s + 2) {
          if (!wait_ge(p.ctrl + C_UX + s + 1, 32u, p.ctrl + C_TMO, 7u)) *(LAS unsigned*)(smem + L_DEAD) = 1;
          rdy = s + 2;
        }
      }
      DPT(4, tM0);
      sch = sched_fix(rm, ma.o3, sb, sch);
      const unsigned tP = __builtin_amdgcn_readfirstlane(sch.x);
      tM = __builtin_amdgcn_readfirstlane(sch.y);
      f32x4 d = {0, 0, 0, 0};
      asm volatile("" : "+v"(d));
      DPT(5, tM0);
      wait_until(tP);
      DPT(6, tP);
      unsigned pfail = 0;
      {
        v4u v[5];
#pragma unroll
        for (int q = 0; q < 4; ++q) v[q] = ld_mb(rm, po + q * 1024, sb);
        v[4] = ld_mb(rm, pm_o, sb);
        unsigned mx = 0;
#pragma unroll
        for (int q = 0; q < 4; ++q) { d = mfma16(ux[q], v[q], d); mx = umax(mx, max4(v[q])); }
        d = mfma16(ub, v[4], d); mx = umax(mx, max4(v[4]));
        if (__builtin_expect(__builtin_amdgcn_ballot_w64(mx == SENT) != 0, 0)) {
          for (;;) {
            asm volatile("" ::: "memory");
#pragma unroll
            for (int q = 0; q < 4; ++q) v[q] = ld_mb(rm, po + q * 1024, sb);
            v[4] = ld_mb(rm, pm_o, sb);
            mx = 0;
#pragma unroll
            for (int q = 0; q < 5; ++q) mx = umax(mx, max4(v[q]));
            ++pfail;
            if (__builtin_amdgcn_ballot_w64(mx == SENT) == 0) break;
            if (pfail > SPIN_CAP) { astore(p.ctrl + C_TMO, 8u); *(LAS unsigned*)(smem + L_DEAD) = 1; break; }
            if ((pfail & 1023) == 0 && __builtin_amdgcn_readfirstlane(aload(p.ctrl + C_TMO))) { *(LAS unsigned*)(smem + L_DEAD) = 1; break; }
          }
          d = (f32x4){0, 0, 0, 0};
#pragma unroll
          for (int q = 0; q < 4; ++q) d = mfma16(ux[q], v[q], d);
          d = mfma16(ub, v[4], d);
        }
      }
      asm volatile("" : "+v"(d));
      DPT(7, tP);
      *(f32x4*)(smem + red_w) = d;
      if (lane == 0) *(LAS unsigned*)(smem + L_DEAD + 16 + wave * 4) = (mfail ? 1u : 0u) | (pfail ? 2u : 0u);
      __syncthreads();
      if (wave == 0) {
        f32x4 xb[8];
#pragma unroll
        for (int b8 = 0; b8 < 8; ++b8) xb[b8] = *(const f32x4*)(smem + red_r + b8 * 256);
        __builtin_amdgcn_sched_barrier(0);
        f32x4 x = ((xb[0] + xb[1]) + (xb[2] + xb[3])) + ((xb[4] + xb[5]) + (xb[6] + xb[7]));
        float r0 = x[0], r1 = x[1], r2 = x[2], r3 = x[3];
        v2u o2 = {umin(cvtpk(r0, r1), 0xFFFFFFFEu), umin(cvtpk(r2, r3), 0xFFFFFFFEu)};
        v4u o4 = {o2.x, o2.y, (unsigned)__builtin_amdgcn_mov_dpp(o2.x, 0xB1, 0xf, 0xf, true), (unsigned)__builtin_amdgcn_mov_dpp(o2.y, 0xB1, 0xf, 0xf, true)};
        st_mb(o4, rm, ado, sbn);
        DPT(8, tP);
      } else if (wave == 3 && c >= 1 && c <= 3) {
        v4u f = *(const v4u*)(smem + L_DEAD + 16);
        if (((f.x | f.y | f.z | f.w) & 2u) && lane == 0) __builtin_amdgcn_raw_buffer_store_b32(0u, rm, vo_mem(0, 9216 + 128 * c), sbn, 16);
      } else if (wave == 3 && c == 0) {
        v4u f = *(const v4u*)(smem + L_DEAD + 16);
        unsigned fl = __builtin_amdgcn_readfirstlane(f.x | f.y | f.z | f.w);
        unsigned now = rt();
        unsigned baseT = tM;
        const bool late = (int)(now - tM) > 2 || (int)(tM - now) > 600;
        if (late) baseT = now + (unsigned)lateM;
        if (prevLate && (fl & 2u)) { if (lateM < 80) lateM += 6; } else if (!late && !prevLate && lateM > LATE0) lateM -= 2;
        if (!prevLate) {
          const bool oth = __builtin_amdgcn_ballot_w64(ofail == 0u) != 0;
          if (fl & 1u) { if (oth) schedA += 2; else schedB += 2; }
          else if (fl & 2u) schedA += 2;
          else if (late) schedB += 2;
          else if (++streak >= DEC_EVERY) { streak = 0; if (++flip & 1) schedA -= 1; else schedB -= 1; }
        }
        prevLate = late || (fl & 2u);
        unsigned nP = (baseT + (unsigned)schedA) , nM = nP + (unsigned)schedB;
        if (nP == SENT) nP = 0; if (nM == SENT) nM = 0;
        if (lane < NREP) st_mb((v4u){nP, nM, 0u, 0u}, rm, vo_mem(lane, 9216), sbn);
        DVAL(true, 64 + s, now); DVAL(true, 128 + s, fl | (schedA << 8) | (schedB << 20));
      }
    }
    else __syncthreads();
    sb = sbn;
    if (*(volatile LAS unsigned*)(smem + L_DEAD)) break;
  }
  if (wave == 3) {
#pragma unroll
    for (int w4 = 0; w4 < 4; ++w4) {
      v4u u4 = *(const v4u*)(smem + L_UIMG + ((8 * w4 + 4 * half + g) * 8 + n7) * 16);
      __builtin_amdgcn_raw_buffer_store_b128(u4, rum, c * 4096 + w4 * 1024 + lane * 16, (unsigned)(NSEG - 1) * (72u * 4096u), 16);
    }
    drain();
    if (lane == 0) astore(p.ctrl + C_UM + c * 32, (unsigned)NSEG);
  }
  };
  if (wave == 0) loop(IC<0>{}); else if (wave == 1) loop(IC<1>{}); else if (wave == 2) loop(IC<2>{}); else loop(IC<3>{});
  __syncthreads();
  if (c == 0 && tid < 16) __hip_atomic_fetch_max(p.ctrl + C_PROG + tid * 32, (unsigned)NSEG + 1u, __ATOMIC_RELAXED, __HIP_MEMORY_SCOPE_AGENT);
}

DEV void spare_role(const Params& p, char* smem, int j, int wave, int lane, int tid) {
  __amdgpu_buffer_rsrc_t rm = mkrsrc(p.mbox, MBOX_BYTES);
  if (j == 0 && tid == 0) {
    const unsigned grp = (__builtin_amdgcn_s_getreg(20 | (3 << 11)) >> 2) & 1u;
    unsigned m = 0;
    for (int i = 0; i < NPAGES; ++i) { unsigned r = p.ctrl[C_CAL + i]; m |= ((r ^ (r >> 1) ^ grp) & 1u) << i; }
    const unsigned full = (1u << NPAGES) - 1u, alt = 0x555u & full;
    const int agree = __builtin_popcount(~(m ^ alt) & full);
    if (agree >= NPAGES - 3) m = alt; else if (agree <= 3) m = ~alt & full;
    astore(p.ctrl + C_POL, 0x1000u | m);
  }
  wg_wait_ge(p.ctrl + C_POL, 0x1000u, p.ctrl + C_TMO, 9u, wave);
  const unsigned pol = read_pol(p);
  DSTAMP(j == 0 && wave == 0, 4); DVAL(j == 0 && wave == 0, 5, pol);
  {
    const v4u sv = {SENT, SENT, SENT, SENT};
    for (int u = j; u < NSEG * NREP * 5; u += NSPARE) {
      const int st = u / (NREP * 5), cell = u % (NREP * 5), k = cell % 5;
      const unsigned sb = __builtin_amdgcn_readfirstlane(sbase(st, pol));
      if (k != 1 || tid < 128) st_mb(sv, rm, cell * 16384 + tid * 16, sb);
      if (k != 1 && k != 4) st_mb(sv, rm, cell * 16384 + 4096 + tid * 16, sb);
    }
  }
  wg_signal(p.ctrl + C_CLR, tid);
  DSTAMP(j == 0 && wave == 0, 6);
  {
    __amdgpu_buffer_rsrc_t rux = mkrsrc(p.data + OFF_UX, SZ_UX);
    __amdgpu_buffer_rsrc_t rkq = mkrsrc(p.data + OFF_KX, 2 * SZ_KX);
    const int xg = j & 3, rk = j >> 2;
    auto wait_slot = [&](int k) {
      const unsigned sbk = __builtin_amdgcn_readfirstlane(sbase(k, pol));
      unsigned tp = SENT;
      for (unsigned spins = 0; spins < SPIN_CAP; ++spins) {
        asm volatile("" ::: "memory");
        v2u sc2 = ld2_mb(rm, vo_mem(xg, 9216), sbk);
        tp = __builtin_amdgcn_readfirstlane(sc2.x);
        if (tp != SENT) break;
        __builtin_amdgcn_s_sleep(8);
        if ((spins & 63u) == 63u && __builtin_amdgcn_readfirstlane(aload(p.ctrl + C_TMO))) break;
      }
      if (j == 0 && lane < 16) __hip_atomic_fetch_max(p.ctrl + C_PROG + lane * 32, (unsigned)k, __ATOMIC_RELAXED, __HIP_MEMORY_SCOPE_AGENT);
      return tp;
    };
    for (int s = 1; s < NSEG; ++s) {
      if (wave == 0) {
        wait_slow(p.ctrl + C_UX + s, 32u, p.ctrl + C_TMO, 15u);
        wait_flags(p.ctrl + C_KQ + s * 32, 32, 1, 1u, lane, p.ctrl + C_TMO, 18u);
        if (s >= 5) {
          unsigned tp = wait_slot(s - 4);
          wait_until(tp - 80u);
        }
      }
      __syncthreads();
      v4u v[12];
#pragma unroll
      for (int i = 0; i < 12; ++i) v[i] = ld_sc1(rux, (s * 72 + xg + 4 * (3 * rk + i / 4)) * 16384 + ((i & 3) * 256 + tid) * 16);
      v4u k6[6];
#pragma unroll
      for (int i = 0; i < 6; ++i) {
        int ch = rk * 6 + i;
        unsigned off = (ch < 32) ? (unsigned)(((ch >> 1) & 1) * SZ_KX + (s * 32 + xg + 4 * (ch >> 2)) * 8192 + (ch & 1) * 4096 + tid * 16) : 0x7ffffff0u;
        k6[i] = ld_sc1(rkq, off);
      }
#pragma unroll
      for (int i = 0; i < 12; ++i) asm volatile("" :: "v"(v[i]));
#pragma unroll
      for (int i = 0; i < 6; ++i) asm volatile("" :: "v"(k6[i]));
    }
    if (j == 0 && wave == 0) for (int k = NSEG - 4; k < NSEG; ++k) wait_slot(k);
  }
}

template <int N> DEV void wait_vm() { asm volatile("s_waitcnt vmcnt(%0)" :: "n"(N) : "memory"); }

DEV int tile_src_off(int wave, int lane) {
  const int row = wave * 8 + (lane >> 3), r = row & 15;
  return row * 576 + (((lane & 7) ^ ((r >> 1) & 7)) * 8);
}
struct XDma { const u16* src; char* dst; bool on; };
DEV XDma x_dma_setup(const u16* x, char* smem, int s, int buf, int wave, int lane, bool on) {
  XDma d;
  d.src = x + (size_t)s * 32 * 576 + tile_src_off(wave, lane);
  d.dst = smem + L_X + buf * XBUF + wave * 1024;
  d.on = on;
  return d;
}
DEV void x_dma_piece(const XDma& d, int wave, int i) {
  if (d.on) __builtin_amdgcn_global_load_lds((const unsigned*)(d.src + i * 64), (LAS unsigned*)(d.dst + i * 4096), 16, 0, 0);
}
template <int DEPTH, class F, class G>
DEV void xloop(const char* tile, int lane, F body, G side) {
  const int r = lane & 15, g = lane >> 4, f = (r >> 1) & 7;
  const char* pe = tile + (r >> 3) * 1024 + (r & 7) * 128 + ((g ^ f) * 16);
  const char* po = tile + (r >> 3) * 1024 + (r & 7) * 128 + (((4 + g) ^ f) * 16);
  auto rd = [&](int ks, int tt) { return *(const v4u*)(((ks & 1) ? po : pe) + (ks >> 1) * 4096 + tt * 2048); };
  v4u f0[DEPTH + 1], f1[DEPTH + 1];
#pragma unroll
  for (int i = 0; i < DEPTH; ++i) { f0[i] = rd(i, 0); f1[i] = rd(i, 1); }
#pragma unroll
  for (int ks = 0; ks < 18; ++ks) {
    if (ks + DEPTH < 18) {
      f0[(ks + DEPTH) % (DEPTH + 1)] = rd(ks + DEPTH, 0);
      f1[(ks + DEPTH) % (DEPTH + 1)] = rd(ks + DEPTH, 1);
    }
    __builtin_amdgcn_sched_barrier(0);
    body(ks, f0[ks % (DEPTH + 1)], f1[ks % (DEPTH + 1)]);
    __builtin_amdgcn_sched_barrier(0);
    side(ks);
  }
}
template <int NST, class FC, class FS>
DEV void stream(const u16* x, char* smem, int s0, int st, int wave, int lane, int tid, FC compute, FS signal) {
  const int n = (NSEG - s0 + st - 1) / st;
  {
    XDma d0 = x_dma_setup(x, smem, s0, 0, wave, lane, true), d1 = x_dma_setup(x, smem, min(s0 + st, NSEG - 1), 1, wave, lane, n > 1);
#pragma unroll
    for (int i = 0; i < 9; ++i) x_dma_piece(d0, wave, i);
#pragma unroll
    for (int i = 0; i < 9; ++i) x_dma_piece(d1, wave, i);
  }
  int buf = 0;
  for (int i = 0; i < n; ++i) {
    const int s = s0 + i * st;
    constexpr int EARLY = 4;
    if (i <= EARLY || i + 1 >= n) wait_vm<0>(); else wait_vm<9 + NST>();
    RAW_BARRIER();
    if (tid == 0) {
      if (i >= 1 && i <= EARLY) signal(s - st);
      else if (i >= EARLY + 2) signal(s - 2 * st);
      if (i > EARLY && i + 1 >= n) signal(s - st);
    }
    XDma d = x_dma_setup(x, smem, min(s + 2 * st, NSEG - 1), buf == 0 ? 2 : buf - 1, wave, lane, i + 2 < n);
    compute(s, smem + L_X + buf * XBUF, d);
    buf = buf == 2 ? 0 : buf + 1;
  }
  wait_vm<0>();
  RAW_BARRIER();
  if (tid == 0) signal(s0 + (n - 1) * st);
}
template <int NTW, class F>
DEV void load_kfrags(char* smem, int wave, int lane, int tid, F srcf, v4u (&w)[NTW][18]) {
  constexpr int CH = NTW * 8;
  constexpr int RS = CH * 16 + 16;
  constexpr int PER = 64 * CH / 256;
  const int g = lane >> 4, q4 = (lane & 15) >> 2, p4 = lane & 3;
  constexpr int DP = 6;
  v4u st[DP][PER];
  auto ld = [&](int t) {
#pragma unroll
    for (int i = 0; i < PER; ++i) { int ch = tid + 256 * i; st[t % DP][i] = srcf(64 * t + ch / CH, ch % CH); }
  };
#pragma unroll
  for (int t = 0; t < DP; ++t) ld(t);
#pragma unroll
  for (int t = 0; t < 9; ++t) {
    __syncthreads();
#pragma unroll
    for (int i = 0; i < PER; ++i) { int ch = tid + 256 * i; *(v4u*)(smem + L_WST + (ch / CH) * RS + (ch % CH) * 16) = st[t % DP][i]; }
    if (t + DP < 9) ld(t + DP);
    __syncthreads();
#pragma unroll
    for (int tl = 0; tl < NTW; ++tl)
#pragma unroll
      for (int kk = 0; kk < 2; ++kk) {
        int a0 = L_WST + (32 * kk + 8 * g + q4) * RS + ((wave * NTW + tl) * 16 + 4 * p4) * 2;
        v2u x0 = tr_read(smem, a0), x1 = tr_read(smem, a0 + 4 * RS);
        w[tl][2 * t + kk] = (v4u){x0.x, x0.y, x1.x, x1.y};
      }
  }
  __syncthreads();
}

DEV void kq_worker(const Params& p, char* smem, int h, int par, int wave, int lane, int tid) {
  v4u w[4][18];
  load_kfrags<4>(smem, wave, lane, tid, [&](int row, int cc) {
    return *(const v4u*)((cc < 16 ? p.Wk : p.Wq) + (size_t)row * 4096 + h * 128 + (cc & 15) * 8); }, w);
  __amdgpu_buffer_rsrc_t ro = mkrsrc(p.data + (wave < 2 ? OFF_KX : OFF_QX), SZ_KX);
  stream<4>(p.x, smem, par, 2, wave, lane, tid,
    [&](int s, const char* xb, const XDma& dm) {
      f32x4 acc[2][4];
#pragma unroll
      for (int a = 0; a < 2; ++a)
#pragma unroll
        for (int b = 0; b < 4; ++b) acc[a][b] = (f32x4){0, 0, 0, 0};
      xloop<2>(xb, lane, [&](int ks, v4u b0, v4u b1) {
#pragma unroll
        for (int tl = 0; tl < 4; ++tl) { acc[0][tl] = mfma16(w[tl][ks], b0, acc[0][tl]); acc[1][tl] = mfma16(w[tl][ks], b1, acc[1][tl]); }
      }, [&](int ks) { if (!(ks & 1)) x_dma_piece(dm, wave, ks >> 1); });
#pragma unroll
      for (int tt = 0; tt < 2; ++tt)
#pragma unroll
        for (int uu = 0; uu < 2; ++uu) {
          f32x4 a = acc[tt][2 * uu], b = acc[tt][2 * uu + 1];
          v4u o = {cvtpk(a[0], a[1]), cvtpk(a[2], a[3]), cvtpk(b[0], b[1]), cvtpk(b[2], b[3])};
          st_sc1(o, ro, (s * 32 + h) * 8192 + (tt * 4 + (wave & 1) * 2 + uu) * 1024 + lane * 16);
        }
      DSTAMP(h == 0 && wave == 1, 256 + s); DVAL(h == 0 && wave == 1, 320 + s, (unsigned)clk());
    },
    [&](int s) { astore(p.ctrl + C_KQ + s * 32 + h, 1u); });
}

DEV void vu_worker(const Params& p, char* smem, int h, int par, int wave, int lane, int tid) {
  const int g = lane >> 4;
  v4u wo[9][4];
  ABlk b0, b1;
  wv_block_load(p, h, 6 + par, tid, b0); wv_block_load(p, h, 8, tid, b1);
  load_wo(p, smem, h, wave, lane, tid, wo);
  v4u wv[2][18];
  load_kfrags<2>(smem, wave, lane, tid, [&](int row, int cc) {
    return *(const v4u*)(p.Wv + (size_t)row * 4096 + h * 128 + cc * 8); }, wv);
  DSTAMP(h == 0 && par == 0 && wave == 0, 8);
  wvo_unit<0, 9>(p, smem, h, 6 + par, 0, wave, lane, tid, b0, wo);
  if (par == 0) wvo_unit<0, 5>(p, smem, h, 8, 1, wave, lane, tid, b1, wo);
  else wvo_unit<5, 9>(p, smem, h, 8, 1, wave, lane, tid, b1, wo);
  wg_signal(p.ctrl + C_WVO, tid);
  DSTAMP(h == 0 && par == 0 && wave == 0, 9);
  __amdgpu_buffer_rsrc_t ro = mkrsrc(p.data + OFF_UX, SZ_UX);
  stream<9>(p.x, smem, par, 2, wave, lane, tid,
    [&](int s, const char* xb, const XDma& dm) {
      f32x4 acc[2][2];
#pragma unroll
      for (int a = 0; a < 2; ++a)
#pragma unroll
        for (int b = 0; b < 2; ++b) acc[a][b] = (f32x4){0, 0, 0, 0};
      xloop<3>(xb, lane, [&](int ks, v4u b0, v4u b1) {
#pragma unroll
        for (int tl = 0; tl < 2; ++tl) { acc[0][tl] = mfma16(wv[tl][ks], b0, acc[0][tl]); acc[1][tl] = mfma16(wv[tl][ks], b1, acc[1][tl]); }
      }, [&](int ks) { if (!(ks & 1)) x_dma_piece(dm, wave, ks >> 1); });
#pragma unroll
      for (int tt = 0; tt < 2; ++tt) {
        f32x4 a = acc[tt][0], b = acc[tt][1];
        *(v4u*)(smem + L_V + (tt * 4 + wave) * 1024 + lane * 16) = (v4u){cvtpk(a[0], a[1]), cvtpk(a[2], a[3]), cvtpk(b[0], b[1]), cvtpk(b[2], b[3])};
      }
      RAW_BARRIER();
      v4u a[2][4];
#pragma unroll
      for (int tt = 0; tt < 2; ++tt)
#pragma unroll
        for (int u = 0; u < 4; ++u) a[tt][u] = *(const v4u*)(smem + L_V + (tt * 4 + u) * 1024 + lane * 16);
#pragma unroll
      for (int tl = 0; tl < 9; ++tl) {
        f32x4 d0 = {0, 0, 0, 0}, d1 = {0, 0, 0, 0};
#pragma unroll
        for (int u = 0; u < 4; ++u) { d0 = mfma16(a[0][u], wo[tl][u], d0); d1 = mfma16(a[1][u], wo[tl][u], d1); }
        const int n = (wave * 9 + tl) * 16 + (lane & 15), c = n >> 3;
        v4u o = {cvtpk(d0[0], d0[1]), cvtpk(d0[2], d0[3]), cvtpk(d1[0], d1[1]), cvtpk(d1[2], d1[3])};
        st_sc1(o, ro, ((s * 72 + c) * 16 + (h >> 1)) * 1024 + (g * 16 + (h & 1) * 8 + (n & 7)) * 16);
      }
      DSTAMP(h == 0 && wave == 1, 192 + s); DVAL(h == 0 && wave == 1, 384 + s, (unsigned)clk());
    },
    [&](int s) { aadd(p.ctrl + C_UX + s, 1u); });
}

DEV void b1_worker(const Params& p, char* smem, int widx, int wave, int lane, int tid) {
  if (widx >= 64 && widx < 127) {
    const int slab = (widx - 64) / 9, sub = (widx - 64) % 9;
    DSTAMP(widx == 64 && wave == 0, 30);
    v4u w[4][18];
    load_kfrags<4>(smem, wave, lane, tid, [&](int row, int cc) {
      int col = slab * 256 + cc * 8; if (col > 1720) col = 1720;
      return *(const v4u*)(p.wqkv + (size_t)row * 1728 + col); }, w);
    DSTAMP(widx == 64 && wave == 0, 31);
    __amdgpu_buffer_rsrc_t rqk = mkrsrc(p.data + OFF_QKB, (unsigned)(2048 * 1152 * 2));
    __amdgpu_buffer_rsrc_t rvt = mkrsrc(p.data + OFF_VTB, (unsigned)(576 * 2048 * 2));
    const int g = lane >> 4;
    const int col0 = slab * 256 + wave * 64;
    const bool isv = col0 >= 1152;
    stream<4>(p.x, smem, sub, 9, wave, lane, tid,
      [&](int s, const char* xb, const XDma& dm) {
        f32x4 acc[2][4];
#pragma unroll
        for (int a = 0; a < 2; ++a)
#pragma unroll
          for (int b = 0; b < 4; ++b) acc[a][b] = (f32x4){0, 0, 0, 0};
        if (!isv) {
          xloop<2>(xb, lane, [&](int ks, v4u b0, v4u b1) {
#pragma unroll
            for (int tl = 0; tl < 4; ++tl) { acc[0][tl] = mfma16(w[tl][ks], b0, acc[0][tl]); acc[1][tl] = mfma16(w[tl][ks], b1, acc[1][tl]); }
          }, [&](int ks) { if (!(ks & 1)) x_dma_piece(dm, wave, ks >> 1); });
#pragma unroll
          for (int tt = 0; tt < 2; ++tt)
#pragma unroll
            for (int tp = 0; tp < 2; ++tp) {
              f32x4 a = acc[tt][2 * tp], b = acc[tt][2 * tp + 1];
              auto r0 = __builtin_amdgcn_permlane16_swap(cvtpk(a[0], a[1]), cvtpk(b[0], b[1]), false, false);
              auto r1 = __builtin_amdgcn_permlane16_swap(cvtpk(a[2], a[3]), cvtpk(b[2], b[3]), false, false);
              st_sc1((v4u){r0[0], r1[0], r0[1], r1[1]}, rqk,
                     ((s * 32 + tt * 16 + (lane & 15)) * 1152 + col0 + 16 * (2 * tp + (g & 1)) + 8 * (g >> 1)) * 2);
            }
        } else {
          xloop<2>(xb, lane, [&](int ks, v4u b0, v4u b1) {
#pragma unroll
            for (int tl = 0; tl < 4; ++tl) { acc[0][tl] = mfma16(b0, w[tl][ks], acc[0][tl]); acc[1][tl] = mfma16(b1, w[tl][ks], acc[1][tl]); }
          }, [&](int ks) { if (!(ks & 1)) x_dma_piece(dm, wave, ks >> 1); });
#pragma unroll
          for (int tl = 0; tl < 4; ++tl) {
            f32x4 a = acc[0][tl], b = acc[1][tl];
            st_sc1((v4u){cvtpk(a[0], a[1]), cvtpk(a[2], a[3]), cvtpk(b[0], b[1]), cvtpk(b[2], b[3])}, rvt,
                   (((col0 - 1152 + tl * 16 + (lane & 15)) * 64 + s) * 4 + g) * 16);
          }
        }
      },
      [&](int) {});
    DSTAMP(widx == 64 && wave == 0, 32);
  }
  wg_signal(p.ctrl + C_B1, tid);
}

DEV void b2_item(const Params& p, char* smem, int hg, int qb, int wave, int lane, int tid) {
  __amdgpu_buffer_rsrc_t rqk = mkrsrc(p.data + OFF_QKB, (unsigned)(2048 * 1152 * 2));
  __amdgpu_buffer_rsrc_t rvt = mkrsrc(p.data + OFF_VTB, (unsigned)(576 * 2048 * 2));
  __amdgpu_buffer_rsrc_t rob = mkrsrc(p.data + OFF_OB, (unsigned)SZ_OB);
  const int g = lane >> 4, li = lane & 15;
  const int hb = hg * 4 + wave;
  const int q0 = qb * 128, k0 = q0 - 256;
  const float SC = 0.23570226039551584f * 1.4426950408889634f;
  const float NEG = -1e30f;
  const unsigned OOB = 0x7ffffff0u;
  auto rowfrag = [&](int tok, int colbase) {
    unsigned off = (tok >= 0 && g < 3) ? (unsigned)((tok * 1152 + colbase + 8 * g) * 2) : OOB;
    v4u v = ld_sc1(rqk, off);
    if (g == 2) { v.y = 0u; v.z = 0u; v.w = 0u; }
    return v;
  };
  v4u kf[24], vf[12][2], qf[8];
#pragma unroll
  for (int qt = 0; qt < 8; ++qt) qf[qt] = rowfrag(q0 + 16 * qt + li, 18 * hb);
#pragma unroll
  for (int t = 0; t < 24; ++t) kf[t] = rowfrag(k0 + 16 * t + li, 576 + 18 * hb);
#pragma unroll
  for (int b = 0; b < 12; ++b)
#pragma unroll
    for (int dt = 0; dt < 2; ++dt) {
      const int blk = 4 * qb - 8 + b, d = 16 * dt + li;
      v4u v = ld_sc1(rvt, (blk >= 0 && d < 18) ? (unsigned)((((18 * hb + d) * 64 + blk) * 4 + g) * 16) : OOB);
      if (dt == 1 && li == 2) v = (v4u){0x3F803F80u, 0x3F803F80u, 0x3F803F80u, 0x3F803F80u};
      vf[b][dt] = v;
    }
#pragma unroll
  for (int qt = 0; qt < 8; ++qt) {
    f32x4 sc[17];
#pragma unroll
    for (int tau = 0; tau < 17; ++tau) {
      const float c = (k0 + 16 * (qt + tau)) >= 0 ? 0.f : NEG;
      f32x4 ci = {c, c, c, c};
      if (tau == 0) {
#pragma unroll
        for (int r = 0; r < 4; ++r) ci[r] = (4 * g + r > li) ? c : NEG;
      }
      if (tau == 16) {
#pragma unroll
        for (int r = 0; r < 4; ++r) ci[r] = (4 * g + r <= li) ? c : NEG;
      }
      sc[tau] = mfma16(kf[qt + tau], qf[qt], ci);
    }
    float m = fmaxf(fmaxf(sc[0][0], sc[0][1]), fmaxf(sc[0][2], sc[0][3]));
#pragma unroll
    for (int tau = 1; tau < 17; ++tau) m = fmaxf(fmaxf(fmaxf(m, sc[tau][0]), sc[tau][1]), fmaxf(sc[tau][2], sc[tau][3]));
    m = xmax(m);
    const float ms = -m * SC;
    unsigned pk[18][2];
#pragma unroll
    for (int t = 0; t < 17; ++t) {
      float e0 = __builtin_amdgcn_exp2f(fmaf(sc[t][0], SC, ms)), e1 = __builtin_amdgcn_exp2f(fmaf(sc[t][1], SC, ms));
      float e2 = __builtin_amdgcn_exp2f(fmaf(sc[t][2], SC, ms)), e3 = __builtin_amdgcn_exp2f(fmaf(sc[t][3], SC, ms));
      pk[t + (qt & 1)][0] = cvtpk(e0, e1); pk[t + (qt & 1)][1] = cvtpk(e2, e3);
    }
    pk[(qt & 1) ? 0 : 17][0] = 0u; pk[(qt & 1) ? 0 : 17][1] = 0u;
    f32x4 o0 = {0, 0, 0, 0}, o1 = {0, 0, 0, 0};
#pragma unroll
    for (int b = 0; b < 9; ++b) {
      v4u pf = {pk[2 * b][0], pk[2 * b][1], pk[2 * b + 1][0], pk[2 * b + 1][1]};
      o0 = mfma16(vf[(qt >> 1) + b][0], pf, o0);
      o1 = mfma16(vf[(qt >> 1) + b][1], pf, o1);
    }
    const float inv = __builtin_amdgcn_rcpf(__shfl(o1[2], li));
    char* orow = smem + (16 * qt + li) * 144 + wave * 36;
    *(unsigned*)(orow + 8 * g) = cvtpk(o0[0] * inv, o0[1] * inv);
    *(unsigned*)(orow + 8 * g + 4) = cvtpk(o0[2] * inv, o0[3] * inv);
    if (g == 0) *(unsigned*)(orow + 32) = cvtpk(o1[0] * inv, o1[1] * inv);
  }
  __syncthreads();
#pragma unroll
  for (int i = 0; i < 5; ++i) {
    int ch = tid + 256 * i;
    if (ch < 1152) { int row = ch / 9, cc = ch % 9; st_sc1(*(const v4u*)(smem + ch * 16), rob, ((q0 + row) * 576 + hg * 72) * 2 + cc * 16); }
  }
  wg_signal(p.ctrl + C_OB + qb * 32, tid);
}

constexpr int S0 = 36;
DEV void fa_item(const Params& p, char* smem, int s, int j, int widx, int wave, int lane, int tid) {
  __amdgpu_buffer_rsrc_t rpx = mkrsrc(p.data + OFF_PX, SZ_PX);
  __amdgpu_buffer_rsrc_t rux = mkrsrc(p.data + OFF_UX, SZ_UX);
  __amdgpu_buffer_rsrc_t rum = mkrsrc(p.data + OFF_UM, SZ_UM);
  __amdgpu_buffer_rsrc_t rab = mkrsrc(p.data + OFF_AB, (unsigned)SZ_OB);
  __amdgpu_buffer_rsrc_t rg = mkrsrc(p.data + OFF_GATE, SZ_GATE);
  const int g = lane >> 4, li = lane & 15;
  const int nt = j * 4 + wave;
  if (wave == 0) {
    wait_ge(p.ctrl + C_UX + s, 32u, p.ctrl + C_TMO, 19u);
    if (s >= S0) wait_ge(p.ctrl + C_FB + s, 9u, p.ctrl + C_TMO, 20u);
  }
  __syncthreads();
  v4u ux[2][16];
#pragma unroll
  for (int sl = 0; sl < 2; ++sl)
#pragma unroll
    for (int pp = 0; pp < 16; ++pp) ux[sl][pp] = ld_sc1(rux, ((s * 72 + nt * 2 + sl) * 16 + pp) * 1024 + lane * 16);
  v2u bp[2] = {{0u, 0u}, {0u, 0u}};
  float gt[2] = {1.f, 1.f};
  if (s >= S0) {
#pragma unroll
    for (int qt = 0; qt < 2; ++qt) {
      bp[qt] = ld2_sc1(rab, ((s * 32 + qt * 16 + li) * 576 + nt * 16 + 4 * g) * 2);
      gt[qt] = __uint_as_float(ld1_sc1(rg, (s * 32 + qt * 16 + li) * 4));
    }
  }
  if (wave == 1) {
    wait_slow(p.ctrl + C_PROG + (4 + widx % 12) * 32, umin((unsigned)s + 3u, 65u), p.ctrl + C_TMO, 17u);
    wait_flags(p.ctrl + C_PX, 32, 32, (unsigned)s + 1u, lane, p.ctrl + C_TMO, 11u);
    wait_flags(p.ctrl + C_UM, NC, 32, (unsigned)s + 1u, lane, p.ctrl + C_TMO, 12u);
  }
  __syncthreads();
  v4u um[2][4];
  {
    v4u v[20];
#pragma unroll
    for (int i = 0; i < 20; ++i) v[i] = ld_sc1(rpx, s * 81920 + (i * 256 + tid) * 16);
#pragma unroll
    for (int sl = 0; sl < 2; ++sl)
#pragma unroll
      for (int mm = 0; mm < 4; ++mm) um[sl][mm] = ld_sc1(rum, ((s * 72 + nt * 2 + sl) * 4 + mm) * 1024 + lane * 16);
#pragma unroll
    for (int i = 0; i < 20; ++i) *(v4u*)(smem + (i * 256 + tid) * 16) = v[i];
  }
  __syncthreads();
#pragma unroll
  for (int qt = 0; qt < 2; ++qt) {
    f32x4 aA[2] = {{0, 0, 0, 0}, {0, 0, 0, 0}}, aB[2] = {{0, 0, 0, 0}, {0, 0, 0, 0}};
    v4u pe[3], po[3];
    auto ldp = [&](int i) {
      const char* b = (i < 16) ? smem + (qt * 32 + 2 * i) * 1024 + lane * 16 : smem + 65536 + (qt * 8 + 2 * (i - 16)) * 1024 + lane * 16;
      pe[i % 3] = *(const v4u*)b; po[i % 3] = *(const v4u*)(b + 1024);
    };
    ldp(0); ldp(1);
#pragma unroll
    for (int i = 0; i < 20; ++i) {
      if (i + 2 < 20) ldp(i + 2);
      __builtin_amdgcn_sched_barrier(0);
#pragma unroll
      for (int sl = 0; sl < 2; ++sl) {
        v4u u = (i < 16) ? ux[sl][i & 15] : um[sl][i & 3];
        aA[sl] = mfma16(u, pe[i % 3], aA[sl]); aB[sl] = mfma16(u, po[i % 3], aB[sl]);
      }
      __builtin_amdgcn_sched_barrier(0);
    }
    const int tok = s * 32 + qt * 16 + li;
    float o[4];
#pragma unroll
    for (int r = 0; r < 4; ++r) {
      auto sw = __builtin_amdgcn_permlane32_swap(__float_as_uint(aB[0][r]), __float_as_uint(aA[1][r]), false, false);
      float own = (lane < 32) ? aA[0][r] : aB[1][r];
      float oth = (lane < 32) ? __uint_as_float(sw[1]) : __uint_as_float(sw[0]);
      unsigned bw = (r < 2) ? bp[qt].x : bp[qt].y;
      o[r] = gt[qt] * (own + oth) + bf2f((r & 1) ? (bw >> 16) : (bw & 0xffffu));
    }
    v2u ov = {cvtpk(o[0], o[1]), cvtpk(o[2], o[3])};
    if (s >= S0) *(v2u*)(p.out + (size_t)tok * 576 + nt * 16 + 4 * g) = ov;
    else st2_sc1(ov, rab, (tok * 576 + nt * 16 + 4 * g) * 2);
  }
  if (s < S0) wg_signal(p.ctrl + C_FA + s, tid); else __syncthreads();
}

DEV void fb_stage(const Params& p, char* smem, int widx, int wave, int lane, int tid) {
  if ((widx >> 2) >= 27) return;
  __amdgpu_buffer_rsrc_t rob = mkrsrc(p.data + OFF_OB, (unsigned)SZ_OB);
  __amdgpu_buffer_rsrc_t rab = mkrsrc(p.data + OFF_AB, (unsigned)SZ_OB);
  __amdgpu_buffer_rsrc_t rg = mkrsrc(p.data + OFF_GATE, SZ_GATE);
  const int g = lane >> 4, li = lane & 15;
  const int j = (widx >> 2) % 9, grp = ((widx >> 2) / 9) * 4 + (widx & 3);
  const int nt = j * 4 + wave;
  v4u wf[1][18];
  load_kfrags<1>(smem, wave, lane, tid, [&](int row, int cc) { return *(const v4u*)(p.wo + (size_t)row * 576 + j * 64 + cc * 8); }, wf);
  v4u gv[9];
#pragma unroll
  for (int i = 0; i < 9; ++i) gv[i] = *(const v4u*)(p.gw + (i * 8 + (lane & 7)) * 8);
  DSTAMP(widx == 0 && wave == 0, 33);
  if (wave == 0) wait_flags(p.ctrl + C_OB, 16, 32, 8u, lane, p.ctrl + C_TMO, 10u);
  __syncthreads();
  DSTAMP(widx == 0 && wave == 0, 34);
  auto seg_of = [&](int it) { int k = it + (S0 - grp + 11) / 12; int ns = (NSEG - grp + 11) / 12; if (k >= ns) k -= ns; return grp + 12 * k; };
  const int nit = (NSEG - grp + 11) / 12;
  v4u v[9], xv[9];
  auto issue = [&](int s) {
#pragma unroll
    for (int i = 0; i < 9; ++i) v[i] = ld_sc1(rob, (s * 32 * 576 + tile_src_off(wave, lane) + i * 64) * 2);
#pragma unroll
    for (int i = 0; i < 9; ++i) xv[i] = *(const v4u*)(p.x + (size_t)(s * 32 + wave * 8 + (lane >> 3)) * 576 + (i * 8 + (lane & 7)) * 8);
  };
  issue(seg_of(0));
  bool lowok = false;
  for (int it = 0; it < nit; ++it) {
    const int s = seg_of(it);
    char* buf = smem + (it & 1) * 40960;
    DSTAMP(widx == 0 && wave == 0, 35 + it);
#pragma unroll
    for (int i = 0; i < 9; ++i) *(v4u*)(buf + (i * 4 + wave) * 1024 + lane * 16) = v[i];
    {
      float a = 0;
#pragma unroll
      for (int i = 0; i < 9; ++i)
#pragma unroll
        for (int e = 0; e < 4; ++e) {
          a += bf2f(xv[i][e] & 0xffffu) * bf2f(gv[i][e] & 0xffffu);
          a += bf2f(xv[i][e] >> 16) * bf2f(gv[i][e] >> 16);
        }
      a += __shfl_xor(a, 1); a += __shfl_xor(a, 2); a += __shfl_xor(a, 4);
      const float gs = 1.f / (1.f + __expf(-a));
      if ((lane & 7) == 0) {
        *(float*)(buf + 36864 + (wave * 8 + (lane >> 3)) * 4) = gs;
        if (j == 0 && s >= S0) st1_sc1(__float_as_uint(gs), rg, (s * 32 + wave * 8 + (lane >> 3)) * 4);
      }
    }
    if (s < S0 && !lowok) {
      drain();
      __syncthreads();
      if (tid == 0) for (int k = 0; k < it; ++k) aadd(p.ctrl + C_FB + seg_of(k), 1u);
      if (wave == 0) wait_flags(p.ctrl + C_FA, S0, 1, 9u, lane, p.ctrl + C_TMO, 21u);
      lowok = true;
    }
    __syncthreads();
    v2u ap[2] = {{0u, 0u}, {0u, 0u}};
    if (s < S0) {
#pragma unroll
      for (int qt = 0; qt < 2; ++qt) ap[qt] = ld2_sc1(rab, ((s * 32 + qt * 16 + li) * 576 + nt * 16 + 4 * g) * 2);
    }
    if (it + 1 < nit) issue(seg_of(it + 1));
    f32x4 bacc[2] = {{0, 0, 0, 0}, {0, 0, 0, 0}};
    xloop<3>(buf, lane, [&](int ks, v4u b0, v4u b1) { bacc[0] = mfma16(wf[0][ks], b0, bacc[0]); bacc[1] = mfma16(wf[0][ks], b1, bacc[1]); }, [&](int) {});
#pragma unroll
    for (int qt = 0; qt < 2; ++qt) {
      const float gt = *(const float*)(buf + 36864 + (qt * 16 + li) * 4);
      const int tok = s * 32 + qt * 16 + li;
      float o[4];
#pragma unroll
      for (int r = 0; r < 4; ++r) {
        unsigned aw = (r < 2) ? ap[qt].x : ap[qt].y;
        o[r] = (1.f - gt) * bacc[qt][r] + gt * bf2f((r & 1) ? (aw >> 16) : (aw & 0xffffu));
      }
      v2u ov = {cvtpk(o[0], o[1]), cvtpk(o[2], o[3])};
      if (s < S0) *(v2u*)(p.out + (size_t)tok * 576 + nt * 16 + 4 * g) = ov;
      else st2_sc1(ov, rab, (tok * 576 + nt * 16 + 4 * g) * 2);
    }
  }
  __syncthreads();
}

template <int STAGE>
DEV void fa_loop(const Params& p, char* smem, int widx, int wave, int lane, int tid) {
  LAS int* cmd = (LAS int*)(smem + 163824);
  for (unsigned spins = 0;; ++spins) {
    if (tid == 0) {
      int c = -1;
      if (STAGE == 2) {
        unsigned t = __hip_atomic_fetch_add(p.ctrl + C_FQ + 32, 1u, __ATOMIC_RELAXED, __HIP_MEMORY_SCOPE_AGENT);
        c = t < (unsigned)((NSEG - S0) * 9) ? (int)t + S0 * 9 : -4;
      } else {
        const unsigned head = aload(p.ctrl + C_FQ);
        if (STAGE == 0 && aload(p.ctrl + C_B1) >= 64u) c = -4;
        else if (STAGE == 1 && head >= (unsigned)(S0 * 9)) c = -4;
        else if (head < (unsigned)(S0 * 9) && aload(p.ctrl + C_PROG + (4 + widx % 12) * 32) >= head / 9u + 3u) {
          unsigned t = __hip_atomic_fetch_add(p.ctrl + C_FQ, 1u, __ATOMIC_RELAXED, __HIP_MEMORY_SCOPE_AGENT);
          c = t < (unsigned)(S0 * 9) ? (int)t : -5;
        }
      }
      if (spins > SPIN_CAP || ((spins & 63u) == 63u && aload(p.ctrl + C_TMO))) { astore(p.ctrl + C_TMO, 22u); c = -4; }
      *cmd = c;
    }
    __syncthreads();
    const int c = __builtin_amdgcn_readfirstlane(*(volatile LAS int*)cmd);
    __builtin_amdgcn_fence(__ATOMIC_ACQUIRE, "wavefront");
    __syncthreads();
    if (c == -4) break;
    if (c == -1) { __builtin_amdgcn_s_sleep(16); continue; }
    if (c == -5) continue;
    fa_item(p, smem, c / 9, c % 9, widx, wave, lane, tid);
    DSTAMP(wave == 0 && (c % 9) == 0, 448 + c / 9);
  }
}

DEV void worker_role(const Params& p, char* smem, int widx, int wave, int lane, int tid) {
  const int slab = ((widx >> 3) << 2) | (widx & 3), par = (widx >> 2) & 1;
  if (slab < 32) vu_worker(p, smem, slab, par, wave, lane, tid);
  else kq_worker(p, smem, slab - 32, par, wave, lane, tid);
  __syncthreads();
  DSTAMP(widx == 0 && wave == 0, 10);
  if (widx >= 64) b1_worker(p, smem, widx, wave, lane, tid);
  fa_loop<0>(p, smem, widx, wave, lane, tid);
  DSTAMP(widx == 0 && wave == 0, 12);
  b2_item(p, smem, widx & 7, widx >> 3, wave, lane, tid);
  DSTAMP(widx == 0 && wave == 0, 13);
  fa_loop<1>(p, smem, widx, wave, lane, tid);
  DSTAMP(widx == 0 && wave == 0, 14);
  fb_stage(p, smem, widx, wave, lane, tid);
  DSTAMP(widx == 0 && wave == 0, 15);
  fa_loop<2>(p, smem, widx, wave, lane, tid);
}

__global__ void __launch_bounds__(256, 1) fused_kernel(Params p) {
  extern __shared__ __attribute__((aligned(16))) char smem[];
  const int tid = threadIdx.x, lane = tid & 63, wave = __builtin_amdgcn_readfirstlane(tid >> 6);
  const int b = blockIdx.x;
  const int idx = (b >> 3) * 4 + (b & 3);
  DSTAMP(wave == 0, 1024 + b);
  if ((b & 7) < 4) {
    if (idx < NH) head_role(p, smem, idx, wave, lane, tid);
    else if (idx < NH + NC) col_role(p, smem, idx - NH, wave, lane, tid);
    else spare_role(p, smem, idx - NH - NC, wave, lane, tid);
  } else {
    worker_role(p, smem, idx, wave, lane, tid);
  }
  DSTAMP(wave == 0, 1280 + b);
}

__global__ void __launch_bounds__(256) prep_kernel(Params p) {
  __shared__ unsigned sm[8];
  const int tid = threadIdx.x, lane = tid & 63, wave = __builtin_amdgcn_readfirstlane(tid >> 6), b = blockIdx.x;
  if (b >= NPAGES) {
    ((v4u*)p.ctrl)[(b - NPAGES) * 256 + tid] = (v4u){0u, 0u, 0u, 0u};
    return;
  }
  __amdgpu_buffer_rsrc_t rm = mkrsrc(p.mbox, MBOX_BYTES);
  unsigned acc = 0, mn0 = 1u << 30, mn1 = 1u << 30;
  const unsigned off = b * (2u << 20) + (1u << 20) + wave * 16384;
  acc += ld1_sc1(rm, off + 128);
  drain();
  asm volatile("" :: "v"(acc));
#pragma unroll 1
  for (int q = 0; q < 6; ++q) {
    unsigned long long t0 = clk();
    acc += ld1_sc1(rm, off + (q & 1) * 8192 + 512 + (q >> 1) * 256);
    drain();
    asm volatile("" :: "v"(acc));
    unsigned d = (unsigned)(clk() - t0);
    if (q & 1) mn1 = min(mn1, d); else mn0 = min(mn0, d);
  }
  if (lane == 0) { sm[wave * 2] = mn0; sm[wave * 2 + 1] = mn1; }
  __syncthreads();
  if (tid == 0) {
    unsigned a0 = min(min(sm[0], sm[2]), min(sm[4], sm[6])), a1 = min(min(sm[1], sm[3]), min(sm[5], sm[7]));
    const unsigned grp = (__builtin_amdgcn_s_getreg(20 | (3 << 11)) >> 2) & 1u;
    p.ctrl[C_CAL + b] = (a1 < a0 ? 1u : 0u) | (grp << 1);
  }
}

}

extern "C" void kernel_launch(void* const* d_in, const int* in_sizes, int n_in, void* d_out, int out_size, void* d_ws,
                              size_t ws_size, hipStream_t stream) {
  (void)in_sizes; (void)n_in; (void)out_size; (void)ws_size;
  Params p;
  p.x = (const u16*)d_in[0]; p.mem0 = (const u16*)d_in[1];
  p.Wq = (const u16*)d_in[2]; p.Wk = (const u16*)d_in[3]; p.Wv = (const u16*)d_in[4]; p.Wo = (const u16*)d_in[5];
  p.wqkv = (const u16*)d_in[8]; p.wo = (const u16*)d_in[9]; p.gw = (const u16*)d_in[10];
  p.out = (u16*)d_out;
  char* ws = (char*)d_ws;
  p.ctrl = (unsigned*)ws;
  size_t a = ((size_t)ws + CTRL_BYTES + (2u << 20) - 1) & ~(size_t)((2u << 20) - 1);
  p.mbox = (char*)a;
  p.data = p.mbox + MBOX_BYTES;
  hipFuncSetAttribute((const void*)fused_kernel, hipFuncAttributeMaxDynamicSharedMemorySize, LDS_BYTES);
  prep_kernel<<<NPAGES + 24, 256, 0, stream>>>(p);
  fused_kernel<<<256, 256, LDS_BYTES, stream>>>(p);
}
```

```cpp
#include <hip/hip_runtime.h>
#include <hip/hip_bf16.h>

typedef unsigned short u16;
typedef unsigned v4u __attribute__((ext_vector_type(4)));
typedef unsigned v2u __attribute__((ext_vector_type(2)));
typedef unsigned v8u __attribute__((ext_vector_type(8)));
typedef __attribute__((ext_vector_type(8))) __bf16 bf16x8;
typedef __attribute__((ext_vector_type(16))) __bf16 bf16x16;
typedef __attribute__((ext_vector_type(4))) float f32x4;
typedef __attribute__((ext_vector_type(4))) short s16x4;
#define LAS __attribute__((address_space(3)))
#define DEV __device__ __forceinline__

namespace {

constexpr int NSEG = 64;
constexpr int NH = 32, NC = 72, NSPARE = 24, NWORK = 128;
constexpr unsigned SENT = 0xFFFFFFFFu;
constexpr unsigned SPIN_CAP = 1u << 22;
constexpr int NREP = 4;
constexpr int STEP_SPAN = NREP * 5 * 16384;
constexpr int NPAGES = 11;
constexpr unsigned MBOX_BYTES = NPAGES * (2u << 20);

constexpr size_t CTRL_BYTES = 98304 + 4096;
constexpr unsigned SZ_WVO = 72u * 294912u, SZ_KX = 64u * 32u * 8192u, SZ_UX = 64u * 72u * 16384u, SZ_PX = 64u * 81920u, SZ_UM = 64u * 72u * 4096u;
constexpr unsigned LW_UX = 0, LW_UM = SZ_UX, LW_PX = SZ_UX + SZ_UM;
constexpr unsigned LR_KX = 0, LR_QX = SZ_KX, LR_WVO = 2 * SZ_KX;
constexpr unsigned SPAN_BYTES = 200u << 20;
static_assert(LW_PX + SZ_PX <= SPAN_BYTES / 2 && LR_WVO + SZ_WVO <= SPAN_BYTES / 2, "span");
constexpr size_t OFF_QKB = 0;
constexpr size_t SZ_QKB = (size_t)2048 * 1152 * 2 + 4096;
constexpr size_t OFF_VTB = OFF_QKB + SZ_QKB;
constexpr size_t SZ_VTB = (size_t)608 * 2048 * 2;
constexpr size_t OFF_OB = OFF_VTB + SZ_VTB;
constexpr size_t SZ_OB = (size_t)2048 * 576 * 2;
constexpr size_t OFF_AB = OFF_OB + SZ_OB;
constexpr size_t OFF_GATE = OFF_AB + SZ_OB;
constexpr size_t SZ_GATE = 8192;
constexpr size_t DATA_BYTES = OFF_GATE + SZ_GATE;

constexpr int C_TMO = 0;
constexpr int C_POL = 64;
constexpr int C_CLR = 128;
constexpr int C_WVO = 192;
constexpr int C_AUX = 256;
constexpr int C_B1 = 320;
constexpr int C_FQ = 384;
constexpr int C_FA = 1152;
constexpr int C_FB = 1280;
constexpr int C_PROG = 512;
constexpr int C_UX = 1024;
constexpr int C_OB = 2048;
constexpr int C_KQ = 4096;
constexpr int C_PX = 8192;
constexpr int C_DBG = 12288;
constexpr int C_UM = 16384;
constexpr int C_CAL = 24576;

struct Params {
  const u16 *x, *mem0, *Wq, *Wk, *Wv, *Wo, *wqkv, *wo, *gw;
  u16* out;
  unsigned* ctrl;
  char* mbox;
  char* span;
  char* data;
};

template <int N> struct IC { static constexpr int value = N; };
DEV unsigned umin(unsigned a, unsigned b) { return a < b ? a : b; }
DEV unsigned umax(unsigned a, unsigned b) { return a > b ? a : b; }
DEV float bf2f(unsigned v) { return __builtin_bit_cast(float, v << 16); }
typedef __attribute__((ext_vector_type(2))) float f32x2;
typedef __attribute__((ext_vector_type(2))) __bf16 bf16x2;
DEV unsigned cvtpk(float lo, float hi) { return __builtin_bit_cast(unsigned, __builtin_convertvector((f32x2){lo, hi}, bf16x2)); }
DEV unsigned long long clk() { unsigned long long t; asm volatile("s_memtime %0\n\ts_waitcnt lgkmcnt(0)" : "=s"(t) :: "memory"); return t; }
DEV unsigned rt() { unsigned long long t; asm volatile("s_memrealtime %0\n\ts_waitcnt lgkmcnt(0)" : "=s"(t) :: "memory"); return (unsigned)t; }
DEV unsigned vo_mem(int rep, unsigned o) { return (unsigned)(rep * 5 + (o >> 13)) * 16384u + (o & 8191u); }
DEV unsigned vo_p(int rep, unsigned o) { return (unsigned)(rep * 5 + 2 + (o >> 13)) * 16384u + (o & 8191u); }
DEV unsigned sbase(int s, unsigned pol) { int pg = s / 6; return (unsigned)pg * (2u << 20) + (unsigned)(s - 6 * pg) * STEP_SPAN + (((pol >> pg) & 1u) << 13); }
DEV unsigned sp_off(unsigned o, unsigned par) { return ((o >> 13) << 14) | ((((o >> 20) ^ par) & 1u) << 13) | (o & 8191u); }
DEV unsigned par_r(unsigned pol) { return (pol ^ (unsigned)NPAGES) & 1u; }
DEV __amdgpu_buffer_rsrc_t mkrsrc(const void* p, unsigned bytes) { return __builtin_amdgcn_make_buffer_rsrc((void*)p, 0, (int)bytes, 0x00020000); }
DEV v4u ld_sc1(__amdgpu_buffer_rsrc_t r, unsigned off) { return __builtin_amdgcn_raw_buffer_load_b128(r, off, 0, 16); }
DEV v2u ld2_sc1(__amdgpu_buffer_rsrc_t r, unsigned off) { return __builtin_amdgcn_raw_buffer_load_b64(r, off, 0, 16); }
DEV unsigned ld1_sc1(__amdgpu_buffer_rsrc_t r, unsigned off) { return __builtin_amdgcn_raw_buffer_load_b32(r, off, 0, 16); }
DEV void st_sc1(v4u v, __amdgpu_buffer_rsrc_t r, unsigned off) { __builtin_amdgcn_raw_buffer_store_b128(v, r, off, 0, 16); }
DEV void st2_sc1(v2u v, __amdgpu_buffer_rsrc_t r, unsigned off) { __builtin_amdgcn_raw_buffer_store_b64(v, r, off, 0, 16); }
DEV void st1_sc1(unsigned v, __amdgpu_buffer_rsrc_t r, unsigned off) { __builtin_amdgcn_raw_buffer_store_b32(v, r, off, 0, 16); }
DEV v4u ld_mb(__amdgpu_buffer_rsrc_t r, unsigned vo, unsigned so) { return __builtin_amdgcn_raw_buffer_load_b128(r, vo, so, 16); }
DEV v2u ld2_mb(__amdgpu_buffer_rsrc_t r, unsigned vo, unsigned so) { return __builtin_amdgcn_raw_buffer_load_b64(r, vo, so, 16); }
DEV void st_mb(v4u v, __amdgpu_buffer_rsrc_t r, unsigned vo, unsigned so) { __builtin_amdgcn_raw_buffer_store_b128(v, r, vo, so, 16); }
DEV void drain() { asm volatile("s_waitcnt vmcnt(0)" ::: "memory"); }
DEV unsigned max4(v4u v) { return umax(umax(v.x, v.y), umax(v.z, v.w)); }
DEV v2u tr_read(const char* smem, unsigned off) {
  s16x4 v = __builtin_amdgcn_ds_read_tr16_b64_v4i16((LAS s16x4*)(smem + off));
  return __builtin_bit_cast(v2u, v);
}
DEV v4u trfrag(const char* smem, int off) {
  v2u x0 = tr_read(smem, off), x1 = tr_read(smem, off + 512);
  return (v4u){x0.x, x0.y, x1.x, x1.y};
}
DEV f32x4 mfma16(v4u a, v4u b, f32x4 c) {
  return __builtin_amdgcn_mfma_f32_16x16x32_bf16(__builtin_bit_cast(bf16x8, a), __builtin_bit_cast(bf16x8, b), c, 0, 0, 0);
}
DEV f32x4 smfma(v4u a, v8u b, f32x4 c, int idx) {
  return __builtin_amdgcn_smfmac_f32_16x16x64_bf16(__builtin_bit_cast(bf16x8, a), __builtin_bit_cast(bf16x16, b), c, idx, 0, 0);
}
template <int CTRL> DEV float dpp(float x) {
  return __builtin_bit_cast(float, __builtin_amdgcn_mov_dpp(__builtin_bit_cast(int, x), CTRL, 0xf, 0xf, true));
}
constexpr int ROR8 = 0x128;
DEV float amax(float a, float b) { float r; asm("v_max_f32 %0, %1, %2\n\ts_nop 1" : "=v"(r) : "v"(a), "v"(b)); return r; }
DEV float amax3(float a, float b, float c) { return fmaxf(fmaxf(a, b), c); }
DEV float xmax(float x) {
  auto s = __builtin_amdgcn_permlane16_swap(__float_as_uint(x), __float_as_uint(x), false, false);
  x = amax(__uint_as_float(s[0]), __uint_as_float(s[1]));
  auto t = __builtin_amdgcn_permlane32_swap(__float_as_uint(x), __float_as_uint(x), false, false);
  return amax(__uint_as_float(t[0]), __uint_as_float(t[1]));
}
DEV float xmax8(float x) {
  float t;
  asm("s_nop 1\n\tv_max_f32_dpp %0, %0, %0 row_ror:8 row_mask:0xf bank_mask:0xf\n\tv_mov_b32 %1, %0\n\ts_nop 1\n\t"
      "v_permlane16_swap_b32 %0, %1\n\tv_max_f32 %0, %0, %1\n\tv_mov_b32 %1, %0\n\ts_nop 1\n\t"
      "v_permlane32_swap_b32 %0, %1\n\tv_max_f32 %0, %0, %1" : "+v"(x), "=&v"(t));
  return x;
}
DEV float xsum(float x) {
  auto s = __builtin_amdgcn_permlane16_swap(__float_as_uint(x), __float_as_uint(x), false, false);
  x = __uint_as_float(s[0]) + __uint_as_float(s[1]);
  auto t = __builtin_amdgcn_permlane32_swap(__float_as_uint(x), __float_as_uint(x), false, false);
  return __uint_as_float(t[0]) + __uint_as_float(t[1]);
}
DEV unsigned aload(unsigned* p) { return __hip_atomic_load(p, __ATOMIC_RELAXED, __HIP_MEMORY_SCOPE_AGENT); }
DEV void aadd(unsigned* p, unsigned v) { __hip_atomic_fetch_add(p, v, __ATOMIC_RELAXED, __HIP_MEMORY_SCOPE_AGENT); }
DEV void astore(unsigned* p, unsigned v) { __hip_atomic_store(p, v, __ATOMIC_RELAXED, __HIP_MEMORY_SCOPE_AGENT); }

DEV bool wait_ge(unsigned* w, unsigned target, unsigned* tmo, unsigned code) {
  for (unsigned spins = 0;; ++spins) {
    if ((unsigned)__builtin_amdgcn_readfirstlane(aload(w)) >= target) { __builtin_amdgcn_fence(__ATOMIC_ACQUIRE, "wavefront"); return true; }
    __builtin_amdgcn_s_sleep(2);
    if (spins > SPIN_CAP) { astore(tmo, code); return false; }
    if ((spins & 255u) == 255u && __builtin_amdgcn_readfirstlane(aload(tmo))) return false;
  }
}
DEV bool wait_slow(unsigned* w, unsigned target, unsigned* tmo, unsigned code) {
  for (unsigned spins = 0;; ++spins) {
    if ((unsigned)__builtin_amdgcn_readfirstlane(aload(w)) >= target) { __builtin_amdgcn_fence(__ATOMIC_ACQUIRE, "wavefront"); return true; }
    __builtin_amdgcn_s_sleep(12);
    if (spins > SPIN_CAP) { astore(tmo, code); return false; }
    if ((spins & 63u) == 63u && __builtin_amdgcn_readfirstlane(aload(tmo))) return false;
  }
}
DEV bool wait_flags(unsigned* w, int n, int st, unsigned target, int lane, unsigned* tmo, unsigned code) {
  for (unsigned spins = 0;; ++spins) {
    unsigned a = aload(w + min(lane, n - 1) * st), b = aload(w + min(lane + 64, n - 1) * st);
    if (__builtin_amdgcn_ballot_w64(a < target || b < target) == 0) { __builtin_amdgcn_fence(__ATOMIC_ACQUIRE, "wavefront"); return true; }
    __builtin_amdgcn_s_sleep(6);
    if (spins > SPIN_CAP) { astore(tmo, code); return false; }
    if ((spins & 63u) == 63u && __builtin_amdgcn_readfirstlane(aload(tmo))) return false;
  }
}
DEV void wg_wait_ge(unsigned* w, unsigned target, unsigned* tmo, unsigned code, int wave) {
  if (wave == 0) wait_ge(w, target, tmo, code);
  __syncthreads();
}
DEV void wg_signal(unsigned* w, int tid) {
  drain();
  __syncthreads();
  if (tid == 0) aadd(w, 1u);
}
DEV void wait_until(unsigned t) {
  for (int guard = 0; guard < 64; ++guard) {
    int rem = (int)(t - rt());
    if (rem <= 0 || rem > 1000) return;
    if (rem <= 16) { int n = (rem * 24) >> 6; for (int i = 0; i < n; ++i) __builtin_amdgcn_s_sleep(1); return; }
    int n = ((rem - 12) * 24) >> 6; if (n > 16) n = 16;
    for (int i = 0; i < n; ++i) __builtin_amdgcn_s_sleep(1);
  }
}

#define DSTAMP(cond, slot)
#define DVAL(cond, slot, v)
#define DPT(k, ref)
#define DPDUMP(slot)
#define RAW_BARRIER() do { asm volatile("s_waitcnt lgkmcnt(0)" ::: "memory"); __builtin_amdgcn_s_barrier(); asm volatile("" ::: "memory"); } while (0)
constexpr int L_MEM = 0;
constexpr int L_KQ = 10240;
constexpr int L_SP = 43008;
constexpr int L_YP = 90112;
constexpr int L_UIMG = 10240;
constexpr int L_RED = 14336;
constexpr int L_DEAD = 51200;
constexpr int L_PXS = 98304;
constexpr int L_STAGE = 53248;
constexpr int L_X = 0;
constexpr int XBUF = 36864;
constexpr int L_V = 110592;
constexpr int L_WST = 118784;
constexpr int LDS_BYTES = 160 * 1024;

struct MemAddr { unsigned o0, o1, o2, o3; };
DEV MemAddr mem_addr(int rep, int wave, int lane) {
  MemAddr a;
  a.o0 = vo_mem(rep, wave * 3072 + lane * 16);
  a.o1 = vo_mem(rep, wave * 3072 + 1024 + lane * 16);
  a.o2 = vo_mem(rep, wave * 3072 + 2048 + lane * 16);
  a.o3 = vo_mem(rep, 9216);
  return a;
}
DEV unsigned gather_mem(const Params& p, __amdgpu_buffer_rsrc_t rm, char* smem, int wave, int lane, const MemAddr& a, unsigned sb, v4u& v0, v4u& v1, v4u& v2) {
  v0 = ld_mb(rm, a.o0, sb); v1 = ld_mb(rm, a.o1, sb); v2 = ld_mb(rm, a.o2, sb);
  unsigned mx = umax(umax(max4(v0), max4(v1)), max4(v2));
  unsigned spins = 0;
  if (__builtin_expect(__builtin_amdgcn_ballot_w64(mx == SENT) != 0, 0)) {
    for (;;) {
      asm volatile("" ::: "memory");
      v0 = ld_mb(rm, a.o0, sb); v1 = ld_mb(rm, a.o1, sb); v2 = ld_mb(rm, a.o2, sb);
      mx = umax(umax(max4(v0), max4(v1)), max4(v2));
      ++spins;
      if (__builtin_amdgcn_ballot_w64(mx == SENT) == 0) break;
      if (spins > SPIN_CAP) { astore(p.ctrl + C_TMO, 1u); *(LAS unsigned*)(smem + L_DEAD) = 1; break; }
      if ((spins & 1023) == 0 && __builtin_amdgcn_readfirstlane(aload(p.ctrl + C_TMO))) { *(LAS unsigned*)(smem + L_DEAD) = 1; break; }
    }
  }
  if (wave < 3) {
    char* d = smem + L_MEM + wave * 3072 + lane * 16;
    *(v4u*)d = v0; *(v4u*)(d + 1024) = v1; *(v4u*)(d + 2048) = v2;
  }
  return spins;
}
DEV v2u sched_fix(__amdgpu_buffer_rsrc_t rm, unsigned off, unsigned sb, v2u v) {
  if (__builtin_expect((unsigned)__builtin_amdgcn_readfirstlane(umax(v.x, v.y)) == SENT, 0)) {
    for (int i = 0; i < 8 && (unsigned)__builtin_amdgcn_readfirstlane(umax(v.x, v.y)) == SENT; ++i) {
      asm volatile("" ::: "memory");
      v = ld2_mb(rm, off, sb);
    }
    if ((unsigned)__builtin_amdgcn_readfirstlane(umax(v.x, v.y)) == SENT) { unsigned n = rt(); v = (v2u){n, n}; }
  }
  return v;
}
DEV void zero_acc(f32x4 (&acc)[4]) {
#pragma unroll
  for (int i = 0; i < 4; ++i) acc[i] = (f32x4){0, 0, 0, 0};
  asm volatile("" : "+v"(acc[0]), "+v"(acc[1]), "+v"(acc[2]), "+v"(acc[3]));
}
DEV void proj36(const char* smem, const v8u (&w)[4][9], int lane, f32x4 (&acc)[4]) {
  const int idx = (lane & 8) ? 0xEEEEEEEE : 0x44444444;
  v4u a[9];
  __builtin_amdgcn_sched_barrier(0);
#pragma unroll
  for (int t = 0; t < 9; ++t) a[t] = *(const v4u*)(smem + L_MEM + t * 1024 + lane * 16);
  __builtin_amdgcn_sched_barrier(0);
#pragma unroll
  for (int t = 0; t < 9; ++t) {
#pragma unroll
    for (int i = 0; i < 4; ++i) acc[i] = smfma(a[t], w[i][t], acc[i], idx);
  }
  __builtin_amdgcn_sched_barrier(0);
}
DEV v2u pairsum(f32x4 a0, f32x4 a1) {
  float r[4];
#pragma unroll
  for (int i = 0; i < 4; ++i) {
    auto sw = __builtin_amdgcn_permlane32_swap(__float_as_uint(a0[i]), __float_as_uint(a1[i]), false, false);
    r[i] = __uint_as_float(sw[0]) + __uint_as_float(sw[1]);
  }
  return (v2u){cvtpk(r[0], r[1]), cvtpk(r[2], r[3])};
}
DEV unsigned read_pol(const Params& p) {
  return __builtin_amdgcn_readfirstlane(aload(p.ctrl + C_POL)) & 0xfffu;
}

DEV void dma16(const void* g, unsigned lds) {
  unsigned keep;
  asm volatile("s_mov_b32 %0, m0\n\ts_mov_b32 m0, %2\n\ts_nop 0\n\tglobal_load_lds_dwordx4 %1, off\n\ts_mov_b32 m0, %0"
               : "=&s"(keep) : "v"(g), "s"(lds) : "memory");
}
DEV unsigned lds_addr(const char* p) { return __builtin_amdgcn_readfirstlane((unsigned)(size_t)(LAS const char*)p); }
template <int N> DEV void wait_vm() { asm volatile("s_waitcnt vmcnt(%0)" :: "n"(N) : "memory"); }
template <int RB, class FS, class FU>
DEV void staged_load(char* smem, int wave, int lane, FS src, FU use) {
  constexpr int CH = RB / 16, BLK = 64 * RB, PW = BLK / 4096;
  constexpr int NB = (147456 / BLK) < 9 ? (147456 / BLK) : 9;
  const unsigned lb = lds_addr(smem) + wave * PW * 1024;
  auto issue = [&](int t) {
#pragma unroll
    for (int i = 0; i < PW; ++i) {
      const int ch = (wave * PW + i) * 64 + lane;
      dma16(src(64 * t + ch / CH, ch % CH), lb + (t % NB) * BLK + i * 1024);
    }
  };
  RAW_BARRIER();
#pragma unroll
  for (int t = 0; t < NB; ++t) issue(t);
#pragma unroll
  for (int t = 0; t < 9; ++t) {
    constexpr int dummy = 0; (void)dummy;
    const int younger = (t + NB <= 9 ? NB - 1 : 8 - t) * PW;
    if (younger >= 24) wait_vm<24>(); else if (younger >= 16) wait_vm<16>(); else if (younger >= 14) wait_vm<14>(); else if (younger >= 12) wait_vm<12>();
    else if (younger >= 10) wait_vm<10>(); else if (younger >= 8) wait_vm<8>(); else if (younger >= 6) wait_vm<6>(); else if (younger >= 4) wait_vm<4>();
    else if (younger >= 2) wait_vm<2>(); else wait_vm<0>();
    RAW_BARRIER();
    use(t, (t % NB) * BLK);
    if (t + NB < 9) { RAW_BARRIER(); issue(t + NB); }
  }
  RAW_BARRIER();
}

DEV f32x4 mfma16k16(v2u a, v2u b, f32x4 c) {
  return __builtin_amdgcn_mfma_f32_16x16x16bf16_1k(__builtin_bit_cast(s16x4, a), __builtin_bit_cast(s16x4, b), c, 0, 0, 0);
}
template <int WV>
DEV void head_loop(const Params& p, char* smem, int h, int lane, const v8u (&w)[4][9], unsigned pol) {
  constexpr int wave = WV;
  __amdgpu_buffer_rsrc_t rm = mkrsrc(p.mbox, MBOX_BYTES);
  __amdgpu_buffer_rsrc_t rs = mkrsrc(p.span, SPAN_BYTES);
  const unsigned pr = par_r(pol), pw = pr ^ 1u;
  const float SC = 0.08838834764831845f * 1.4426950408889634f;
  const int g = lane >> 4, qi = lane & 15, hi = (lane >> 3) & 1;
  const float NEG = -1e30f;
  const float c0 = (wave == 0 && g >= 2) ? NEG : 0.f;
  const int rep = h & 3;
  v2u selq[2], selk[2];
#pragma unroll
  for (int tl = 0; tl < 2; ++tl) {
    unsigned e[4];
#pragma unroll
    for (int jj = 0; jj < 4; ++jj) e[jj] = (4 * g + jj == 8 * tl + (qi & 7)) ? 0x3F80u : 0u;
    selq[tl] = (v2u){e[0] | (e[1] << 16), e[2] | (e[3] << 16)};
    selk[tl] = hi ? (v2u){0u, 0u} : selq[tl];
  }
  const f32x4 zero4 = {0, 0, 0, 0}, c04 = {c0, c0, c0, c0};
  const MemAddr ma = mem_addr(rep, wave == 3 ? 0 : wave, lane);
  unsigned adx = hi ? vo_p(0, 16384 + (h >> 3) * 1024 + (h & 3) * 256 + ((h >> 2) & 1) * 128 + (qi & 7) * 16)
                    : vo_p(0, (h >> 1) * 1024 + g * 256 + (h & 1) * 128 + (qi & 7) * 16);
  if (hi && g != 0) adx = 0x70000000u;
  auto px_out = [&](int sp) {
    const char* q = smem + L_PXS + (sp & 1) * 2560;
    v4u a0 = *(const v4u*)(q + lane * 16), a1 = *(const v4u*)(q + 1024 + lane * 16), a2 = *(const v4u*)(q + 2048 + (lane & 31) * 16);
    st_sc1(a0, rs, sp_off(LW_PX + sp * 81920 + h * 1024 + lane * 16, pw));
    st_sc1(a1, rs, sp_off(LW_PX + sp * 81920 + (32 + h) * 1024 + lane * 16, pw));
    if (lane < 32) st_sc1(a2, rs, sp_off(LW_PX + sp * 81920 + 65536 + (((lane >> 4) * 8 + (h >> 2)) * 64 + (h & 3) * 16 + (lane & 15)) * 16, pw));
  };
  unsigned tM = rt();
  for (int s = 0; s < NSEG; ++s) {
    const char* kq = smem + L_KQ + (s & 1) * 16384 + lane * 16;
    v4u kxw[2];
    kxw[0] = *(const v4u*)(kq + wave * 1024); kxw[1] = *(const v4u*)(kq + (4 + wave) * 1024);
    const unsigned sb = __builtin_amdgcn_readfirstlane(sbase(s, pol));
    f32x4 acc[4]; zero_acc(acc);
    f32x4 t0, t1, t2, dq0, dq1, dk0, dk1;
    asm volatile("" : "+v"(kxw[0]), "+v"(kxw[1]));
    const unsigned tM0 = tM; (void)tM0;
    DPT(10, tM0);
    constexpr int T0 = (wave == 3) ? 0 : 3 * wave;
    const int sidx = (lane & 8) ? 0xEEEEEEEE : 0x44444444;
    {
      v4u g3[3];
      wait_until(tM);
      gather_mem(p, rm, smem, wave, lane, ma, sb, g3[0], g3[1], g3[2]);
      __builtin_amdgcn_sched_barrier(0);
#pragma unroll
      for (int i = 0; i < 4; ++i) acc[i] = smfma(g3[0], w[i][T0], acc[i], sidx);
      __builtin_amdgcn_sched_barrier(0);
      DPT(11, tM0);
      RAW_BARRIER();
      v4u a[6];
#pragma unroll
      for (int j = 0; j < 6; ++j) a[j] = *(const v4u*)(smem + L_MEM + ((T0 + 3 + j) % 9) * 1024 + lane * 16);
      __builtin_amdgcn_sched_barrier(0);
#pragma unroll
      for (int j = 1; j < 3; ++j)
#pragma unroll
        for (int i = 0; i < 4; ++i) acc[i] = smfma(g3[j], w[i][T0 + j], acc[i], sidx);
#pragma unroll
      for (int j = 0; j < 6; ++j)
#pragma unroll
        for (int i = 0; i < 4; ++i) acc[i] = smfma(a[j], w[i][(T0 + 3 + j) % 9], acc[i], sidx);
      __builtin_amdgcn_sched_barrier(0);
    }
    DPT(12, tM0);
    v2u sch = ld2_mb(rm, ma.o3, sb);
    v2u pkq = pairsum(acc[0], acc[1]), pkk = pairsum(acc[2], acc[3]);
    asm volatile("" : "+v"(pkq), "+v"(pkk));
    DPT(13, tM0);
    asm("s_nop 1\n\tv_mfma_f32_16x16x16_bf16 %0, %4, %6, 0\n\tv_mfma_f32_16x16x16_bf16 %1, %4, %7, 0\n\t"
        "v_mfma_f32_16x16x16_bf16 %2, %5, %8, 0\n\tv_mfma_f32_16x16x16_bf16 %3, %5, %9, 0\n\ts_nop 7"
        : "=&v"(dq0), "=&v"(dq1), "=&v"(dk0), "=&v"(dk1) : "v"(pkq), "v"(pkk), "v"(selq[0]), "v"(selq[1]), "v"(selk[0]), "v"(selk[1]));
    v4u qf = {cvtpk(dq0[0], dq0[1]), cvtpk(dq0[2], dq0[3]), cvtpk(dq1[0], dq1[1]), cvtpk(dq1[2], dq1[3])};
    v4u kf = {cvtpk(dk0[0], dk0[1]), cvtpk(dk0[2], dk0[3]), cvtpk(dk1[0], dk1[1]), cvtpk(dk1[2], dk1[3])};
    asm("s_nop 1\n\tv_mfma_f32_16x16x32_bf16 %0, %3, %6, 0\n\tv_mfma_f32_16x16x32_bf16 %1, %4, %6, 0\n\t"
        "v_mfma_f32_16x16x32_bf16 %2, %5, %6, %7\n\ts_nop 7"
        : "=&v"(t1), "=&v"(t2), "=&v"(t0) : "v"(kxw[0]), "v"(kxw[1]), "v"(kf), "v"(qf), "v"(c04));
    f32x4 va = {hi ? t2[0] : t1[0], hi ? t2[1] : t1[1], hi ? t2[2] : t1[2], hi ? t2[3] : t1[3]};
    f32x2 vb = {hi ? t0[2] : t0[0], hi ? t0[3] : t0[1]};
    asm volatile("" : "+v"(va), "+v"(vb));
    DPT(14, tM0);
    *(v4u*)(smem + L_YP + wave * 1024 + lane * 16) = kf;
    if (wave > 0) {
      *(f32x4*)(smem + L_SP + (wave - 1) * 1536 + lane * 16) = va;
      *(f32x2*)(smem + L_SP + (wave - 1) * 1536 + 1024 + lane * 8) = vb;
    }
    RAW_BARRIER();
    DPT(15, tM0);
    if (wave == 0) {
      f32x4 pa[3]; f32x2 pb[3];
#pragma unroll
      for (int i = 0; i < 3; ++i) {
        pa[i] = *(const f32x4*)(smem + L_SP + i * 1536 + lane * 16);
        pb[i] = *(const f32x2*)(smem + L_SP + i * 1536 + 1024 + lane * 8);
      }
      __builtin_amdgcn_sched_barrier(0);
      va = (va + pa[0]) + (pa[1] + pa[2]);
      vb = (vb + pb[0]) + (pb[1] + pb[2]);
      float m = fmaxf(fmaxf(fmaxf(va[0], va[1]), va[2]), fmaxf(fmaxf(va[3], vb[0]), vb[1]));
      m = xmax8(m);
      const float ms = -m * SC;
      float v0 = __builtin_amdgcn_exp2f(fmaf(va[0], SC, ms)), v1 = __builtin_amdgcn_exp2f(fmaf(va[1], SC, ms)), v2 = __builtin_amdgcn_exp2f(fmaf(va[2], SC, ms));
      float v3 = __builtin_amdgcn_exp2f(fmaf(va[3], SC, ms)), v4 = __builtin_amdgcn_exp2f(fmaf(vb[0], SC, ms)), v5 = __builtin_amdgcn_exp2f(fmaf(vb[1], SC, ms));
      float l = ((v0 + v1) + (v2 + v3)) + (v4 + v5);
      l += dpp<ROR8>(l);
      l = xsum(l);
      float inv = __builtin_amdgcn_rcpf(l);
      v2u px = {umin(cvtpk(v0 * inv, v1 * inv), 0xFFFFFFFEu), umin(cvtpk(v2 * inv, v3 * inv), 0xFFFFFFFEu)};
      unsigned pm = umin(cvtpk(v4 * inv, v5 * inv), 0xFFFFFFFEu);
      unsigned ox = __builtin_amdgcn_mov_dpp(px.x, ROR8, 0xf, 0xf, true), oy = __builtin_amdgcn_mov_dpp(px.y, ROR8, 0xf, 0xf, true);
      unsigned pmo = __builtin_amdgcn_mov_dpp(pm, ROR8, 0xf, 0xf, true);
      auto s1 = __builtin_amdgcn_permlane16_swap(pm, pm, false, false);
      auto s2 = __builtin_amdgcn_permlane16_swap(pmo, pmo, false, false);
      v4u o;
      o.x = hi ? pmo : px.x; o.y = hi ? pm : px.y; o.z = hi ? s2[1] : ox; o.w = hi ? s1[1] : oy;
#pragma unroll
      for (int rp = 0; rp < NREP; ++rp) st_mb(o, rm, adx, sb + rp * 81920);
    }
    DPT(16, tM0);
    if (wave == 3) {
      if (s > 1 && lane == 0 && (!(s & 1) || s >= 58)) astore(p.ctrl + C_PX + h * 32, (unsigned)(s - 1));
      if (s > 0) px_out(s - 1);
      const int sn = min(s + 1, NSEG - 1);
      const char* srck = p.span + sp_off(LR_KX + (sn * 32 + h) * 8192, pr) + lane * 16;
      const char* srcq = p.span + sp_off(LR_QX + (sn * 32 + h) * 8192, pr) + lane * 16;
      char* dst = smem + L_KQ + ((s + 1) & 1) * 16384;
      const unsigned ldst = __builtin_amdgcn_readfirstlane((unsigned)(size_t)(LAS char*)dst);
#pragma unroll
      for (int i = 0; i < 16; ++i) {
        unsigned keep;
        const char* gp = ((i >> 3) ? srcq : srck) + (i & 7) * 1024;
        asm volatile("s_mov_b32 %0, m0\n\ts_mov_b32 m0, %2\n\ts_nop 0\n\tglobal_load_lds_dwordx4 %1, off sc1\n\ts_mov_b32 m0, %0"
                     : "=&s"(keep) : "v"(gp), "s"(ldst + i * 1024) : "memory");
      }
      if (!wait_ge(p.ctrl + C_KQ + min(s + 2, NSEG - 1) * 32 + h, 1u, p.ctrl + C_TMO, 2u)) *(LAS unsigned*)(smem + L_DEAD) = 1;
      drain();
    }
    if (wave == 1 || wave == 2) {
      constexpr int qt = (wave == 2) ? 1 : 0;
      f32x4 x1 = zero4, x2 = zero4, y = zero4;
#pragma unroll
      for (int u = 0; u < 4; ++u) {
        v4u q = *(const v4u*)(kq + 8192 + (qt * 4 + u) * 1024);
        x1 = mfma16(*(const v4u*)(kq + u * 1024), q, x1); x2 = mfma16(*(const v4u*)(kq + (4 + u) * 1024), q, x2);
        y = mfma16(*(const v4u*)(smem + L_YP + u * 1024 + lane * 16), q, y);
      }
      float v[12];
      const int tq = 16 * qt + qi;
#pragma unroll
      for (int r = 0; r < 4; ++r) {
        v[r] = (g < 2) ? y[r] * SC : NEG;
        v[4 + r] = (4 * g + r <= tq) ? x1[r] * SC : NEG;
        v[8 + r] = (16 + 4 * g + r <= tq) ? x2[r] * SC : NEG;
      }
      float m = amax3(amax3(v[0], v[1], v[2]), amax3(v[3], v[4], v[5]), fmaxf(amax3(v[6], v[7], v[8]), amax3(v[9], v[10], v[11])));
      m = xmax(m);
      float l = 0;
#pragma unroll
      for (int i = 0; i < 12; ++i) { v[i] = __builtin_amdgcn_exp2f(v[i] - m); l += v[i]; }
      l = xsum(l);
      float inv = __builtin_amdgcn_rcpf(l);
#pragma unroll
      for (int i = 0; i < 12; ++i) v[i] *= inv;
      v4u px = {cvtpk(v[4], v[5]), cvtpk(v[6], v[7]), cvtpk(v[8], v[9]), cvtpk(v[10], v[11])};
      v2u pm = {cvtpk(v[0], v[1]), cvtpk(v[2], v[3])};
      *(v4u*)(smem + L_PXS + (s & 1) * 2560 + qt * 1024 + lane * 16) = px;
      if (g < 2) *(v2u*)(smem + L_PXS + (s & 1) * 2560 + 2048 + qt * 256 + qi * 16 + g * 8) = pm;
    }
    sch = sched_fix(rm, ma.o3, sb, sch);
    tM = __builtin_amdgcn_readfirstlane(sch.y);
    RAW_BARRIER();
    if (*(volatile LAS unsigned*)(smem + L_DEAD)) break;
  }
  if (wave == 3) { px_out(NSEG - 1); drain(); if (lane == 0) astore(p.ctrl + C_PX + h * 32, (unsigned)NSEG); }
}

DEV void head_role(const Params& p, char* smem, int h, int wave, int lane, int tid) {
  v8u w[4][9];
  {
    const int gB = lane >> 4, q4 = (lane & 15) >> 2, p4 = lane & 3;
    staged_load<512>(smem, wave, lane,
      [&](int row, int cc) { return (cc < 16 ? p.Wq : p.Wk) + (size_t)row * 4096 + h * 128 + (cc & 15) * 8; },
      [&](int t, int base) {
#pragma unroll
        for (int tl = 0; tl < 4; ++tl) {
          v8u ww;
#pragma unroll
          for (int X = 0; X < 2; ++X)
#pragma unroll
            for (int half = 0; half < 2; ++half) {
              int row0 = 8 * (4 * half + 2 * X + (gB >> 1)) + 4 * (gB & 1);
              v2u v = tr_read(smem, base + (row0 + q4) * 512 + ((tl >> 1) * 128 + 32 * wave + 16 * (tl & 1) + 4 * p4) * 2);
              ww[4 * X + half] = v.x; ww[4 * X + 2 + half] = v.y;
            }
          if (tl >= 2 || (tl == 1 && t < 3)) asm volatile("" : "+a"(ww));
          w[tl][t] = ww;
        }
      });
  }
  DSTAMP(h == 0 && wave == 0, 0);
  if (tid == 0) *(unsigned*)(smem + L_DEAD) = 0;
  if (wave == 0) { wait_ge(p.ctrl + C_CLR, NSPARE, p.ctrl + C_TMO, 3u); wait_ge(p.ctrl + C_KQ + h, 1u, p.ctrl + C_TMO, 4u); wait_ge(p.ctrl + C_KQ + 32 + h, 1u, p.ctrl + C_TMO, 4u); }
  __syncthreads();
  const unsigned pol = read_pol(p);
  {
    __amdgpu_buffer_rsrc_t rs = mkrsrc(p.span, SPAN_BYTES);
    v4u v[4];
#pragma unroll
    for (int i = 0; i < 4; ++i) { int f = wave * 4 + i; v[i] = ld_sc1(rs, sp_off((wave < 2 ? LR_KX : LR_QX) + h * 8192 + (f & 7) * 1024 + lane * 16, par_r(pol))); }
#pragma unroll
    for (int i = 0; i < 4; ++i) *(v4u*)(smem + L_KQ + (wave * 4 + i) * 1024 + lane * 16) = v[i];
  }
  __syncthreads();
  DSTAMP(h == 0 && wave == 0, 1);
  if (wave == 0) head_loop<0>(p, smem, h, lane, w, pol);
  else if (wave == 1) head_loop<1>(p, smem, h, lane, w, pol);
  else if (wave == 2) head_loop<2>(p, smem, h, lane, w, pol);
  else head_loop<3>(p, smem, h, lane, w, pol);
}

constexpr int L_WOST = 0;
constexpr int L_AST = 118784;
DEV void load_wo(const Params& p, char* smem, int h, int par, int wave, int lane, int tid, v4u (&wo)[9][4]) {
  constexpr int RS = 1152;
  const int g = lane >> 4, q4 = (lane & 15) >> 2, p4 = lane & 3;
  const char* src = (const char*)(p.Wo + (size_t)h * 128 * 576) + lane * 16;
  RAW_BARRIER();
#pragma unroll
  for (int hf = 0; hf < 2; ++hf)
#pragma unroll
    for (int i = 0; i < 18; ++i) {
      const int pc = (hf ^ par) * 72 + wave * 18 + i;
      dma16(src + pc * 1024, lds_addr(smem) + pc * 1024);
    }
#pragma unroll
  for (int hf = 0; hf < 2; ++hf) {
    if (hf == 0) { wait_vm<0>(); RAW_BARRIER(); }
#pragma unroll
    for (int tl = 0; tl < 9; ++tl)
#pragma unroll
      for (int uu = 0; uu < 2; ++uu) {
        int a0 = (64 * hf + 32 * uu + 4 * g + q4) * RS + ((wave * 9 + tl) * 16 + 4 * p4) * 2;
        v2u x0 = tr_read(smem, a0), x1 = tr_read(smem, a0 + 16 * RS);
        wo[tl][2 * hf + uu] = (v4u){x0.x, x0.y, x1.x, x1.y};
      }
  }
  RAW_BARRIER();
}
struct ABlk { v4u v[4]; };
DEV void wv_block_load(const Params& p, int h, int t, int tid, ABlk& b) {
#pragma unroll
  for (int i = 0; i < 4; ++i) { int ch = tid + 256 * i; b.v[i] = *(const v4u*)(p.Wv + (size_t)(64 * t + (ch >> 4)) * 4096 + h * 128 + (ch & 15) * 8); }
}
template <int TL0, int TL1>
DEV void wvo_unit(const Params& p, char* smem, int h, int t, int slot, int wave, int lane, int tid, const ABlk& blk, const v4u (&wo)[9][4], unsigned pr) {
  const int g = lane >> 4;
  __amdgpu_buffer_rsrc_t rw = mkrsrc(p.span, SPAN_BYTES);
  char* ab = smem + L_AST + slot * 17408;
#pragma unroll
  for (int i = 0; i < 4; ++i) { int ch = tid + 256 * i; *(v4u*)(ab + (ch >> 4) * 272 + (ch & 15) * 16) = blk.v[i]; }
  RAW_BARRIER();
  v4u a[4][4];
#pragma unroll
  for (int tt = 0; tt < 4; ++tt)
#pragma unroll
    for (int u = 0; u < 4; ++u) {
      const char* src = ab + (16 * tt + (lane & 15)) * 272 + (32 * u + 4 * g) * 2;
      v2u x0 = *(const v2u*)src, x1 = *(const v2u*)(src + 32);
      a[tt][u] = (v4u){x0.x, x0.y, x1.x, x1.y};
    }
#pragma unroll
  for (int tl = TL0; tl < TL1; ++tl) {
    f32x4 d[4];
#pragma unroll
    for (int tt = 0; tt < 4; ++tt) {
      d[tt] = (f32x4){0, 0, 0, 0};
#pragma unroll
      for (int u = 0; u < 4; ++u) d[tt] = mfma16(a[tt][u], wo[tl][u], d[tt]);
    }
    const int n = (wave * 9 + tl) * 16 + (lane & 15), c = n >> 3;
    unsigned off = sp_off(LR_WVO + ((c * 16 + (h >> 1)) * 9 + t) * 2048 + (g * 16 + (h & 1) * 8 + (n & 7)) * 16, pr);
    st_sc1((v4u){cvtpk(d[0][0], d[0][1]), cvtpk(d[2][0], d[2][1]), cvtpk(d[0][2], d[0][3]), cvtpk(d[2][2], d[2][3])}, rw, off);
    st_sc1((v4u){cvtpk(d[1][0], d[1][1]), cvtpk(d[3][0], d[3][1]), cvtpk(d[1][2], d[1][3]), cvtpk(d[3][2], d[3][3])}, rw, off + 1024);
  }
}

#define SCHED_A0 124
#define SCHED_B0 70
#define LATE0 78
#define DEC_EVERY 5
DEV void col_role(const Params& p, char* smem, int c, int wave, int lane, int tid) {
  __amdgpu_buffer_rsrc_t rm = mkrsrc(p.mbox, MBOX_BYTES);
  __amdgpu_buffer_rsrc_t rs = mkrsrc(p.span, SPAN_BYTES);
  if (c < 64) {
    const int hh = ((c >> 3) << 2) | (c & 3), par = (c >> 2) & 1;
    ABlk b0, b1, b2;
    v4u wo[9][4];
    wv_block_load(p, hh, par, tid, b0); wv_block_load(p, hh, par + 2, tid, b1); wv_block_load(p, hh, par + 4, tid, b2);
    load_wo(p, smem, hh, par, wave, lane, tid, wo);
    wg_wait_ge(p.ctrl + C_POL, 0x1000u, p.ctrl + C_TMO, 9u, wave);
    const unsigned pr0 = par_r(read_pol(p));
    wvo_unit<0, 9>(p, smem, hh, par, 0, wave, lane, tid, b0, wo, pr0);
    wvo_unit<0, 9>(p, smem, hh, par + 2, 1, wave, lane, tid, b1, wo, pr0);
    wvo_unit<0, 9>(p, smem, hh, par + 4, 0, wave, lane, tid, b2, wo, pr0);
    wg_signal(p.ctrl + C_WVO + 32, tid);
  }
  if (tid == 0) *(unsigned*)(smem + L_DEAD) = 0;
  if (wave == 0) wait_ge(p.ctrl + C_CLR, NSPARE, p.ctrl + C_TMO, 5u);
  __syncthreads();
  DSTAMP(c == 0 && wave == 0, 2);
  const unsigned pol = read_pol(p);
  const int rep = c & 3;
  const unsigned slice = (c >> 3) * 1024 + ((c & 3) * 16 + ((c >> 2) & 1) * 8) * 16;
  if (wave == 0) {
    const unsigned sb0 = __builtin_amdgcn_readfirstlane(sbase(0, pol));
    if (lane < 8 * NREP) {
      int r = lane & 7, rp = lane >> 3;
      v4u v = *(const v4u*)(p.mem0 + r * 576 + 8 * c);
      v.x = umin(v.x, 0xFFFFFFFEu); v.y = umin(v.y, 0xFFFFFFFEu); v.z = umin(v.z, 0xFFFFFFFEu); v.w = umin(v.w, 0xFFFFFFFEu);
      st_mb(v, rm, vo_mem(rp, slice + r * 16), sb0);
    }
    if (c == 0) {
      unsigned now = rt(); if (now == SENT) now = 0;
      if (lane < NREP) st_mb((v4u){now, now, 0u, 0u}, rm, vo_mem(lane, 9216), sb0);
    }
    wait_ge(p.ctrl + C_WVO, 64u, p.ctrl + C_TMO, 6u);
  }
  __syncthreads();
  v8u w[4][9];
#pragma unroll
  for (int t = 8; t >= 0; --t) {
    if (t == 5) { wg_wait_ge(p.ctrl + C_WVO + 32, 64u, p.ctrl + C_TMO, 6u, wave); DSTAMP(c == 0 && wave == 0, 3); }
#pragma unroll
    for (int tl = 0; tl < 4; ++tl) {
      unsigned off = sp_off(LR_WVO + ((c * 16 + wave * 4 + tl) * 9 + t) * 2048, par_r(pol)) + lane * 16;
      v4u x0 = ld_sc1(rs, off), x1 = ld_sc1(rs, off + 1024);
      w[tl][t] = __builtin_shufflevector(x0, x1, 0, 1, 2, 3, 4, 5, 6, 7);
    }
  }
#pragma unroll
  for (int tl = 1; tl < 4; ++tl)
#pragma unroll
    for (int t = 0; t < 9; ++t)
      if (tl >= 2 || t < 3) asm volatile("" : "+a"(w[tl][t]));
  auto loop = [&](auto WV) {
  constexpr int wave = decltype(WV)::value;
  const int g = lane >> 4, half = (lane >> 3) & 1, n7 = lane & 7;
  const int uw_off = L_UIMG + (((2 * (4 * wave + (lane >> 5)) + ((lane & 15) >> 3)) * 8 + (lane & 7)) * 8 + 4 * ((lane >> 4) & 1)) * 2;
  const int red_w = ((g >> 1) == half) ? L_RED + (((wave * 2 + half) * 8 + n7) * 8 + 4 * (g & 1)) * 4 : L_RED + 2048 + lane * 16;
  const int red_r = L_RED + (((lane >> 1) & 7) * 8 + 4 * (lane & 1)) * 4;
  const MemAddr ma = mem_addr(rep, wave, lane);
  const unsigned po = vo_p(rep, wave * 4096 + lane * 16), pm_o = vo_p(rep, 16384 + wave * 1024 + lane * 16);
  const unsigned ado = (lane & 1) ? 0x70000000u : vo_mem(lane >> 4, slice + ((lane >> 1) & 7) * 16);
  const unsigned pw = par_r(pol) ^ 1u;
  DSTAMP(c == 0 && wave == 0, 20);
  wait_ge(p.ctrl + C_UX, 32u, p.ctrl + C_TMO, 7u);
  DSTAMP(c == 0 && wave == 0, 21);
  int rdy = 1;
  unsigned tM = rt();
  int schedA = SCHED_A0, schedB = SCHED_B0, streak = 0, flip = 0; bool prevLate = true; int lateM = LATE0;
  unsigned sb = __builtin_amdgcn_readfirstlane(sbase(0, pol));
  for (int s = 0; s < NSEG; ++s) {
    f32x4 acc[4]; zero_acc(acc);
    unsigned mfail = 0;
    const unsigned tM0 = tM; (void)tM0;
    DPT(0, tM0);
    if (wave < 3) { wait_until(tM); v4u g0, g1, g2; mfail = gather_mem(p, rm, smem, wave, lane, ma, sb, g0, g1, g2); }
    DPT(1, tM0);
    __syncthreads();
    DPT(2, tM0);
    v2u sch = ld2_mb(rm, ma.o3, sb);
    unsigned ofail = SENT;
    if (wave == 3 && c == 0) ofail = __builtin_amdgcn_raw_buffer_load_b32(rm, vo_mem(0, 9216 + 128 * (1 + (lane % 3))), sb, 16);
    v4u ux[4];
    const unsigned uso = __builtin_amdgcn_readfirstlane(sp_off(LW_UX + (s * 72 + c) * 16384 + wave * 4096, pw));
#pragma unroll
    for (int q = 0; q < 4; ++q) ux[q] = __builtin_amdgcn_raw_buffer_load_b128(rs, q * 1024 + lane * 16, uso, 16);
    unsigned fl2 = 32u;
    const bool chk = rdy < s + 3 && rdy < NSEG;
    if (__builtin_expect(chk, 0)) fl2 = aload(p.ctrl + C_UX + lane);
    proj36(smem, w, lane, acc);
    v2u pk0 = pairsum(acc[0], acc[1]), pk1 = pairsum(acc[2], acc[3]);
    asm volatile("" : "+v"(pk0), "+v"(pk1));
    DPT(3, tM0);
    *(v2u*)(smem + uw_off) = pk0;
    *(v2u*)(smem + uw_off + 512) = pk1;
    v4u ub = *(const v4u*)(smem + L_UIMG + ((8 * wave + 4 * half + g) * 8 + n7) * 16);
    const unsigned sbn = __builtin_amdgcn_readfirstlane(sbase(s + 1, pol));
    if (s < NSEG - 1) {
      asm volatile("s_waitcnt vmcnt(0)" : "+v"(ux[0]), "+v"(ux[1]), "+v"(ux[2]), "+v"(ux[3]), "+v"(ub), "+v"(fl2) :: "memory");
      if (__builtin_expect(chk, 0)) {
        unsigned long long nb = __builtin_amdgcn_ballot_w64(fl2 < 32u);
        rdy = nb ? (int)__builtin_ctzll(nb) : NSEG;
        __builtin_amdgcn_fence(__ATOMIC_ACQUIRE, "wavefront");
        if (rdy < s + 2) {
          if (!wait_ge(p.ctrl + C_UX + s + 1, 32u, p.ctrl + C_TMO, 7u)) *(LAS unsigned*)(smem + L_DEAD) = 1;
          rdy = s + 2;
        }
      }
      DPT(4, tM0);
      sch = sched_fix(rm, ma.o3, sb, sch);
      const unsigned tP = __builtin_amdgcn_readfirstlane(sch.x);
      tM = __builtin_amdgcn_readfirstlane(sch.y);
      f32x4 d = {0, 0, 0, 0};
      asm volatile("" : "+v"(d));
      DPT(5, tM0);
      wait_until(tP);
      DPT(6, tP);
      unsigned pfail = 0;
      {
        v4u v[5];
#pragma unroll
        for (int q = 0; q < 4; ++q) v[q] = ld_mb(rm, po + q * 1024, sb);
        v[4] = ld_mb(rm, pm_o, sb);
        unsigned mx = 0;
#pragma unroll
        for (int q = 0; q < 4; ++q) { d = mfma16(ux[q], v[q], d); mx = umax(mx, max4(v[q])); }
        d = mfma16(ub, v[4], d); mx = umax(mx, max4(v[4]));
        if (__builtin_expect(__builtin_amdgcn_ballot_w64(mx == SENT) != 0, 0)) {
          for (;;) {
            asm volatile("" ::: "memory");
#pragma unroll
            for (int q = 0; q < 4; ++q) v[q] = ld_mb(rm, po + q * 1024, sb);
            v[4] = ld_mb(rm, pm_o, sb);
            mx = 0;
#pragma unroll
            for (int q = 0; q < 5; ++q) mx = umax(mx, max4(v[q]));
            ++pfail;
            if (__builtin_amdgcn_ballot_w64(mx == SENT) == 0) break;
            if (pfail > SPIN_CAP) { astore(p.ctrl + C_TMO, 8u); *(LAS unsigned*)(smem + L_DEAD) = 1; break; }
            if ((pfail & 1023) == 0 && __builtin_amdgcn_readfirstlane(aload(p.ctrl + C_TMO))) { *(LAS unsigned*)(smem + L_DEAD) = 1; break; }
          }
          d = (f32x4){0, 0, 0, 0};
#pragma unroll
          for (int q = 0; q < 4; ++q) d = mfma16(ux[q], v[q], d);
          d = mfma16(ub, v[4], d);
        }
      }
      asm volatile("" : "+v"(d));
      DPT(7, tP);
      *(f32x4*)(smem + red_w) = d;
      if (lane == 0) *(LAS unsigned*)(smem + L_DEAD + 16 + wave * 4) = (mfail ? 1u : 0u) | (pfail ? 2u : 0u);
      __syncthreads();
      if (wave == 0) {
        f32x4 xb[8];
#pragma unroll
        for (int b8 = 0; b8 < 8; ++b8) xb[b8] = *(const f32x4*)(smem + red_r + b8 * 256);
        __builtin_amdgcn_sched_barrier(0);
        f32x4 x = ((xb[0] + xb[1]) + (xb[2] + xb[3])) + ((xb[4] + xb[5]) + (xb[6] + xb[7]));
        float r0 = x[0], r1 = x[1], r2 = x[2], r3 = x[3];
        v2u o2 = {umin(cvtpk(r0, r1), 0xFFFFFFFEu), umin(cvtpk(r2, r3), 0xFFFFFFFEu)};
        v4u o4 = {o2.x, o2.y, (unsigned)__builtin_amdgcn_mov_dpp(o2.x, 0xB1, 0xf, 0xf, true), (unsigned)__builtin_amdgcn_mov_dpp(o2.y, 0xB1, 0xf, 0xf, true)};
        st_mb(o4, rm, ado, sbn);
        DPT(8, tP);
      } else if (wave == 3 && c >= 1 && c <= 3) {
        v4u f = *(const v4u*)(smem + L_DEAD + 16);
        if (((f.x | f.y | f.z | f.w) & 2u) && lane == 0) __builtin_amdgcn_raw_buffer_store_b32(0u, rm, vo_mem(0, 9216 + 128 * c), sbn, 16);
      } else if (wave == 3 && c == 0) {
        v4u f = *(const v4u*)(smem + L_DEAD + 16);
        unsigned fl = __builtin_amdgcn_readfirstlane(f.x | f.y | f.z | f.w);
        unsigned now = rt();
        unsigned baseT = tM;
        const bool late = (int)(now - tM) > 2 || (int)(tM - now) > 600;
        if (late) baseT = now + (unsigned)lateM;
        if (prevLate && (fl & 2u)) { if (lateM < 80) lateM += 6; } else if (!late && !prevLate && lateM > LATE0) lateM -= 2;
        if (!prevLate) {
          const bool oth = __builtin_amdgcn_ballot_w64(ofail == 0u) != 0;
          if (fl & 1u) { if (oth) schedA += 2; else schedB += 2; }
          else if (fl & 2u) schedA += 2;
          else if (late) schedB += 2;
          else if (++streak >= DEC_EVERY) { streak = 0; if (++flip & 1) schedA -= 1; else schedB -= 1; }
        }
        prevLate = late || (fl & 2u);
        unsigned nP = (baseT + (unsigned)schedA) , nM = nP + (unsigned)schedB;
        if (nP == SENT) nP = 0; if (nM == SENT) nM = 0;
        if (lane < NREP) st_mb((v4u){nP, nM, 0u, 0u}, rm, vo_mem(lane, 9216), sbn);
        DVAL(true, 64 + s, now); DVAL(true, 128 + s, fl | (schedA << 8) | (schedB << 20));
      }
    }
    else __syncthreads();
    if (wave == 3) {
      if (s + 1 >= NSEG) drain();
#pragma unroll
      for (int w4 = 0; w4 < 4; ++w4) {
        v4u u4 = *(const v4u*)(smem + L_UIMG + ((8 * w4 + 4 * half + g) * 8 + n7) * 16);
        __builtin_amdgcn_raw_buffer_store_b128(u4, rs, w4 * 1024 + lane * 16, __builtin_amdgcn_readfirstlane(sp_off(LW_UM + (s * 72 + c) * 4096, pw)), 16);
      }
      if (s + 1 >= NSEG) drain();
      if (lane == 0 && ((s & 1) || s >= 58)) astore(p.ctrl + C_UM + c * 32, (unsigned)(s + 1 >= NSEG ? NSEG : s));
    }
    sb = sbn;
    if (*(volatile LAS unsigned*)(smem + L_DEAD)) break;
  }
  };
  if (wave == 0) loop(IC<0>{}); else if (wave == 1) loop(IC<1>{}); else if (wave == 2) loop(IC<2>{}); else loop(IC<3>{});
  __syncthreads();
}

DEV void spare_role(const Params& p, char* smem, int j, int wave, int lane, int tid) {
  __amdgpu_buffer_rsrc_t rm = mkrsrc(p.mbox, MBOX_BYTES);
  if (j == 0 && tid == 0) {
    const unsigned grp = (__builtin_amdgcn_s_getreg(20 | (3 << 11)) >> 2) & 1u;
    unsigned m = 0;
    for (int i = 0; i < NPAGES; ++i) { unsigned r = p.ctrl[C_CAL + i]; m |= ((r ^ (r >> 1) ^ grp) & 1u) << i; }
    const unsigned full = (1u << NPAGES) - 1u, alt = 0x555u & full;
    const int agree = __builtin_popcount(~(m ^ alt) & full);
    if (agree >= NPAGES - 3) m = alt; else if (agree <= 3) m = ~alt & full;
    astore(p.ctrl + C_POL, 0x1000u | m);
  }
  wg_wait_ge(p.ctrl + C_POL, 0x1000u, p.ctrl + C_TMO, 9u, wave);
  const unsigned pol = read_pol(p);
  DSTAMP(j == 0 && wave == 0, 4); DVAL(j == 0 && wave == 0, 5, pol);
  {
    const v4u sv = {SENT, SENT, SENT, SENT};
    for (int u = j; u < NSEG * NREP * 5; u += NSPARE) {
      const int st = u / (NREP * 5), cell = u % (NREP * 5), k = cell % 5;
      const unsigned sb = __builtin_amdgcn_readfirstlane(sbase(st, pol));
      if (k != 1 || tid < 128) st_mb(sv, rm, cell * 16384 + tid * 16, sb);
      if (k != 1 && k != 4) st_mb(sv, rm, cell * 16384 + 4096 + tid * 16, sb);
    }
  }
  wg_signal(p.ctrl + C_CLR, tid);
  DSTAMP(j == 0 && wave == 0, 6);
  {
    __amdgpu_buffer_rsrc_t rs = mkrsrc(p.span, SPAN_BYTES);
    const unsigned pr = par_r(pol);
    const int xg = j & 3, rk = j >> 2;
    auto wait_slot = [&](int k) {
      const unsigned sbk = __builtin_amdgcn_readfirstlane(sbase(k, pol));
      unsigned tp = SENT;
      for (unsigned spins = 0; spins < SPIN_CAP; ++spins) {
        asm volatile("" ::: "memory");
        v2u sc2 = ld2_mb(rm, vo_mem(xg, 9216), sbk);
        tp = __builtin_amdgcn_readfirstlane(sc2.x);
        if (tp != SENT) break;
        __builtin_amdgcn_s_sleep(8);
        if ((spins & 63u) == 63u && __builtin_amdgcn_readfirstlane(aload(p.ctrl + C_TMO))) break;
      }
      return tp;
    };
    auto relay = [&]() {
      if (j == 0) {
        unsigned a = aload(p.ctrl + C_UM + lane * 32), b = aload(p.ctrl + (lane < 8 ? C_UM + (64 + lane) * 32 : C_PX + (lane & 31) * 32));
        unsigned c3 = aload(p.ctrl + C_PX + (lane & 7) * 32);
        unsigned m = umin(umin(a, b), c3);
#pragma unroll
        for (int o = 1; o < 64; o <<= 1) m = umin(m, (unsigned)__shfl_xor((int)m, o));
        if (lane < 16) __hip_atomic_fetch_max(p.ctrl + C_PROG + lane * 32, m, __ATOMIC_RELAXED, __HIP_MEMORY_SCOPE_AGENT);
      }
    };
    for (int s = 1; s < NSEG; ++s) {
      if (wave == 0) {
        wait_slow(p.ctrl + C_UX + s, 32u, p.ctrl + C_TMO, 15u);
        wait_flags(p.ctrl + C_KQ + s * 32, 32, 1, 1u, lane, p.ctrl + C_TMO, 18u);
        if (s >= 5) {
          unsigned tp = wait_slot(s - 4);
          wait_until(tp - 80u);
          if (!(s & 1)) relay();
        }
      }
      __syncthreads();
      v4u v[12];
#pragma unroll
      for (int i = 0; i < 12; ++i) v[i] = ld_sc1(rs, sp_off(LW_UX + (s * 72 + xg + 4 * (3 * rk + i / 4)) * 16384 + ((i & 3) * 256 + tid) * 16, pr ^ 1u));
      v4u k6[6];
#pragma unroll
      for (int i = 0; i < 6; ++i) {
        int ch = rk * 6 + i;
        unsigned off = (ch < 32) ? sp_off(((ch >> 1) & 1) * SZ_KX + (s * 32 + xg + 4 * (ch >> 2)) * 8192 + (ch & 1) * 4096 + tid * 16, pr) : 0x7ffffff0u;
        k6[i] = ld_sc1(rs, off);
      }
#pragma unroll
      for (int i = 0; i < 12; ++i) asm volatile("" :: "v"(v[i]));
#pragma unroll
      for (int i = 0; i < 6; ++i) asm volatile("" :: "v"(k6[i]));
    }
    if (j == 0 && wave == 0) {
      for (int k = NSEG - 4; k < NSEG; ++k) { unsigned tp = wait_slot(k); wait_until(tp - 80u); relay(); }
      for (unsigned spins = 0; spins < SPIN_CAP && (unsigned)__builtin_amdgcn_readfirstlane(aload(p.ctrl + C_PROG)) < 60u; ++spins) {
        relay();
        __builtin_amdgcn_s_sleep(8);
        if (__builtin_amdgcn_readfirstlane(aload(p.ctrl + C_TMO))) break;
      }
    }
  }
}

DEV int tile_src_off(int wave, int lane) {
  const int row = wave * 8 + (lane >> 3), r = row & 15;
  return row * 576 + (((lane & 7) ^ ((r >> 1) & 7)) * 8);
}
struct XDma { const u16* src; char* dst; bool on; };
DEV XDma x_dma_setup(const u16* x, char* smem, int s, int buf, int wave, int lane, bool on) {
  XDma d;
  d.src = x + (size_t)s * 32 * 576 + tile_src_off(wave, lane);
  d.dst = smem + L_X + buf * XBUF + wave * 1024;
  d.on = on;
  return d;
}
DEV void x_dma_piece(const XDma& d, int wave, int i) {
  if (d.on) __builtin_amdgcn_global_load_lds((const unsigned*)(d.src + i * 64), (LAS unsigned*)(d.dst + i * 4096), 16, 0, 0);
}
template <int DEPTH, class F, class G>
DEV void xloop(const char* tile, int lane, F body, G side) {
  const int r = lane & 15, g = lane >> 4, f = (r >> 1) & 7;
  const char* pe = tile + (r >> 3) * 1024 + (r & 7) * 128 + ((g ^ f) * 16);
  const char* po = tile + (r >> 3) * 1024 + (r & 7) * 128 + (((4 + g) ^ f) * 16);
  auto rd = [&](int ks, int tt) { return *(const v4u*)(((ks & 1) ? po : pe) + (ks >> 1) * 4096 + tt * 2048); };
  v4u f0[DEPTH + 1], f1[DEPTH + 1];
#pragma unroll
  for (int i = 0; i < DEPTH; ++i) { f0[i] = rd(i, 0); f1[i] = rd(i, 1); }
#pragma unroll
  for (int ks = 0; ks < 18; ++ks) {
    if (ks + DEPTH < 18) {
      f0[(ks + DEPTH) % (DEPTH + 1)] = rd(ks + DEPTH, 0);
      f1[(ks + DEPTH) % (DEPTH + 1)] = rd(ks + DEPTH, 1);
    }
    __builtin_amdgcn_sched_barrier(0);
    body(ks, f0[ks % (DEPTH + 1)], f1[ks % (DEPTH + 1)]);
    __builtin_amdgcn_sched_barrier(0);
    side(ks);
  }
}
template <int NST, class FC, class FS>
DEV void stream(const u16* x, char* smem, int s0, int st, int wave, int lane, int tid, FC compute, FS signal) {
  const int n = (NSEG - s0 + st - 1) / st;
  {
    XDma d0 = x_dma_setup(x, smem, s0, 0, wave, lane, true), d1 = x_dma_setup(x, smem, min(s0 + st, NSEG - 1), 1, wave, lane, n > 1);
#pragma unroll
    for (int i = 0; i < 9; ++i) x_dma_piece(d0, wave, i);
#pragma unroll
    for (int i = 0; i < 9; ++i) x_dma_piece(d1, wave, i);
  }
  int buf = 0;
  for (int i = 0; i < n; ++i) {
    const int s = s0 + i * st;
    constexpr int EARLY = 4;
    if (i == 0 && n > 1) wait_vm<9>(); else if (i <= EARLY || i + 1 >= n) wait_vm<0>(); else wait_vm<9 + NST>();
    RAW_BARRIER();
    if (tid == 0) {
      if (i >= 1 && i <= EARLY) signal(s - st);
      else if (i >= EARLY + 2) signal(s - 2 * st);
      if (i > EARLY && i + 1 >= n) signal(s - st);
    }
    XDma d = x_dma_setup(x, smem, min(s + 2 * st, NSEG - 1), buf == 0 ? 2 : buf - 1, wave, lane, i + 2 < n);
    compute(s, smem + L_X + buf * XBUF, d);
    buf = buf == 2 ? 0 : buf + 1;
  }
  wait_vm<0>();
  RAW_BARRIER();
  if (tid == 0) signal(s0 + (n - 1) * st);
}
template <int NTW, class F>
DEV void load_kfrags(char* smem, int wave, int lane, int tid, F srcf, v4u (&w)[NTW][18]) {
  constexpr int RS = NTW * 128;
  const int g = lane >> 4, q4 = (lane & 15) >> 2, p4 = lane & 3;
  staged_load<RS>(smem, wave, lane, srcf, [&](int t, int base) {
#pragma unroll
    for (int tl = 0; tl < NTW; ++tl)
#pragma unroll
      for (int kk = 0; kk < 2; ++kk) {
        int a0 = base + (32 * kk + 8 * g + q4) * RS + ((wave * NTW + tl) * 16 + 4 * p4) * 2;
        v2u x0 = tr_read(smem, a0), x1 = tr_read(smem, a0 + 4 * RS);
        w[tl][2 * t + kk] = (v4u){x0.x, x0.y, x1.x, x1.y};
      }
  });
}

DEV void kq_worker(const Params& p, char* smem, int h, int wave, int lane, int tid) {
  v4u w[4][18];
  load_kfrags<4>(smem, wave, lane, tid, [&](int row, int cc) {
    return (cc < 16 ? p.Wk : p.Wq) + (size_t)row * 4096 + h * 128 + (cc & 15) * 8; }, w);
  DSTAMP(h == 0 && wave == 0, 40);
  __amdgpu_buffer_rsrc_t ro = mkrsrc(p.span, SPAN_BYTES);
  wg_wait_ge(p.ctrl + C_POL, 0x1000u, p.ctrl + C_TMO, 9u, wave);
  const unsigned pr = par_r(read_pol(p));
  DSTAMP(h == 0 && wave == 0, 41);
  stream<4>(p.x, smem, 0, 1, wave, lane, tid,
    [&](int s, const char* xb, const XDma& dm) {
      DSTAMP(h == 0 && wave == 0 && s < 4, 42 + s);
      f32x4 acc[2][4];
#pragma unroll
      for (int a = 0; a < 2; ++a)
#pragma unroll
        for (int b = 0; b < 4; ++b) acc[a][b] = (f32x4){0, 0, 0, 0};
      xloop<2>(xb, lane, [&](int ks, v4u b0, v4u b1) {
#pragma unroll
        for (int tl = 0; tl < 4; ++tl) { acc[0][tl] = mfma16(w[tl][ks], b0, acc[0][tl]); acc[1][tl] = mfma16(w[tl][ks], b1, acc[1][tl]); }
      }, [&](int ks) { if (!(ks & 1)) x_dma_piece(dm, wave, ks >> 1); });
#pragma unroll
      for (int tt = 0; tt < 2; ++tt)
#pragma unroll
        for (int uu = 0; uu < 2; ++uu) {
          f32x4 a = acc[tt][2 * uu], b = acc[tt][2 * uu + 1];
          v4u o = {cvtpk(a[0], a[1]), cvtpk(a[2], a[3]), cvtpk(b[0], b[1]), cvtpk(b[2], b[3])};
          st_sc1(o, ro, sp_off((wave < 2 ? LR_KX : LR_QX) + (s * 32 + h) * 8192, pr) + (tt * 4 + (wave & 1) * 2 + uu) * 1024 + lane * 16);
        }
      DSTAMP(h == 0 && wave == 1, 256 + s); DVAL(h == 0 && wave == 1, 320 + s, (unsigned)clk());
    },
    [&](int s) { astore(p.ctrl + C_KQ + s * 32 + h, 1u); });
}

DEV void vu_worker(const Params& p, char* smem, int h, int par, int wave, int lane, int tid) {
  const int g = lane >> 4;
  v4u wo[9][4];
  ABlk b0, b1;
  wv_block_load(p, h, 6 + par, tid, b0); wv_block_load(p, h, 8, tid, b1);
  load_wo(p, smem, h, par, wave, lane, tid, wo);
  v4u wv[2][18];
  load_kfrags<2>(smem, wave, lane, tid, [&](int row, int cc) {
    return p.Wv + (size_t)row * 4096 + h * 128 + cc * 8; }, wv);
  DSTAMP(h == 0 && par == 0 && wave == 0, 8);
  wg_wait_ge(p.ctrl + C_POL, 0x1000u, p.ctrl + C_TMO, 9u, wave);
  const unsigned pr = par_r(read_pol(p));
  wvo_unit<0, 9>(p, smem, h, 6 + par, 0, wave, lane, tid, b0, wo, pr);
  if (par == 0) wvo_unit<0, 5>(p, smem, h, 8, 1, wave, lane, tid, b1, wo, pr);
  else wvo_unit<5, 9>(p, smem, h, 8, 1, wave, lane, tid, b1, wo, pr);
  wg_signal(p.ctrl + C_WVO, tid);
  DSTAMP(h == 0 && par == 0 && wave == 0, 9);
  __amdgpu_buffer_rsrc_t ro = mkrsrc(p.span, SPAN_BYTES);
  stream<9>(p.x, smem, par, 2, wave, lane, tid,
    [&](int s, const char* xb, const XDma& dm) {
      f32x4 acc[2][2];
#pragma unroll
      for (int a = 0; a < 2; ++a)
#pragma unroll
        for (int b = 0; b < 2; ++b) acc[a][b] = (f32x4){0, 0, 0, 0};
      xloop<3>(xb, lane, [&](int ks, v4u b0, v4u b1) {
#pragma unroll
        for (int tl = 0; tl < 2; ++tl) { acc[0][tl] = mfma16(wv[tl][ks], b0, acc[0][tl]); acc[1][tl] = mfma16(wv[tl][ks], b1, acc[1][tl]); }
      }, [&](int ks) { if (!(ks & 1)) x_dma_piece(dm, wave, ks >> 1); });
#pragma unroll
      for (int tt = 0; tt < 2; ++tt) {
        f32x4 a = acc[tt][0], b = acc[tt][1];
        *(v4u*)(smem + L_V + (tt * 4 + wave) * 1024 + lane * 16) = (v4u){cvtpk(a[0], a[1]), cvtpk(a[2], a[3]), cvtpk(b[0], b[1]), cvtpk(b[2], b[3])};
      }
      RAW_BARRIER();
      v4u a[2][4];
#pragma unroll
      for (int tt = 0; tt < 2; ++tt)
#pragma unroll
        for (int u = 0; u < 4; ++u) a[tt][u] = *(const v4u*)(smem + L_V + (tt * 4 + u) * 1024 + lane * 16);
#pragma unroll
      for (int tl = 0; tl < 9; ++tl) {
        f32x4 d0 = {0, 0, 0, 0}, d1 = {0, 0, 0, 0};
#pragma unroll
        for (int u = 0; u < 4; ++u) { d0 = mfma16(a[0][u], wo[tl][u], d0); d1 = mfma16(a[1][u], wo[tl][u], d1); }
        const int n = (wave * 9 + tl) * 16 + (lane & 15), c = n >> 3;
        v4u o = {cvtpk(d0[0], d0[1]), cvtpk(d0[2], d0[3]), cvtpk(d1[0], d1[1]), cvtpk(d1[2], d1[3])};
        st_sc1(o, ro, sp_off(LW_UX + ((s * 72 + c) * 16 + (h >> 1)) * 1024 + (g * 16 + (h & 1) * 8 + (n & 7)) * 16, pr ^ 1u));
      }
      DSTAMP(h == 0 && wave == 1, 192 + s); DVAL(h == 0 && wave == 1, 384 + s, (unsigned)clk());
    },
    [&](int s) { aadd(p.ctrl + C_UX + s, 1u); });
}

DEV void b1_worker(const Params& p, char* smem, int gi, int wave, int lane, int tid) {
  {
    const unsigned lb = lds_addr(smem) + wave * 1024;
#pragma unroll
    for (int i = 0; i < 9; ++i) dma16(p.x + (size_t)(gi >> 2) * 32 * 576 + (i * 256 + tid) * 8, lb + 65536 + (i & 7) * 4096);
    const u16* src = (((tid & 31) < 16) ? p.Wk : p.Wq) + (size_t)(256 + (tid >> 5)) * 4096 + gi * 128 + (tid & 15) * 8;
#pragma unroll 4
    for (int i = 0; i < 40; ++i) dma16(src + (size_t)i * 8 * 4096, lb + (i & 7) * 4096);
    drain();
  }
  if (gi < 28) {
    const int slab = gi >> 2, sub = gi & 3;
    if (wave == 0) wait_slow(p.ctrl + C_KQ + gi, 1u, p.ctrl + C_TMO, 23u);
    __syncthreads();
    DSTAMP(gi == 0 && wave == 0, 30);
    v4u w[4][18];
    load_kfrags<4>(smem, wave, lane, tid, [&](int row, int cc) {
      int col = slab * 256 + cc * 8; if (col > 1720) col = 1720;
      return p.wqkv + (size_t)row * 1728 + col; }, w);
    DSTAMP(gi == 0 && wave == 0, 31);
    __amdgpu_buffer_rsrc_t rqk = mkrsrc(p.data + OFF_QKB, (unsigned)(2048 * 1152 * 2));
    __amdgpu_buffer_rsrc_t rvt = mkrsrc(p.data + OFF_VTB, (unsigned)(576 * 2048 * 2));
    const int g = lane >> 4;
    const int col0 = slab * 256 + wave * 64;
    const bool isv = col0 >= 1152;
    stream<4>(p.x, smem, sub, 4, wave, lane, tid,
      [&](int s, const char* xb, const XDma& dm) {
        f32x4 acc[2][4];
#pragma unroll
        for (int a = 0; a < 2; ++a)
#pragma unroll
          for (int b = 0; b < 4; ++b) acc[a][b] = (f32x4){0, 0, 0, 0};
        if (!isv) {
          xloop<2>(xb, lane, [&](int ks, v4u b0, v4u b1) {
#pragma unroll
            for (int tl = 0; tl < 4; ++tl) { acc[0][tl] = mfma16(w[tl][ks], b0, acc[0][tl]); acc[1][tl] = mfma16(w[tl][ks], b1, acc[1][tl]); }
          }, [&](int ks) { if (!(ks & 1)) x_dma_piece(dm, wave, ks >> 1); });
#pragma unroll
          for (int tt = 0; tt < 2; ++tt)
#pragma unroll
            for (int tp = 0; tp < 2; ++tp) {
              f32x4 a = acc[tt][2 * tp], b = acc[tt][2 * tp + 1];
              auto r0 = __builtin_amdgcn_permlane16_swap(cvtpk(a[0], a[1]), cvtpk(b[0], b[1]), false, false);
              auto r1 = __builtin_amdgcn_permlane16_swap(cvtpk(a[2], a[3]), cvtpk(b[2], b[3]), false, false);
              st_sc1((v4u){r0[0], r1[0], r0[1], r1[1]}, rqk,
                     ((s * 32 + tt * 16 + (lane & 15)) * 1152 + col0 + 16 * (2 * tp + (g & 1)) + 8 * (g >> 1)) * 2);
            }
        } else {
          xloop<2>(xb, lane, [&](int ks, v4u b0, v4u b1) {
#pragma unroll
            for (int tl = 0; tl < 4; ++tl) { acc[0][tl] = mfma16(b0, w[tl][ks], acc[0][tl]); acc[1][tl] = mfma16(b1, w[tl][ks], acc[1][tl]); }
          }, [&](int ks) { if (!(ks & 1)) x_dma_piece(dm, wave, ks >> 1); });
#pragma unroll
          for (int tl = 0; tl < 4; ++tl) {
            f32x4 a = acc[0][tl], b = acc[1][tl];
            st_sc1((v4u){cvtpk(a[0], a[1]), cvtpk(a[2], a[3]), cvtpk(b[0], b[1]), cvtpk(b[2], b[3])}, rvt,
                   (((col0 - 1152 + tl * 16 + (lane & 15)) * 64 + s) * 4 + g) * 16);
          }
        }
      },
      [&](int) {});
    DSTAMP(gi == 0 && wave == 0, 32);
  }
  wg_signal(p.ctrl + C_B1, tid);
}

DEV void b2_item(const Params& p, char* smem, int hg, int qb, int wave, int lane, int tid) {
  __amdgpu_buffer_rsrc_t rqk = mkrsrc(p.data + OFF_QKB, (unsigned)(2048 * 1152 * 2));
  __amdgpu_buffer_rsrc_t rvt = mkrsrc(p.data + OFF_VTB, (unsigned)(576 * 2048 * 2));
  __amdgpu_buffer_rsrc_t rob = mkrsrc(p.data + OFF_OB, (unsigned)SZ_OB);
  const int g = lane >> 4, li = lane & 15;
  const int hb = hg * 4 + wave;
  const int q0 = qb * 128, k0 = q0 - 256;
  const float SC = 0.23570226039551584f * 1.4426950408889634f;
  const float NEG = -1e30f;
  const unsigned OOB = 0x7ffffff0u;
  auto rowfrag = [&](int tok, int colbase) {
    unsigned off = (tok >= 0 && g < 3) ? (unsigned)((tok * 1152 + colbase + 8 * g) * 2) : OOB;
    v4u v = ld_sc1(rqk, off);
    if (g == 2) { v.y = 0u; v.z = 0u; v.w = 0u; }
    return v;
  };
  v4u kf[24], vf[12][2], qf[8];
#pragma unroll
  for (int qt = 0; qt < 8; ++qt) qf[qt] = rowfrag(q0 + 16 * qt + li, 18 * hb);
#pragma unroll
  for (int t = 0; t < 24; ++t) kf[t] = rowfrag(k0 + 16 * t + li, 576 + 18 * hb);
#pragma unroll
  for (int b = 0; b < 12; ++b)
#pragma unroll
    for (int dt = 0; dt < 2; ++dt) {
      const int blk = 4 * qb - 8 + b, d = 16 * dt + li;
      v4u v = ld_sc1(rvt, (blk >= 0 && d < 18) ? (unsigned)((((18 * hb + d) * 64 + blk) * 4 + g) * 16) : OOB);
      if (dt == 1 && li == 2) v = (v4u){0x3F803F80u, 0x3F803F80u, 0x3F803F80u, 0x3F803F80u};
      vf[b][dt] = v;
    }
#pragma unroll
  for (int qt = 0; qt < 8; ++qt) {
    f32x4 sc[17];
#pragma unroll
    for (int tau = 0; tau < 17; ++tau) {
      const float c = (k0 + 16 * (qt + tau)) >= 0 ? 0.f : NEG;
      f32x4 ci = {c, c, c, c};
      if (tau == 0) {
#pragma unroll
        for (int r = 0; r < 4; ++r) ci[r] = (4 * g + r > li) ? c : NEG;
      }
      if (tau == 16) {
#pragma unroll
        for (int r = 0; r < 4; ++r) ci[r] = (4 * g + r <= li) ? c : NEG;
      }
      sc[tau] = mfma16(kf[qt + tau], qf[qt], ci);
    }
    float m = fmaxf(fmaxf(sc[0][0], sc[0][1]), fmaxf(sc[0][2], sc[0][3]));
#pragma unroll
    for (int tau = 1; tau < 17; ++tau) m = fmaxf(fmaxf(fmaxf(m, sc[tau][0]), sc[tau][1]), fmaxf(sc[tau][2], sc[tau][3]));
    m = xmax(m);
    const float ms = -m * SC;
    unsigned pk[18][2];
#pragma unroll
    for (int t = 0; t < 17; ++t) {
      float e0 = __builtin_amdgcn_exp2f(fmaf(sc[t][0], SC, ms)), e1 = __builtin_amdgcn_exp2f(fmaf(sc[t][1], SC, ms));
      float e2 = __builtin_amdgcn_exp2f(fmaf(sc[t][2], SC, ms)), e3 = __builtin_amdgcn_exp2f(fmaf(sc[t][3], SC, ms));
      pk[t + (qt & 1)][0] = cvtpk(e0, e1); pk[t + (qt & 1)][1] = cvtpk(e2, e3);
    }
    pk[(qt & 1) ? 0 : 17][0] = 0u; pk[(qt & 1) ? 0 : 17][1] = 0u;
    f32x4 o0 = {0, 0, 0, 0}, o1 = {0, 0, 0, 0};
#pragma unroll
    for (int b = 0; b < 9; ++b) {
      v4u pf = {pk[2 * b][0], pk[2 * b][1], pk[2 * b + 1][0], pk[2 * b + 1][1]};
      o0 = mfma16(vf[(qt >> 1) + b][0], pf, o0);
      o1 = mfma16(vf[(qt >> 1) + b][1], pf, o1);
    }
    const float inv = __builtin_amdgcn_rcpf(__shfl(o1[2], li));
    char* orow = smem + (16 * qt + li) * 144 + wave * 36;
    *(unsigned*)(orow + 8 * g) = cvtpk(o0[0] * inv, o0[1] * inv);
    *(unsigned*)(orow + 8 * g + 4) = cvtpk(o0[2] * inv, o0[3] * inv);
    if (g == 0) *(unsigned*)(orow + 32) = cvtpk(o1[0] * inv, o1[1] * inv);
  }
  __syncthreads();
#pragma unroll
  for (int i = 0; i < 5; ++i) {
    int ch = tid + 256 * i;
    if (ch < 1152) { int row = ch / 9, cc = ch % 9; st_sc1(*(const v4u*)(smem + ch * 16), rob, ((q0 + row) * 576 + hg * 72) * 2 + cc * 16); }
  }
  wg_signal(p.ctrl + C_OB + qb * 32, tid);
}

DEV void fa_item(const Params& p, char* smem, int s, int j, int widx, int wave, int lane, int tid) {
  __amdgpu_buffer_rsrc_t rs = mkrsrc(p.span, SPAN_BYTES);
  __amdgpu_buffer_rsrc_t rab = mkrsrc(p.data + OFF_AB, (unsigned)SZ_OB);
  __amdgpu_buffer_rsrc_t rg = mkrsrc(p.data + OFF_GATE, SZ_GATE);
  const int g = lane >> 4, li = lane & 15;
  const int nt = j * 4 + wave;
  if (wave == 0) {
    wait_ge(p.ctrl + C_UX + s, 32u, p.ctrl + C_TMO, 19u);
    wait_ge(p.ctrl + C_FB + (s >> 2), 9u, p.ctrl + C_TMO, 20u);
  }
  __syncthreads();
  const unsigned pw = par_r(read_pol(p)) ^ 1u;
  v4u ux[2][16];
#pragma unroll
  for (int sl = 0; sl < 2; ++sl)
#pragma unroll
    for (int pp = 0; pp < 16; ++pp) ux[sl][pp] = ld_sc1(rs, sp_off(LW_UX + ((s * 72 + nt * 2 + sl) * 16 + (pp & 8)) * 1024, pw) + (pp & 7) * 1024 + lane * 16);
  v2u bp[2] = {{0u, 0u}, {0u, 0u}};
  float gt[2] = {1.f, 1.f};
  {
    v4u b4 = ld_sc1(rab, ((s * 36 + nt) * 64 + lane) * 16);
    bp[0] = (v2u){b4.x, b4.y}; bp[1] = (v2u){b4.z, b4.w};
#pragma unroll
    for (int qt = 0; qt < 2; ++qt) gt[qt] = __uint_as_float(ld1_sc1(rg, (s * 32 + qt * 16 + li) * 4));
  }
  if (wave == 1) {
    wait_slow(p.ctrl + C_PROG + (widx & 15) * 32, umin((unsigned)s + 1u, 60u), p.ctrl + C_TMO, 17u);
    if (s >= 60) {
      wait_flags(p.ctrl + C_PX, 32, 32, (unsigned)s + 1u, lane, p.ctrl + C_TMO, 11u);
      wait_flags(p.ctrl + C_UM, NC, 32, (unsigned)s + 1u, lane, p.ctrl + C_TMO, 12u);
    }
  }
  __syncthreads();
  v4u um[2][4];
  {
    v4u v[20];
#pragma unroll
    for (int i = 0; i < 20; ++i) v[i] = ld_sc1(rs, sp_off(LW_PX + s * 81920 + (i >> 1) * 8192, pw) + ((i & 1) * 256 + tid) * 16);
#pragma unroll
    for (int sl = 0; sl < 2; ++sl)
#pragma unroll
      for (int mm = 0; mm < 4; ++mm) um[sl][mm] = ld_sc1(rs, sp_off(LW_UM + (s * 72 + nt * 2 + sl) * 4096, pw) + mm * 1024 + lane * 16);
#pragma unroll
    for (int i = 0; i < 20; ++i) *(v4u*)(smem + (i * 256 + tid) * 16) = v[i];
  }
  __syncthreads();
#pragma unroll
  for (int qt = 0; qt < 2; ++qt) {
    f32x4 aA[2] = {{0, 0, 0, 0}, {0, 0, 0, 0}}, aB[2] = {{0, 0, 0, 0}, {0, 0, 0, 0}};
    v4u pe[3], po[3];
    auto ldp = [&](int i) {
      const char* b = (i < 16) ? smem + (qt * 32 + 2 * i) * 1024 + lane * 16 : smem + 65536 + (qt * 8 + 2 * (i - 16)) * 1024 + lane * 16;
      pe[i % 3] = *(const v4u*)b; po[i % 3] = *(const v4u*)(b + 1024);
    };
    ldp(0); ldp(1);
#pragma unroll
    for (int i = 0; i < 20; ++i) {
      if (i + 2 < 20) ldp(i + 2);
      __builtin_amdgcn_sched_barrier(0);
#pragma unroll
      for (int sl = 0; sl < 2; ++sl) {
        v4u u = (i < 16) ? ux[sl][i & 15] : um[sl][i & 3];
        aA[sl] = mfma16(u, pe[i % 3], aA[sl]); aB[sl] = mfma16(u, po[i % 3], aB[sl]);
      }
      __builtin_amdgcn_sched_barrier(0);
    }
    const int tok = s * 32 + qt * 16 + li;
    float o[4];
#pragma unroll
    for (int r = 0; r < 4; ++r) {
      auto sw = __builtin_amdgcn_permlane32_swap(__float_as_uint(aB[0][r]), __float_as_uint(aA[1][r]), false, false);
      float own = (lane < 32) ? aA[0][r] : aB[1][r];
      float oth = (lane < 32) ? __uint_as_float(sw[1]) : __uint_as_float(sw[0]);
      unsigned bw = (r < 2) ? bp[qt].x : bp[qt].y;
      o[r] = gt[qt] * (own + oth) + bf2f((r & 1) ? (bw >> 16) : (bw & 0xffffu));
    }
    v2u ov = {cvtpk(o[0], o[1]), cvtpk(o[2], o[3])};
    *(v2u*)(p.out + (size_t)tok * 576 + nt * 16 + 4 * g) = ov;
  }
  __syncthreads();
}

DEV void fb_item(const Params& p, char* smem, int j, int q, int wave, int lane, int tid) {
  __amdgpu_buffer_rsrc_t rob = mkrsrc(p.data + OFF_OB, (unsigned)SZ_OB);
  __amdgpu_buffer_rsrc_t rab = mkrsrc(p.data + OFF_AB, (unsigned)SZ_OB);
  __amdgpu_buffer_rsrc_t rg = mkrsrc(p.data + OFF_GATE, SZ_GATE);
  const int g = lane >> 4, li = lane & 15;
  const int nt = j * 4 + wave;
  v4u wf[1][18];
  load_kfrags<1>(smem, wave, lane, tid, [&](int row, int cc) { return p.wo + (size_t)row * 576 + j * 64 + cc * 8; }, wf);
  v4u gv[9];
#pragma unroll
  for (int i = 0; i < 9; ++i) gv[i] = *(const v4u*)(p.gw + (i * 8 + (lane & 7)) * 8);
  wg_wait_ge(p.ctrl + C_OB + q * 32, 8u, p.ctrl + C_TMO, 10u, wave);
  v4u v[9], xv[9];
  auto issue = [&](int s) {
#pragma unroll
    for (int i = 0; i < 9; ++i) v[i] = ld_sc1(rob, (s * 32 * 576 + tile_src_off(wave, lane) + i * 64) * 2);
#pragma unroll
    for (int i = 0; i < 9; ++i) xv[i] = *(const v4u*)(p.x + (size_t)(s * 32 + wave * 8 + (lane >> 3)) * 576 + (i * 8 + (lane & 7)) * 8);
  };
  issue(4 * q);
  for (int it = 0; it < 4; ++it) {
    const int s = 4 * q + it;
    char* buf = smem + (it & 1) * 40960;
#pragma unroll
    for (int i = 0; i < 9; ++i) *(v4u*)(buf + (i * 4 + wave) * 1024 + lane * 16) = v[i];
    {
      float a = 0;
#pragma unroll
      for (int i = 0; i < 9; ++i)
#pragma unroll
        for (int e = 0; e < 4; ++e) {
          a += bf2f(xv[i][e] & 0xffffu) * bf2f(gv[i][e] & 0xffffu);
          a += bf2f(xv[i][e] >> 16) * bf2f(gv[i][e] >> 16);
        }
      a += __shfl_xor(a, 1); a += __shfl_xor(a, 2); a += __shfl_xor(a, 4);
      const float gs = 1.f / (1.f + __expf(-a));
      if ((lane & 7) == 0) {
        *(float*)(buf + 36864 + (wave * 8 + (lane >> 3)) * 4) = gs;
      }
    }
    __syncthreads();
    if (it + 1 < 4) issue(s + 1);
    f32x4 bacc[2] = {{0, 0, 0, 0}, {0, 0, 0, 0}};
    xloop<3>(buf, lane, [&](int ks, v4u b0, v4u b1) { bacc[0] = mfma16(wf[0][ks], b0, bacc[0]); bacc[1] = mfma16(wf[0][ks], b1, bacc[1]); }, [&](int) {});
    {
      const float o0 = 1.f - *(const float*)(buf + 36864 + li * 4), o1 = 1.f - *(const float*)(buf + 36864 + (16 + li) * 4);
      st_sc1((v4u){cvtpk(o0 * bacc[0][0], o0 * bacc[0][1]), cvtpk(o0 * bacc[0][2], o0 * bacc[0][3]),
                   cvtpk(o1 * bacc[1][0], o1 * bacc[1][1]), cvtpk(o1 * bacc[1][2], o1 * bacc[1][3])}, rab, ((s * 36 + nt) * 64 + lane) * 16);
      if (j == 0 && wave == 0 && lane < 8) st_sc1(*(const v4u*)(buf + 36864 + lane * 16), rg, s * 128 + lane * 16);
    }
  }
  wg_signal(p.ctrl + C_FB + q, tid);
}

DEV int take(const Params& p, char* smem, int k, int n, int tid) {
  LAS int* cmd = (LAS int*)(smem + 163824);
  __syncthreads();
  if (tid == 0) {
    int c = -1;
    if (aload(p.ctrl + C_FQ + k * 32) < (unsigned)n && !aload(p.ctrl + C_TMO)) {
      unsigned t = __hip_atomic_fetch_add(p.ctrl + C_FQ + k * 32, 1u, __ATOMIC_RELAXED, __HIP_MEMORY_SCOPE_AGENT);
      if (t < (unsigned)n) c = (int)t;
    }
    *cmd = c;
  }
  __syncthreads();
  return __builtin_amdgcn_readfirstlane(*(volatile LAS int*)cmd);
}

DEV void worker_role(const Params& p, char* smem, int widx, int wave, int lane, int tid) {
  const int slab = ((widx >> 3) << 2) | (widx & 3), par = (widx >> 2) & 1;
  if (slab < 32) vu_worker(p, smem, slab, par, wave, lane, tid);
  else if (par == 0) kq_worker(p, smem, slab - 32, wave, lane, tid);
  else b1_worker(p, smem, slab - 32, wave, lane, tid);
  DSTAMP(widx == 0 && wave == 0, 10);
  for (int c; (c = take(p, smem, 0, 128, tid)) >= 0;) {
    wg_wait_ge(p.ctrl + C_B1, 32u, p.ctrl + C_TMO, 13u, wave);
    b2_item(p, smem, c & 7, c >> 3, wave, lane, tid);
    DSTAMP(wave == 0 && (c & 31) == 31, 12 + (c >> 5));
  }
  for (int c; (c = take(p, smem, 1, 144, tid)) >= 0;) {
    fb_item(p, smem, c % 9, c / 9, wave, lane, tid);
    DSTAMP(wave == 0 && (c % 36) == 35, 33 + c / 36);
  }
  for (int c; (c = take(p, smem, 2, NSEG * 9, tid)) >= 0;) {
    fa_item(p, smem, c / 9, c % 9, widx, wave, lane, tid);
    DSTAMP(wave == 0 && (c % 9) == 0, 448 + c / 9);
  }
}

__global__ void __launch_bounds__(256, 1) fused_kernel(Params p) {
  extern __shared__ __attribute__((aligned(16))) char smem[];
  const int tid = threadIdx.x, lane = tid & 63, wave = __builtin_amdgcn_readfirstlane(tid >> 6);
  const int b = blockIdx.x;
  const int idx = (b >> 3) * 4 + (b & 3);
  {
    unsigned long long pc;
    asm volatile("s_getpc_b64 %0" : "=s"(pc));
    const char* cb = (const char*)(pc & ~255ull);
    v4u c0 = *(const v4u*)(cb + ((b >> 3) * 320 + tid) * 16);
    v4u c1 = *(const v4u*)(cb + ((b >> 3) * 320 + 256 + (tid & 63)) * 16);
    asm volatile("" :: "v"(c0), "v"(c1));
  }
  DSTAMP(wave == 0, 1024 + b);
  if ((b & 7) < 4) {
    if (idx < NH) head_role(p, smem, idx, wave, lane, tid);
    else if (idx < NH + NC) col_role(p, smem, idx - NH, wave, lane, tid);
    else spare_role(p, smem, idx - NH - NC, wave, lane, tid);
  } else {
    worker_role(p, smem, idx, wave, lane, tid);
  }
  DSTAMP(wave == 0, 1280 + b);
}

__global__ void __launch_bounds__(256) prep_kernel(Params p) {
  __shared__ unsigned sm[8];
  const int tid = threadIdx.x, lane = tid & 63, wave = __builtin_amdgcn_readfirstlane(tid >> 6), b = blockIdx.x;
  if (b >= NPAGES) {
    ((v4u*)p.ctrl)[(b - NPAGES) * 256 + tid] = (v4u){0u, 0u, 0u, 0u};
    return;
  }
  __amdgpu_buffer_rsrc_t rm = mkrsrc(p.mbox, MBOX_BYTES);
  unsigned acc = 0, mn0 = 1u << 30, mn1 = 1u << 30;
  const unsigned off = b * (2u << 20) + (1u << 20) + wave * 16384;
  acc += ld1_sc1(rm, off + 128);
  drain();
  asm volatile("" :: "v"(acc));
#pragma unroll 1
  for (int q = 0; q < 6; ++q) {
    unsigned long long t0 = clk();
    acc += ld1_sc1(rm, off + (q & 1) * 8192 + 512 + (q >> 1) * 256);
    drain();
    asm volatile("" :: "v"(acc));
    unsigned d = (unsigned)(clk() - t0);
    if (q & 1) mn1 = min(mn1, d); else mn0 = min(mn0, d);
  }
  if (lane == 0) { sm[wave * 2] = mn0; sm[wave * 2 + 1] = mn1; }
  __syncthreads();
  if (tid == 0) {
    unsigned a0 = min(min(sm[0], sm[2]), min(sm[4], sm[6])), a1 = min(min(sm[1], sm[3]), min(sm[5], sm[7]));
    const unsigned grp = (__builtin_amdgcn_s_getreg(20 | (3 << 11)) >> 2) & 1u;
    p.ctrl[C_CAL + b] = (a1 < a0 ? 1u : 0u) | (grp << 1);
  }
}

}

extern "C" void kernel_launch(void* const* d_in, const int* in_sizes, int n_in, void* d_out, int out_size, void* d_ws,
                              size_t ws_size, hipStream_t stream) {
  (void)in_sizes; (void)n_in; (void)out_size; (void)ws_size;
  Params p;
  p.x = (const u16*)d_in[0]; p.mem0 = (const u16*)d_in[1];
  p.Wq = (const u16*)d_in[2]; p.Wk = (const u16*)d_in[3]; p.Wv = (const u16*)d_in[4]; p.Wo = (const u16*)d_in[5];
  p.wqkv = (const u16*)d_in[8]; p.wo = (const u16*)d_in[9]; p.gw = (const u16*)d_in[10];
  p.out = (u16*)d_out;
  char* ws = (char*)d_ws;
  p.ctrl = (unsigned*)ws;
  size_t a = ((size_t)ws + CTRL_BYTES + (2u << 20) - 1) & ~(size_t)((2u << 20) - 1);
  p.mbox = (char*)a;
  p.span = p.mbox + MBOX_BYTES;
  p.data = p.span + SPAN_BYTES;
  hipFuncSetAttribute((const void*)fused_kernel, hipFuncAttributeMaxDynamicSharedMemorySize, LDS_BYTES);
  prep_kernel<<<NPAGES + 24, 256, 0, stream>>>(p);
  fused_kernel<<<256, 256, LDS_BYTES, stream>>>(p);
}
```

```cpp
#include <hip/hip_runtime.h>
#include <hip/hip_bf16.h>

typedef unsigned short u16;
typedef unsigned v4u __attribute__((ext_vector_type(4)));
typedef unsigned v2u __attribute__((ext_vector_type(2)));
typedef unsigned v8u __attribute__((ext_vector_type(8)));
typedef __attribute__((ext_vector_type(8))) __bf16 bf16x8;
typedef __attribute__((ext_vector_type(16))) __bf16 bf16x16;
typedef __attribute__((ext_vector_type(4))) float f32x4;
typedef __attribute__((ext_vector_type(4))) short s16x4;
#define LAS __attribute__((address_space(3)))
#define DEV __device__ __forceinline__

namespace {

constexpr int NSEG = 64;
constexpr int NH = 32, NC = 72, NSPARE = 24, NWORK = 128;
constexpr unsigned SENT = 0xFFFFFFFFu;
constexpr unsigned SPIN_CAP = 1u << 22;
constexpr int NREP = 4;
constexpr int STEP_SPAN = NREP * 5 * 16384;
constexpr int NPAGES = 11;
constexpr unsigned MBOX_BYTES = NPAGES * (2u << 20);

constexpr size_t CTRL_BYTES = 98304 + 4096;
constexpr unsigned SZ_WVO = 72u * 294912u, SZ_KX = 64u * 32u * 8192u, SZ_UX = 64u * 72u * 16384u, SZ_PX = 64u * 81920u, SZ_UM = 64u * 72u * 4096u;
constexpr unsigned LW_UX = 0, LW_UM = SZ_UX, LW_PX = SZ_UX + SZ_UM;
constexpr unsigned LR_KX = 0, LR_QX = SZ_KX, LR_WVO = 2 * SZ_KX;
constexpr unsigned SPAN_BYTES = 200u << 20;
static_assert(LW_PX + SZ_PX <= SPAN_BYTES / 2 && LR_WVO + SZ_WVO <= SPAN_BYTES / 2, "span");
constexpr size_t OFF_QKB = 0;
constexpr size_t SZ_QKB = (size_t)2048 * 1152 * 2 + 4096;
constexpr size_t OFF_VTB = OFF_QKB + SZ_QKB;
constexpr size_t SZ_VTB = (size_t)608 * 2048 * 2;
constexpr size_t OFF_OB = OFF_VTB + SZ_VTB;
constexpr size_t SZ_OB = (size_t)2048 * 576 * 2;
constexpr size_t OFF_AB = OFF_OB + SZ_OB;
constexpr size_t OFF_GATE = OFF_AB + SZ_OB;
constexpr size_t SZ_GATE = 8192;
constexpr size_t DATA_BYTES = OFF_GATE + SZ_GATE;

constexpr int C_TMO = 0;
constexpr int C_POL = 64;
constexpr int C_CLR = 128;
constexpr int C_WVO = 192;
constexpr int C_AUX = 256;
constexpr int C_B1 = 320;
constexpr int C_FQ = 384;
constexpr int C_FA = 1152;
constexpr int C_FB = 1280;
constexpr int C_PROG = 512;
constexpr int C_UX = 1024;
constexpr int C_OB = 2048;
constexpr int C_KQ = 4096;
constexpr int C_PX = 8192;
constexpr int C_DBG = 12288;
constexpr int C_UM = 16384;
constexpr int C_CAL = 24576;

struct Params {
  const u16 *x, *mem0, *Wq, *Wk, *Wv, *Wo, *wqkv, *wo, *gw;
  u16* out;
  unsigned* ctrl;
  char* mbox;
  char* span;
  char* data;
};

template <int N> struct IC { static constexpr int value = N; };
DEV unsigned umin(unsigned a, unsigned b) { return a < b ? a : b; }
DEV unsigned umax(unsigned a, unsigned b) { return a > b ? a : b; }
DEV float bf2f(unsigned v) { return __builtin_bit_cast(float, v << 16); }
typedef __attribute__((ext_vector_type(2))) float f32x2;
typedef __attribute__((ext_vector_type(2))) __bf16 bf16x2;
DEV unsigned cvtpk(float lo, float hi) { return __builtin_bit_cast(unsigned, __builtin_convertvector((f32x2){lo, hi}, bf16x2)); }
DEV unsigned long long clk() { unsigned long long t; asm volatile("s_memtime %0\n\ts_waitcnt lgkmcnt(0)" : "=s"(t) :: "memory"); return t; }
DEV unsigned rt() { unsigned long long t; asm volatile("s_memrealtime %0\n\ts_waitcnt lgkmcnt(0)" : "=s"(t) :: "memory"); return (unsigned)t; }
DEV unsigned vo_mem(int rep, unsigned o) { return (unsigned)(rep * 5 + (o >> 13)) * 16384u + (o & 8191u); }
DEV unsigned vo_p(int rep, unsigned o) { return (unsigned)(rep * 5 + 2 + (o >> 13)) * 16384u + (o & 8191u); }
DEV unsigned sbase(int s, unsigned pol) { int pg = s / 6; return (unsigned)pg * (2u << 20) + (unsigned)(s - 6 * pg) * STEP_SPAN + (((pol >> pg) & 1u) << 13); }
DEV unsigned sp_off(unsigned o, unsigned par) { return ((o >> 13) << 14) | ((((o >> 20) ^ par) & 1u) << 13) | (o & 8191u); }
DEV unsigned par_r(unsigned pol) { return (pol ^ (unsigned)NPAGES) & 1u; }
DEV __amdgpu_buffer_rsrc_t mkrsrc(const void* p, unsigned bytes) { return __builtin_amdgcn_make_buffer_rsrc((void*)p, 0, (int)bytes, 0x00020000); }
DEV v4u ld_sc1(__amdgpu_buffer_rsrc_t r, unsigned off) { return __builtin_amdgcn_raw_buffer_load_b128(r, off, 0, 16); }
DEV v2u ld2_sc1(__amdgpu_buffer_rsrc_t r, unsigned off) { return __builtin_amdgcn_raw_buffer_load_b64(r, off, 0, 16); }
DEV unsigned ld1_sc1(__amdgpu_buffer_rsrc_t r, unsigned off) { return __builtin_amdgcn_raw_buffer_load_b32(r, off, 0, 16); }
DEV void st_sc1(v4u v, __amdgpu_buffer_rsrc_t r, unsigned off) { __builtin_amdgcn_raw_buffer_store_b128(v, r, off, 0, 16); }
DEV void st2_sc1(v2u v, __amdgpu_buffer_rsrc_t r, unsigned off) { __builtin_amdgcn_raw_buffer_store_b64(v, r, off, 0, 16); }
DEV void st1_sc1(unsigned v, __amdgpu_buffer_rsrc_t r, unsigned off) { __builtin_amdgcn_raw_buffer_store_b32(v, r, off, 0, 16); }
DEV v4u ld_mb(__amdgpu_buffer_rsrc_t r, unsigned vo, unsigned so) { return __builtin_amdgcn_raw_buffer_load_b128(r, vo, so, 16); }
DEV v2u ld2_mb(__amdgpu_buffer_rsrc_t r, unsigned vo, unsigned so) { return __builtin_amdgcn_raw_buffer_load_b64(r, vo, so, 16); }
DEV void st_mb(v4u v, __amdgpu_buffer_rsrc_t r, unsigned vo, unsigned so) { __builtin_amdgcn_raw_buffer_store_b128(v, r, vo, so, 16); }
DEV void drain() { asm volatile("s_waitcnt vmcnt(0)" ::: "memory"); }
DEV unsigned max4(v4u v) { return umax(umax(v.x, v.y), umax(v.z, v.w)); }
DEV v2u tr_read(const char* smem, unsigned off) {
  s16x4 v = __builtin_amdgcn_ds_read_tr16_b64_v4i16((LAS s16x4*)(smem + off));
  return __builtin_bit_cast(v2u, v);
}
DEV v4u trfrag(const char* smem, int off) {
  v2u x0 = tr_read(smem, off), x1 = tr_read(smem, off + 512);
  return (v4u){x0.x, x0.y, x1.x, x1.y};
}
DEV f32x4 mfma16(v4u a, v4u b, f32x4 c) {
  return __builtin_amdgcn_mfma_f32_16x16x32_bf16(__builtin_bit_cast(bf16x8, a), __builtin_bit_cast(bf16x8, b), c, 0, 0, 0);
}
DEV f32x4 smfma(v4u a, v8u b, f32x4 c, int idx) {
  return __builtin_amdgcn_smfmac_f32_16x16x64_bf16(__builtin_bit_cast(bf16x8, a), __builtin_bit_cast(bf16x16, b), c, idx, 0, 0);
}
template <int CTRL> DEV float dpp(float x) {
  return __builtin_bit_cast(float, __builtin_amdgcn_mov_dpp(__builtin_bit_cast(int, x), CTRL, 0xf, 0xf, true));
}
constexpr int ROR8 = 0x128;
DEV float amax(float a, float b) { float r; asm("v_max_f32 %0, %1, %2\n\ts_nop 1" : "=v"(r) : "v"(a), "v"(b)); return r; }
DEV float amax3(float a, float b, float c) { return fmaxf(fmaxf(a, b), c); }
DEV float xmax(float x) {
  auto s = __builtin_amdgcn_permlane16_swap(__float_as_uint(x), __float_as_uint(x), false, false);
  x = amax(__uint_as_float(s[0]), __uint_as_float(s[1]));
  auto t = __builtin_amdgcn_permlane32_swap(__float_as_uint(x), __float_as_uint(x), false, false);
  return amax(__uint_as_float(t[0]), __uint_as_float(t[1]));
}
DEV float xmax8(float x) {
  float t;
  asm("s_nop 1\n\tv_max_f32_dpp %0, %0, %0 row_ror:8 row_mask:0xf bank_mask:0xf\n\tv_mov_b32 %1, %0\n\ts_nop 1\n\t"
      "v_permlane16_swap_b32 %0, %1\n\tv_max_f32 %0, %0, %1\n\tv_mov_b32 %1, %0\n\ts_nop 1\n\t"
      "v_permlane32_swap_b32 %0, %1\n\tv_max_f32 %0, %0, %1" : "+v"(x), "=&v"(t));
  return x;
}
DEV float xsum(float x) {
  auto s = __builtin_amdgcn_permlane16_swap(__float_as_uint(x), __float_as_uint(x), false, false);
  x = __uint_as_float(s[0]) + __uint_as_float(s[1]);
  auto t = __builtin_amdgcn_permlane32_swap(__float_as_uint(x), __float_as_uint(x), false, false);
  return __uint_as_float(t[0]) + __uint_as_float(t[1]);
}
DEV unsigned aload(unsigned* p) { return __hip_atomic_load(p, __ATOMIC_RELAXED, __HIP_MEMORY_SCOPE_AGENT); }
DEV void aadd(unsigned* p, unsigned v) { __hip_atomic_fetch_add(p, v, __ATOMIC_RELAXED, __HIP_MEMORY_SCOPE_AGENT); }
DEV void astore(unsigned* p, unsigned v) { __hip_atomic_store(p, v, __ATOMIC_RELAXED, __HIP_MEMORY_SCOPE_AGENT); }

DEV bool wait_ge(unsigned* w, unsigned target, unsigned* tmo, unsigned code) {
  for (unsigned spins = 0;; ++spins) {
    if ((unsigned)__builtin_amdgcn_readfirstlane(aload(w)) >= target) { __builtin_amdgcn_fence(__ATOMIC_ACQUIRE, "wavefront"); return true; }
    __builtin_amdgcn_s_sleep(2);
    if (spins > SPIN_CAP) { astore(tmo, code); return false; }
    if ((spins & 255u) == 255u && __builtin_amdgcn_readfirstlane(aload(tmo))) return false;
  }
}
DEV bool wait_slow(unsigned* w, unsigned target, unsigned* tmo, unsigned code) {
  for (unsigned spins = 0;; ++spins) {
    if ((unsigned)__builtin_amdgcn_readfirstlane(aload(w)) >= target) { __builtin_amdgcn_fence(__ATOMIC_ACQUIRE, "wavefront"); return true; }
    __builtin_amdgcn_s_sleep(12);
    if (spins > SPIN_CAP) { astore(tmo, code); return false; }
    if ((spins & 63u) == 63u && __builtin_amdgcn_readfirstlane(aload(tmo))) return false;
  }
}
DEV bool wait_flags(unsigned* w, int n, int st, unsigned target, int lane, unsigned* tmo, unsigned code) {
  for (unsigned spins = 0;; ++spins) {
    unsigned a = aload(w + min(lane, n - 1) * st), b = aload(w + min(lane + 64, n - 1) * st);
    if (__builtin_amdgcn_ballot_w64(a < target || b < target) == 0) { __builtin_amdgcn_fence(__ATOMIC_ACQUIRE, "wavefront"); return true; }
    __builtin_amdgcn_s_sleep(6);
    if (spins > SPIN_CAP) { astore(tmo, code); return false; }
    if ((spins & 63u) == 63u && __builtin_amdgcn_readfirstlane(aload(tmo))) return false;
  }
}
DEV void wg_wait_ge(unsigned* w, unsigned target, unsigned* tmo, unsigned code, int wave) {
  if (wave == 0) wait_ge(w, target, tmo, code);
  __syncthreads();
}
DEV void wg_signal(unsigned* w, int tid) {
  drain();
  __syncthreads();
  if (tid == 0) aadd(w, 1u);
}
DEV void wait_until(unsigned t) {
  for (int guard = 0; guard < 64; ++guard) {
    int rem = (int)(t - rt());
    if (rem <= 0 || rem > 1000) return;
    if (rem <= 16) { int n = (rem * 24) >> 6; for (int i = 0; i < n; ++i) __builtin_amdgcn_s_sleep(1); return; }
    int n = ((rem - 12) * 24) >> 6; if (n > 16) n = 16;
    for (int i = 0; i < n; ++i) __builtin_amdgcn_s_sleep(1);
  }
}

#define DSTAMP(cond, slot)
#define DVAL(cond, slot, v)
#define DPT(k, ref)
#define DPDUMP(slot)
#define RAW_BARRIER() do { asm volatile("s_waitcnt lgkmcnt(0)" ::: "memory"); __builtin_amdgcn_s_barrier(); asm volatile("" ::: "memory"); } while (0)
constexpr int L_MEM = 0;
constexpr int L_KQ = 10240;
constexpr int L_SP = 43008;
constexpr int L_YP = 90112;
constexpr int L_UIMG = 10240;
constexpr int L_RED = 14336;
constexpr int L_DEAD = 51200;
constexpr int L_PXS = 98304;
constexpr int L_STAGE = 53248;
constexpr int L_X = 0;
constexpr int XBUF = 36864;
constexpr int L_V = 110592;
constexpr int L_WST = 118784;
constexpr int LDS_BYTES = 160 * 1024;

struct MemAddr { unsigned o0, o1, o2, o3; };
DEV MemAddr mem_addr(int rep, int wave, int lane) {
  MemAddr a;
  a.o0 = vo_mem(rep, wave * 3072 + lane * 16);
  a.o1 = vo_mem(rep, wave * 3072 + 1024 + lane * 16);
  a.o2 = vo_mem(rep, wave * 3072 + 2048 + lane * 16);
  a.o3 = vo_mem(rep, 9216);
  return a;
}
template <class H>
DEV unsigned gather_mem(const Params& p, __amdgpu_buffer_rsrc_t rm, char* smem, int wave, int lane, const MemAddr& a, unsigned sb, v4u& v0, v4u& v1, v4u& v2, H behind) {
  v0 = ld_mb(rm, a.o0, sb); v1 = ld_mb(rm, a.o1, sb); v2 = ld_mb(rm, a.o2, sb);
  __builtin_amdgcn_sched_barrier(0);
  behind();
  __builtin_amdgcn_sched_barrier(0);
  unsigned mx = umax(umax(max4(v0), max4(v1)), max4(v2));
  unsigned spins = 0;
  if (__builtin_expect(__builtin_amdgcn_ballot_w64(mx == SENT) != 0, 0)) {
    for (;;) {
      asm volatile("" ::: "memory");
      v0 = ld_mb(rm, a.o0, sb); v1 = ld_mb(rm, a.o1, sb); v2 = ld_mb(rm, a.o2, sb);
      mx = umax(umax(max4(v0), max4(v1)), max4(v2));
      ++spins;
      if (__builtin_amdgcn_ballot_w64(mx == SENT) == 0) break;
      if (spins > SPIN_CAP) { astore(p.ctrl + C_TMO, 1u); *(LAS unsigned*)(smem + L_DEAD) = 1; break; }
      if ((spins & 1023) == 0 && __builtin_amdgcn_readfirstlane(aload(p.ctrl + C_TMO))) { *(LAS unsigned*)(smem + L_DEAD) = 1; break; }
    }
  }
  if (wave < 3) {
    char* d = smem + L_MEM + wave * 3072 + lane * 16;
    *(v4u*)d = v0; *(v4u*)(d + 1024) = v1; *(v4u*)(d + 2048) = v2;
  }
  return spins;
}
DEV v2u sched_fix(__amdgpu_buffer_rsrc_t rm, unsigned off, unsigned sb, v2u v) {
  if (__builtin_expect((unsigned)__builtin_amdgcn_readfirstlane(umax(v.x, v.y)) == SENT, 0)) {
    for (int i = 0; i < 8 && (unsigned)__builtin_amdgcn_readfirstlane(umax(v.x, v.y)) == SENT; ++i) {
      asm volatile("" ::: "memory");
      v = ld2_mb(rm, off, sb);
    }
    if ((unsigned)__builtin_amdgcn_readfirstlane(umax(v.x, v.y)) == SENT) { unsigned n = rt(); v = (v2u){n, n}; }
  }
  return v;
}
DEV void zero_acc(f32x4 (&acc)[4]) {
#pragma unroll
  for (int i = 0; i < 4; ++i) acc[i] = (f32x4){0, 0, 0, 0};
  asm volatile("" : "+v"(acc[0]), "+v"(acc[1]), "+v"(acc[2]), "+v"(acc[3]));
}
DEV void proj36(const char* smem, const v8u (&w)[4][9], int lane, f32x4 (&acc)[4]) {
  const int idx = (lane & 8) ? 0xEEEEEEEE : 0x44444444;
  v4u a[9];
  __builtin_amdgcn_sched_barrier(0);
#pragma unroll
  for (int t = 0; t < 9; ++t) a[t] = *(const v4u*)(smem + L_MEM + t * 1024 + lane * 16);
  __builtin_amdgcn_sched_barrier(0);
#pragma unroll
  for (int t = 0; t < 9; ++t) {
#pragma unroll
    for (int i = 0; i < 4; ++i) acc[i] = smfma(a[t], w[i][t], acc[i], idx);
  }
  __builtin_amdgcn_sched_barrier(0);
}
DEV v2u pairsum(f32x4 a0, f32x4 a1) {
  float r[4];
#pragma unroll
  for (int i = 0; i < 4; ++i) {
    auto sw = __builtin_amdgcn_permlane32_swap(__float_as_uint(a0[i]), __float_as_uint(a1[i]), false, false);
    r[i] = __uint_as_float(sw[0]) + __uint_as_float(sw[1]);
  }
  return (v2u){cvtpk(r[0], r[1]), cvtpk(r[2], r[3])};
}
DEV unsigned read_pol(const Params& p) {
  return __builtin_amdgcn_readfirstlane(aload(p.ctrl + C_POL)) & 0xfffu;
}

DEV void dma16(const void* g, unsigned lds) {
  unsigned keep;
  asm volatile("s_mov_b32 %0, m0\n\ts_mov_b32 m0, %2\n\ts_nop 0\n\tglobal_load_lds_dwordx4 %1, off\n\ts_mov_b32 m0, %0"
               : "=&s"(keep) : "v"(g), "s"(lds) : "memory");
}
DEV void dma16_sc1(const void* g, unsigned lds) {
  unsigned keep;
  asm volatile("s_mov_b32 %0, m0\n\ts_mov_b32 m0, %2\n\ts_nop 0\n\tglobal_load_lds_dwordx4 %1, off sc1\n\ts_mov_b32 m0, %0"
               : "=&s"(keep) : "v"(g), "s"(lds) : "memory");
}
DEV unsigned lds_addr(const char* p) { return __builtin_amdgcn_readfirstlane((unsigned)(size_t)(LAS const char*)p); }
template <int N> DEV void wait_vm() { asm volatile("s_waitcnt vmcnt(%0)" :: "n"(N) : "memory"); }
template <int RB, class FS, class FU>
DEV void staged_load(char* smem, int wave, int lane, FS src, FU use) {
  constexpr int CH = RB / 16, BLK = 64 * RB, PW = BLK / 4096;
  constexpr int NB = (147456 / BLK) < 9 ? (147456 / BLK) : 9;
  const unsigned lb = lds_addr(smem) + wave * PW * 1024;
  auto issue = [&](int t) {
#pragma unroll
    for (int i = 0; i < PW; ++i) {
      const int ch = (wave * PW + i) * 64 + lane;
      dma16(src(64 * t + ch / CH, ch % CH), lb + (t % NB) * BLK + i * 1024);
    }
  };
  RAW_BARRIER();
#pragma unroll
  for (int t = 0; t < NB; ++t) issue(t);
#pragma unroll
  for (int t = 0; t < 9; ++t) {
    constexpr int dummy = 0; (void)dummy;
    const int younger = (t + NB <= 9 ? NB - 1 : 8 - t) * PW;
    if (younger >= 24) wait_vm<24>(); else if (younger >= 16) wait_vm<16>(); else if (younger >= 14) wait_vm<14>(); else if (younger >= 12) wait_vm<12>();
    else if (younger >= 10) wait_vm<10>(); else if (younger >= 8) wait_vm<8>(); else if (younger >= 6) wait_vm<6>(); else if (younger >= 4) wait_vm<4>();
    else if (younger >= 2) wait_vm<2>(); else wait_vm<0>();
    RAW_BARRIER();
    use(t, (t % NB) * BLK);
    if (t + NB < 9) { RAW_BARRIER(); issue(t + NB); }
  }
  RAW_BARRIER();
}

DEV f32x4 mfma16k16(v2u a, v2u b, f32x4 c) {
  return __builtin_amdgcn_mfma_f32_16x16x16bf16_1k(__builtin_bit_cast(s16x4, a), __builtin_bit_cast(s16x4, b), c, 0, 0, 0);
}
template <int WV>
DEV void head_loop(const Params& p, char* smem, int h, int lane, const v8u (&w)[4][9], unsigned pol) {
  constexpr int wave = WV;
  __amdgpu_buffer_rsrc_t rm = mkrsrc(p.mbox, MBOX_BYTES);
  __amdgpu_buffer_rsrc_t rs = mkrsrc(p.span, SPAN_BYTES);
  const unsigned pr = par_r(pol), pw = pr ^ 1u;
  const float SC = 0.08838834764831845f * 1.4426950408889634f;
  const int g = lane >> 4, qi = lane & 15, hi = (lane >> 3) & 1;
  const float NEG = -1e30f;
  const float c0 = (wave == 0 && g >= 2) ? NEG : 0.f;
  const int rep = h & 3;
  v2u selq[2], selk[2];
#pragma unroll
  for (int tl = 0; tl < 2; ++tl) {
    unsigned e[4];
#pragma unroll
    for (int jj = 0; jj < 4; ++jj) e[jj] = (4 * g + jj == 8 * tl + (qi & 7)) ? 0x3F80u : 0u;
    selq[tl] = (v2u){e[0] | (e[1] << 16), e[2] | (e[3] << 16)};
    selk[tl] = hi ? (v2u){0u, 0u} : selq[tl];
  }
  const f32x4 zero4 = {0, 0, 0, 0}, c04 = {c0, c0, c0, c0};
  const MemAddr ma = mem_addr(rep, wave == 3 ? 0 : wave, lane);
  unsigned adx = hi ? vo_p(0, 16384 + (h >> 3) * 1024 + (h & 3) * 256 + ((h >> 2) & 1) * 128 + (qi & 7) * 16)
                    : vo_p(0, (h >> 1) * 1024 + g * 256 + (h & 1) * 128 + (qi & 7) * 16);
  if (hi && g != 0) adx = 0x70000000u;
  auto px_out = [&](int sp) {
    const char* q = smem + L_PXS + (sp & 1) * 2560;
    v4u a0 = *(const v4u*)(q + lane * 16), a1 = *(const v4u*)(q + 1024 + lane * 16), a2 = *(const v4u*)(q + 2048 + (lane & 31) * 16);
    st_sc1(a0, rs, sp_off(LW_PX + sp * 81920 + h * 1024 + lane * 16, pw));
    st_sc1(a1, rs, sp_off(LW_PX + sp * 81920 + (32 + h) * 1024 + lane * 16, pw));
    if (lane < 32) st_sc1(a2, rs, sp_off(LW_PX + sp * 81920 + 65536 + (((lane >> 4) * 8 + (h >> 2)) * 64 + (h & 3) * 16 + (lane & 15)) * 16, pw));
  };
  unsigned tM = rt();
  int kq_rdy = 2;
  for (int s = 0; s < NSEG; ++s) {
    const char* kq = smem + L_KQ + (s & 1) * 16384 + lane * 16;
    v4u kxw[2];
    kxw[0] = *(const v4u*)(kq + wave * 1024); kxw[1] = *(const v4u*)(kq + (4 + wave) * 1024);
    const unsigned sb = __builtin_amdgcn_readfirstlane(sbase(s, pol));
    f32x4 acc[4]; zero_acc(acc);
    f32x4 t0, t1, t2, dq0, dq1, dk0, dk1;
    asm volatile("" : "+v"(kxw[0]), "+v"(kxw[1]));
    const unsigned tM0 = tM; (void)tM0;
    DPT(10, tM0);
    constexpr int T0 = (wave == 3) ? 0 : 3 * wave;
    const int sidx = (lane & 8) ? 0xEEEEEEEE : 0x44444444;
    {
      v4u g3[3];
      wait_until(tM);
      gather_mem(p, rm, smem, wave, lane, ma, sb, g3[0], g3[1], g3[2], [] {});
      __builtin_amdgcn_sched_barrier(0);
#pragma unroll
      for (int i = 0; i < 4; ++i) acc[i] = smfma(g3[0], w[i][T0], acc[i], sidx);
      __builtin_amdgcn_sched_barrier(0);
      DPT(11, tM0);
      RAW_BARRIER();
      v4u a[6];
#pragma unroll
      for (int j = 0; j < 6; ++j) a[j] = *(const v4u*)(smem + L_MEM + ((T0 + 3 + j) % 9) * 1024 + lane * 16);
      __builtin_amdgcn_sched_barrier(0);
#pragma unroll
      for (int j = 1; j < 3; ++j)
#pragma unroll
        for (int i = 0; i < 4; ++i) acc[i] = smfma(g3[j], w[i][T0 + j], acc[i], sidx);
#pragma unroll
      for (int j = 0; j < 6; ++j)
#pragma unroll
        for (int i = 0; i < 4; ++i) acc[i] = smfma(a[j], w[i][(T0 + 3 + j) % 9], acc[i], sidx);
      __builtin_amdgcn_sched_barrier(0);
    }
    DPT(12, tM0);
    v2u sch = ld2_mb(rm, ma.o3, sb);
    v2u pkq = pairsum(acc[0], acc[1]), pkk = pairsum(acc[2], acc[3]);
    asm volatile("" : "+v"(pkq), "+v"(pkk));
    DPT(13, tM0);
    asm("s_nop 1\n\tv_mfma_f32_16x16x16_bf16 %0, %4, %6, 0\n\tv_mfma_f32_16x16x16_bf16 %1, %4, %7, 0\n\t"
        "v_mfma_f32_16x16x16_bf16 %2, %5, %8, 0\n\tv_mfma_f32_16x16x16_bf16 %3, %5, %9, 0\n\ts_nop 7"
        : "=&v"(dq0), "=&v"(dq1), "=&v"(dk0), "=&v"(dk1) : "v"(pkq), "v"(pkk), "v"(selq[0]), "v"(selq[1]), "v"(selk[0]), "v"(selk[1]));
    v4u qf = {cvtpk(dq0[0], dq0[1]), cvtpk(dq0[2], dq0[3]), cvtpk(dq1[0], dq1[1]), cvtpk(dq1[2], dq1[3])};
    v4u kf = {cvtpk(dk0[0], dk0[1]), cvtpk(dk0[2], dk0[3]), cvtpk(dk1[0], dk1[1]), cvtpk(dk1[2], dk1[3])};
    asm("s_nop 1\n\tv_mfma_f32_16x16x32_bf16 %0, %3, %6, 0\n\tv_mfma_f32_16x16x32_bf16 %1, %4, %6, 0\n\t"
        "v_mfma_f32_16x16x32_bf16 %2, %5, %6, %7\n\ts_nop 7"
        : "=&v"(t1), "=&v"(t2), "=&v"(t0) : "v"(kxw[0]), "v"(kxw[1]), "v"(kf), "v"(qf), "v"(c04));
    f32x4 va = {hi ? t2[0] : t1[0], hi ? t2[1] : t1[1], hi ? t2[2] : t1[2], hi ? t2[3] : t1[3]};
    f32x2 vb = {hi ? t0[2] : t0[0], hi ? t0[3] : t0[1]};
    asm volatile("" : "+v"(va), "+v"(vb));
    DPT(14, tM0);
    *(v4u*)(smem + L_YP + wave * 1024 + lane * 16) = kf;
    if (wave > 0) {
      *(f32x4*)(smem + L_SP + (wave - 1) * 1536 + lane * 16) = va;
      *(f32x2*)(smem + L_SP + (wave - 1) * 1536 + 1024 + lane * 8) = vb;
    }
    RAW_BARRIER();
    DPT(15, tM0);
    if (wave == 0) {
      f32x4 pa[3]; f32x2 pb[3];
#pragma unroll
      for (int i = 0; i < 3; ++i) {
        pa[i] = *(const f32x4*)(smem + L_SP + i * 1536 + lane * 16);
        pb[i] = *(const f32x2*)(smem + L_SP + i * 1536 + 1024 + lane * 8);
      }
      __builtin_amdgcn_sched_barrier(0);
      va = (va + pa[0]) + (pa[1] + pa[2]);
      vb = (vb + pb[0]) + (pb[1] + pb[2]);
      float m = fmaxf(fmaxf(fmaxf(va[0], va[1]), va[2]), fmaxf(fmaxf(va[3], vb[0]), vb[1]));
      m = xmax8(m);
      const float ms = -m * SC;
      float v0 = __builtin_amdgcn_exp2f(fmaf(va[0], SC, ms)), v1 = __builtin_amdgcn_exp2f(fmaf(va[1], SC, ms)), v2 = __builtin_amdgcn_exp2f(fmaf(va[2], SC, ms));
      float v3 = __builtin_amdgcn_exp2f(fmaf(va[3], SC, ms)), v4 = __builtin_amdgcn_exp2f(fmaf(vb[0], SC, ms)), v5 = __builtin_amdgcn_exp2f(fmaf(vb[1], SC, ms));
      float l = ((v0 + v1) + (v2 + v3)) + (v4 + v5);
      l += dpp<ROR8>(l);
      l = xsum(l);
      float inv = __builtin_amdgcn_rcpf(l);
      v2u px = {umin(cvtpk(v0 * inv, v1 * inv), 0xFFFFFFFEu), umin(cvtpk(v2 * inv, v3 * inv), 0xFFFFFFFEu)};
      unsigned pm = umin(cvtpk(v4 * inv, v5 * inv), 0xFFFFFFFEu);
      unsigned ox = __builtin_amdgcn_mov_dpp(px.x, ROR8, 0xf, 0xf, true), oy = __builtin_amdgcn_mov_dpp(px.y, ROR8, 0xf, 0xf, true);
      unsigned pmo = __builtin_amdgcn_mov_dpp(pm, ROR8, 0xf, 0xf, true);
      auto s1 = __builtin_amdgcn_permlane16_swap(pm, pm, false, false);
      auto s2 = __builtin_amdgcn_permlane16_swap(pmo, pmo, false, false);
      v4u o;
      o.x = hi ? pmo : px.x; o.y = hi ? pm : px.y; o.z = hi ? s2[1] : ox; o.w = hi ? s1[1] : oy;
#pragma unroll
      for (int rp = 0; rp < NREP; ++rp) st_mb(o, rm, adx, sb + rp * 81920);
      *(volatile LAS int*)(smem + L_DEAD + 64) = s + 1;
    }
    DPT(16, tM0);
    if (wave == 3) {
      for (int i = 0; i < 4096 && *(volatile LAS int*)(smem + L_DEAD + 64) != s + 1; ++i) __builtin_amdgcn_s_sleep(1);
      if (s > 1 && lane == 0 && (!(s & 1) || s >= 58)) astore(p.ctrl + C_PX + h * 32, (unsigned)(s - 1));
      if (s > 0) px_out(s - 1);
    }
    if (wave == 3 || wave == 0) {
      const int sn = min(s + 1, NSEG - 1);
      const char* src = p.span + sp_off((wave == 0 ? LR_KX : LR_QX) + (sn * 32 + h) * 8192, pr) + lane * 16;
      const unsigned ldst = lds_addr(smem) + L_KQ + ((s + 1) & 1) * 16384 + (wave == 0 ? 0 : 8192);
      unsigned kf8 = 1u;
      const bool chk = (wave == 0) && kq_rdy < s + 3 && kq_rdy < NSEG;
      if (chk) kf8 = aload(p.ctrl + C_KQ + min(s + 2 + (lane & 7), NSEG - 1) * 32 + h);
#pragma unroll
      for (int i = 0; i < 8; ++i) dma16_sc1(src + i * 1024, ldst + i * 1024);
      if (chk) {
        const unsigned long long nb = __builtin_amdgcn_ballot_w64(kf8 == 0u);
        kq_rdy = s + 2 + (nb ? (int)__builtin_ctzll(nb) : 8);
        __builtin_amdgcn_fence(__ATOMIC_ACQUIRE, "wavefront");
        if (kq_rdy < s + 3) {
          if (!wait_ge(p.ctrl + C_KQ + min(s + 2, NSEG - 1) * 32 + h, 1u, p.ctrl + C_TMO, 2u)) *(LAS unsigned*)(smem + L_DEAD) = 1;
          kq_rdy = s + 3;
        }
      }
      drain();
    }
    if (wave == 1 || wave == 2) {
      constexpr int qt = (wave == 2) ? 1 : 0;
      f32x4 x1 = zero4, x2 = zero4, y = zero4;
#pragma unroll
      for (int u = 0; u < 4; ++u) {
        v4u q = *(const v4u*)(kq + 8192 + (qt * 4 + u) * 1024);
        x1 = mfma16(*(const v4u*)(kq + u * 1024), q, x1); x2 = mfma16(*(const v4u*)(kq + (4 + u) * 1024), q, x2);
        y = mfma16(*(const v4u*)(smem + L_YP + u * 1024 + lane * 16), q, y);
      }
      float v[12];
      const int tq = 16 * qt + qi;
#pragma unroll
      for (int r = 0; r < 4; ++r) {
        v[r] = (g < 2) ? y[r] * SC : NEG;
        v[4 + r] = (4 * g + r <= tq) ? x1[r] * SC : NEG;
        v[8 + r] = (16 + 4 * g + r <= tq) ? x2[r] * SC : NEG;
      }
      float m = amax3(amax3(v[0], v[1], v[2]), amax3(v[3], v[4], v[5]), fmaxf(amax3(v[6], v[7], v[8]), amax3(v[9], v[10], v[11])));
      m = xmax(m);
      float l = 0;
#pragma unroll
      for (int i = 0; i < 12; ++i) { v[i] = __builtin_amdgcn_exp2f(v[i] - m); l += v[i]; }
      l = xsum(l);
      float inv = __builtin_amdgcn_rcpf(l);
#pragma unroll
      for (int i = 0; i < 12; ++i) v[i] *= inv;
      v4u px = {cvtpk(v[4], v[5]), cvtpk(v[6], v[7]), cvtpk(v[8], v[9]), cvtpk(v[10], v[11])};
      v2u pm = {cvtpk(v[0], v[1]), cvtpk(v[2], v[3])};
      *(v4u*)(smem + L_PXS + (s & 1) * 2560 + qt * 1024 + lane * 16) = px;
      if (g < 2) *(v2u*)(smem + L_PXS + (s & 1) * 2560 + 2048 + qt * 256 + qi * 16 + g * 8) = pm;
    }
    sch = sched_fix(rm, ma.o3, sb, sch);
    tM = __builtin_amdgcn_readfirstlane(sch.y);
    RAW_BARRIER();
    if (*(volatile LAS unsigned*)(smem + L_DEAD)) break;
  }
  if (wave == 3) { px_out(NSEG - 1); drain(); if (lane == 0) astore(p.ctrl + C_PX + h * 32, (unsigned)NSEG); }
}

DEV void head_role(const Params& p, char* smem, int h, int wave, int lane, int tid) {
  v8u w[4][9];
  {
    const int gB = lane >> 4, q4 = (lane & 15) >> 2, p4 = lane & 3;
    staged_load<512>(smem, wave, lane,
      [&](int row, int cc) { return (cc < 16 ? p.Wq : p.Wk) + (size_t)row * 4096 + h * 128 + (cc & 15) * 8; },
      [&](int t, int base) {
#pragma unroll
        for (int tl = 0; tl < 4; ++tl) {
          v8u ww;
#pragma unroll
          for (int X = 0; X < 2; ++X)
#pragma unroll
            for (int half = 0; half < 2; ++half) {
              int row0 = 8 * (4 * half + 2 * X + (gB >> 1)) + 4 * (gB & 1);
              v2u v = tr_read(smem, base + (row0 + q4) * 512 + ((tl >> 1) * 128 + 32 * wave + 16 * (tl & 1) + 4 * p4) * 2);
              ww[4 * X + half] = v.x; ww[4 * X + 2 + half] = v.y;
            }
          if (tl >= 2 || (tl == 1 && t < 3)) asm volatile("" : "+a"(ww));
          w[tl][t] = ww;
        }
      });
  }
  DSTAMP(h == 0 && wave == 0, 0);
  if (tid == 0) { *(unsigned*)(smem + L_DEAD) = 0; *(unsigned*)(smem + L_DEAD + 64) = 0; }
  if (wave == 0) { wait_ge(p.ctrl + C_CLR, NSPARE, p.ctrl + C_TMO, 3u); wait_ge(p.ctrl + C_KQ + h, 1u, p.ctrl + C_TMO, 4u); wait_ge(p.ctrl + C_KQ + 32 + h, 1u, p.ctrl + C_TMO, 4u); }
  __syncthreads();
  const unsigned pol = read_pol(p);
  {
    __amdgpu_buffer_rsrc_t rs = mkrsrc(p.span, SPAN_BYTES);
    v4u v[4];
#pragma unroll
    for (int i = 0; i < 4; ++i) { int f = wave * 4 + i; v[i] = ld_sc1(rs, sp_off((wave < 2 ? LR_KX : LR_QX) + h * 8192 + (f & 7) * 1024 + lane * 16, par_r(pol))); }
#pragma unroll
    for (int i = 0; i < 4; ++i) *(v4u*)(smem + L_KQ + (wave * 4 + i) * 1024 + lane * 16) = v[i];
  }
  __syncthreads();
  DSTAMP(h == 0 && wave == 0, 1);
  if (wave == 0) head_loop<0>(p, smem, h, lane, w, pol);
  else if (wave == 1) head_loop<1>(p, smem, h, lane, w, pol);
  else if (wave == 2) head_loop<2>(p, smem, h, lane, w, pol);
  else head_loop<3>(p, smem, h, lane, w, pol);
}

constexpr int L_WOST = 0;
constexpr int L_AST = 118784;
DEV void load_wo(const Params& p, char* smem, int h, int par, int wave, int lane, int tid, v4u (&wo)[9][4]) {
  constexpr int RS = 1152;
  const int g = lane >> 4, q4 = (lane & 15) >> 2, p4 = lane & 3;
  const char* src = (const char*)(p.Wo + (size_t)h * 128 * 576) + lane * 16;
  RAW_BARRIER();
#pragma unroll
  for (int hf = 0; hf < 2; ++hf)
#pragma unroll
    for (int i = 0; i < 18; ++i) {
      const int pc = (hf ^ par) * 72 + wave * 18 + i;
      dma16(src + pc * 1024, lds_addr(smem) + pc * 1024);
    }
#pragma unroll
  for (int hf = 0; hf < 2; ++hf) {
    if (hf == 0) { wait_vm<0>(); RAW_BARRIER(); }
#pragma unroll
    for (int tl = 0; tl < 9; ++tl)
#pragma unroll
      for (int uu = 0; uu < 2; ++uu) {
        int a0 = (64 * hf + 32 * uu + 4 * g + q4) * RS + ((wave * 9 + tl) * 16 + 4 * p4) * 2;
        v2u x0 = tr_read(smem, a0), x1 = tr_read(smem, a0 + 16 * RS);
        wo[tl][2 * hf + uu] = (v4u){x0.x, x0.y, x1.x, x1.y};
      }
  }
  RAW_BARRIER();
}
struct ABlk { v4u v[4]; };
DEV void wv_block_load(const Params& p, int h, int t, int tid, ABlk& b) {
#pragma unroll
  for (int i = 0; i < 4; ++i) { int ch = tid + 256 * i; b.v[i] = *(const v4u*)(p.Wv + (size_t)(64 * t + (ch >> 4)) * 4096 + h * 128 + (ch & 15) * 8); }
}
template <int TL0, int TL1>
DEV void wvo_unit(const Params& p, char* smem, int h, int t, int slot, int wave, int lane, int tid, const ABlk& blk, const v4u (&wo)[9][4], unsigned pr) {
  const int g = lane >> 4;
  __amdgpu_buffer_rsrc_t rw = mkrsrc(p.span, SPAN_BYTES);
  char* ab = smem + L_AST + slot * 17408;
#pragma unroll
  for (int i = 0; i < 4; ++i) { int ch = tid + 256 * i; *(v4u*)(ab + (ch >> 4) * 272 + (ch & 15) * 16) = blk.v[i]; }
  RAW_BARRIER();
  v4u a[4][4];
#pragma unroll
  for (int tt = 0; tt < 4; ++tt)
#pragma unroll
    for (int u = 0; u < 4; ++u) {
      const char* src = ab + (16 * tt + (lane & 15)) * 272 + (32 * u + 4 * g) * 2;
      v2u x0 = *(const v2u*)src, x1 = *(const v2u*)(src + 32);
      a[tt][u] = (v4u){x0.x, x0.y, x1.x, x1.y};
    }
#pragma unroll
  for (int tl = TL0; tl < TL1; ++tl) {
    f32x4 d[4];
#pragma unroll
    for (int tt = 0; tt < 4; ++tt) {
      d[tt] = (f32x4){0, 0, 0, 0};
#pragma unroll
      for (int u = 0; u < 4; ++u) d[tt] = mfma16(a[tt][u], wo[tl][u], d[tt]);
    }
    const int n = (wave * 9 + tl) * 16 + (lane & 15), c = n >> 3;
    unsigned off = sp_off(LR_WVO + ((c * 16 + (h >> 1)) * 9 + t) * 2048 + (g * 16 + (h & 1) * 8 + (n & 7)) * 16, pr);
    st_sc1((v4u){cvtpk(d[0][0], d[0][1]), cvtpk(d[2][0], d[2][1]), cvtpk(d[0][2], d[0][3]), cvtpk(d[2][2], d[2][3])}, rw, off);
    st_sc1((v4u){cvtpk(d[1][0], d[1][1]), cvtpk(d[3][0], d[3][1]), cvtpk(d[1][2], d[1][3]), cvtpk(d[3][2], d[3][3])}, rw, off + 1024);
  }
}

#define SCHED_A0 126
#define SCHED_B0 72
#define LATE0 78
#define WARM_N 0
#define WARM_K 2
#define TU 0
#define PF_KQ 1
#define PF_LEAD 2
#define PF_WIN 88
#define DEC_EVERY 5
DEV void col_role(const Params& p, char* smem, int c, int wave, int lane, int tid) {
  __amdgpu_buffer_rsrc_t rm = mkrsrc(p.mbox, MBOX_BYTES);
  __amdgpu_buffer_rsrc_t rs = mkrsrc(p.span, SPAN_BYTES);
  if (c < 64) {
    const int hh = ((c >> 3) << 2) | (c & 3), par = (c >> 2) & 1;
    ABlk b0, b1, b2;
    v4u wo[9][4];
    wv_block_load(p, hh, par, tid, b0); wv_block_load(p, hh, par + 2, tid, b1); wv_block_load(p, hh, par + 4, tid, b2);
    load_wo(p, smem, hh, par, wave, lane, tid, wo);
    wg_wait_ge(p.ctrl + C_POL, 0x1000u, p.ctrl + C_TMO, 9u, wave);
    const unsigned pr0 = par_r(read_pol(p));
    wvo_unit<0, 9>(p, smem, hh, par, 0, wave, lane, tid, b0, wo, pr0);
    wvo_unit<0, 9>(p, smem, hh, par + 2, 1, wave, lane, tid, b1, wo, pr0);
    wvo_unit<0, 9>(p, smem, hh, par + 4, 0, wave, lane, tid, b2, wo, pr0);
    wg_signal(p.ctrl + C_WVO + 32, tid);
  }
  if (tid == 0) *(unsigned*)(smem + L_DEAD) = 0;
  if (wave == 0) wait_ge(p.ctrl + C_CLR, NSPARE, p.ctrl + C_TMO, 5u);
  __syncthreads();
  DSTAMP(c == 0 && wave == 0, 2);
  const unsigned pol = read_pol(p);
  const int rep = c & 3;
  const unsigned slice = (c >> 3) * 1024 + ((c & 3) * 16 + ((c >> 2) & 1) * 8) * 16;
  if (wave == 0) {
    const unsigned sb0 = __builtin_amdgcn_readfirstlane(sbase(0, pol));
    if (lane < 8 * NREP) {
      int r = lane & 7, rp = lane >> 3;
      v4u v = *(const v4u*)(p.mem0 + r * 576 + 8 * c);
      v.x = umin(v.x, 0xFFFFFFFEu); v.y = umin(v.y, 0xFFFFFFFEu); v.z = umin(v.z, 0xFFFFFFFEu); v.w = umin(v.w, 0xFFFFFFFEu);
      st_mb(v, rm, vo_mem(rp, slice + r * 16), sb0);
    }
    if (c == 0) {
      unsigned now = rt(); if (now == SENT) now = 0;
      if (lane < NREP) st_mb((v4u){now, now, 0u, 0u}, rm, vo_mem(lane, 9216), sb0);
    }
    wait_ge(p.ctrl + C_WVO, 64u, p.ctrl + C_TMO, 6u);
  }
  __syncthreads();
  v8u w[4][9];
#pragma unroll
  for (int t = 8; t >= 0; --t) {
    if (t == 5) { wg_wait_ge(p.ctrl + C_WVO + 32, 64u, p.ctrl + C_TMO, 6u, wave); DSTAMP(c == 0 && wave == 0, 3); }
#pragma unroll
    for (int tl = 0; tl < 4; ++tl) {
      unsigned off = sp_off(LR_WVO + ((c * 16 + wave * 4 + tl) * 9 + t) * 2048, par_r(pol)) + lane * 16;
      v4u x0 = ld_sc1(rs, off), x1 = ld_sc1(rs, off + 1024);
      w[tl][t] = __builtin_shufflevector(x0, x1, 0, 1, 2, 3, 4, 5, 6, 7);
    }
  }
#pragma unroll
  for (int tl = 1; tl < 4; ++tl)
#pragma unroll
    for (int t = 0; t < 9; ++t)
      if (tl >= 2 || t < 3) asm volatile("" : "+a"(w[tl][t]));
  auto loop = [&](auto WV) {
  constexpr int wave = decltype(WV)::value;
  const int g = lane >> 4, half = (lane >> 3) & 1, n7 = lane & 7;
  const int uw_off = L_UIMG + (((2 * (4 * wave + (lane >> 5)) + ((lane & 15) >> 3)) * 8 + (lane & 7)) * 8 + 4 * ((lane >> 4) & 1)) * 2;
  const int red_w = ((g >> 1) == half) ? L_RED + (((wave * 2 + half) * 8 + n7) * 8 + 4 * (g & 1)) * 4 : L_RED + 2048 + lane * 16;
  const int red_r = L_RED + (((lane >> 1) & 7) * 8 + 4 * (lane & 1)) * 4;
  const MemAddr ma = mem_addr(rep, wave, lane);
  const unsigned po = vo_p(rep, wave * 4096 + lane * 16), pm_o = vo_p(rep, 16384 + wave * 1024 + lane * 16);
  const unsigned ado = (lane & 1) ? 0x70000000u : vo_mem(lane >> 4, slice + ((lane >> 1) & 7) * 16);
  const unsigned pw = par_r(pol) ^ 1u;
  DSTAMP(c == 0 && wave == 0, 20);
  wait_ge(p.ctrl + C_UX, 32u, p.ctrl + C_TMO, 7u);
  DSTAMP(c == 0 && wave == 0, 21);
  int rdy = 1;
  unsigned tM = rt();
  int schedA = SCHED_A0, schedB = SCHED_B0, streak = 0, flip = 0; bool prevLate = true; int lateM = LATE0;
  unsigned sb = __builtin_amdgcn_readfirstlane(sbase(0, pol));
  for (int s = 0; s < NSEG; ++s) {
    f32x4 acc[4]; zero_acc(acc);
    unsigned mfail = 0;
    const unsigned tM0 = tM; (void)tM0;
    DPT(0, tM0);
    v4u ux[4];
    const unsigned uso = __builtin_amdgcn_readfirstlane(sp_off(LW_UX + (s * 72 + c) * 16384 + wave * 4096, pw));
    auto ldux = [&] {
#pragma unroll
      for (int q = 0; q < 4; ++q) ux[q] = __builtin_amdgcn_raw_buffer_load_b128(rs, q * 1024 + lane * 16, uso, 16);
    };
    if (wave < 3) { wait_until(tM); v4u g0, g1, g2; mfail = gather_mem(p, rm, smem, wave, lane, ma, sb, g0, g1, g2, [] {}); }
    DPT(1, tM0);
    RAW_BARRIER();
    DPT(2, tM0);
    ldux();
    v2u sch = ld2_mb(rm, ma.o3, sb);
    unsigned ofail = SENT;
    if (wave == 3 && c == 0) ofail = __builtin_amdgcn_raw_buffer_load_b32(rm, vo_mem(0, 9216 + 128 * (1 + (lane % 3))), sb, 16);
    unsigned fl2 = 32u;
    const bool chk = rdy < s + 3 && rdy < NSEG;
    if (__builtin_expect(chk, 0)) fl2 = aload(p.ctrl + C_UX + lane);
    if (TU > 0 && s < NSEG - 1) {
      sch = sched_fix(rm, ma.o3, sb, sch);
      wait_until(__builtin_amdgcn_readfirstlane(sch.x) - (unsigned)TU);
    }
    proj36(smem, w, lane, acc);
    v2u pk0 = pairsum(acc[0], acc[1]), pk1 = pairsum(acc[2], acc[3]);
    asm volatile("" : "+v"(pk0), "+v"(pk1));
    DPT(3, tM0);
    *(v2u*)(smem + uw_off) = pk0;
    *(v2u*)(smem + uw_off + 512) = pk1;
    v4u ub = *(const v4u*)(smem + L_UIMG + ((8 * wave + 4 * half + g) * 8 + n7) * 16);
    const unsigned sbn = __builtin_amdgcn_readfirstlane(sbase(s + 1, pol));
    const unsigned umo = __builtin_amdgcn_readfirstlane(sp_off(LW_UM + (s * 72 + c) * 4096, pw));
    if (s < NSEG - 1) {
      asm volatile("s_waitcnt vmcnt(0)" : "+v"(ux[0]), "+v"(ux[1]), "+v"(ux[2]), "+v"(ux[3]), "+v"(ub), "+v"(fl2), "+v"(sch), "+v"(ofail) :: "memory");
      if (__builtin_expect(chk, 0)) {
        unsigned long long nb = __builtin_amdgcn_ballot_w64(fl2 < 32u);
        rdy = nb ? (int)__builtin_ctzll(nb) : NSEG;
        __builtin_amdgcn_fence(__ATOMIC_ACQUIRE, "wavefront");
        if (rdy < s + 2) {
          if (!wait_ge(p.ctrl + C_UX + s + 1, 32u, p.ctrl + C_TMO, 7u)) *(LAS unsigned*)(smem + L_DEAD) = 1;
          rdy = s + 2;
        }
      }
      DPT(4, tM0);
      sch = sched_fix(rm, ma.o3, sb, sch);
      const unsigned tP = __builtin_amdgcn_readfirstlane(sch.x);
      tM = __builtin_amdgcn_readfirstlane(sch.y);
      f32x4 d = {0, 0, 0, 0};
      asm volatile("" : "+v"(d));
      DPT(5, tM0);
      wait_until(tP);
      DPT(6, tP);
      unsigned pfail = 0;
      {
        v4u v[5];
#pragma unroll
        for (int q = 0; q < 4; ++q) v[q] = ld_mb(rm, po + q * 1024, sb);
        v[4] = ld_mb(rm, pm_o, sb);
        unsigned mx = 0;
#pragma unroll
        for (int q = 0; q < 4; ++q) { d = mfma16(ux[q], v[q], d); mx = umax(mx, max4(v[q])); }
        d = mfma16(ub, v[4], d); mx = umax(mx, max4(v[4]));
        if (__builtin_expect(__builtin_amdgcn_ballot_w64(mx == SENT) != 0, 0)) {
          for (;;) {
            asm volatile("" ::: "memory");
#pragma unroll
            for (int q = 0; q < 4; ++q) v[q] = ld_mb(rm, po + q * 1024, sb);
            v[4] = ld_mb(rm, pm_o, sb);
            mx = 0;
#pragma unroll
            for (int q = 0; q < 5; ++q) mx = umax(mx, max4(v[q]));
            ++pfail;
            if (__builtin_amdgcn_ballot_w64(mx == SENT) == 0) break;
            if (pfail > SPIN_CAP) { astore(p.ctrl + C_TMO, 8u); *(LAS unsigned*)(smem + L_DEAD) = 1; break; }
            if ((pfail & 1023) == 0 && __builtin_amdgcn_readfirstlane(aload(p.ctrl + C_TMO))) { *(LAS unsigned*)(smem + L_DEAD) = 1; break; }
          }
          d = (f32x4){0, 0, 0, 0};
#pragma unroll
          for (int q = 0; q < 4; ++q) d = mfma16(ux[q], v[q], d);
          d = mfma16(ub, v[4], d);
        }
      }
      asm volatile("" : "+v"(d));
      DPT(7, tP);
      *(f32x4*)(smem + red_w) = d;
      if (lane == 0) *(LAS unsigned*)(smem + L_DEAD + 16 + wave * 4) = (mfail ? 1u : 0u) | (pfail ? 2u : 0u);
      RAW_BARRIER();
      if (wave == 0) {
        f32x4 xb[8];
#pragma unroll
        for (int b8 = 0; b8 < 8; ++b8) xb[b8] = *(const f32x4*)(smem + red_r + b8 * 256);
        __builtin_amdgcn_sched_barrier(0);
        f32x4 x = ((xb[0] + xb[1]) + (xb[2] + xb[3])) + ((xb[4] + xb[5]) + (xb[6] + xb[7]));
        float r0 = x[0], r1 = x[1], r2 = x[2], r3 = x[3];
        v2u o2 = {umin(cvtpk(r0, r1), 0xFFFFFFFEu), umin(cvtpk(r2, r3), 0xFFFFFFFEu)};
        v4u o4 = {o2.x, o2.y, (unsigned)__builtin_amdgcn_mov_dpp(o2.x, 0xB1, 0xf, 0xf, true), (unsigned)__builtin_amdgcn_mov_dpp(o2.y, 0xB1, 0xf, 0xf, true)};
        st_mb(o4, rm, ado, sbn);
        DPT(8, tP);
      } else if (wave == 3 && c >= 1 && c <= 3) {
        v4u f = *(const v4u*)(smem + L_DEAD + 16);
        if (((f.x | f.y | f.z | f.w) & 2u) && lane == 0) __builtin_amdgcn_raw_buffer_store_b32(0u, rm, vo_mem(0, 9216 + 128 * c), sbn, 16);
      } else if (wave == 3 && c == 0) {
        v4u f = *(const v4u*)(smem + L_DEAD + 16);
        unsigned fl = __builtin_amdgcn_readfirstlane(f.x | f.y | f.z | f.w);
        unsigned now = rt();
        unsigned baseT = tM;
        const bool late = (int)(now - tM) > 2 || (int)(tM - now) > 600;
        if (late) baseT = now + (unsigned)lateM;
        if (prevLate && (fl & 2u)) { if (lateM < 80) lateM += 6; } else if (!late && !prevLate && lateM > LATE0) lateM -= 2;
        if (!prevLate) {
          const bool oth = __builtin_amdgcn_ballot_w64(ofail == 0u) != 0;
          if (fl & 1u) { if (oth) schedA += 2; else schedB += 2; }
          else if (fl & 2u) schedA += 2;
          else if (late) schedB += 2;
          else if (++streak >= DEC_EVERY) { streak = 0; if (++flip & 1) schedA -= 1; else schedB -= 1; }
        }
        prevLate = late || (fl & 2u);
        const int warm = s < WARM_N ? (WARM_N - s) * WARM_K : 0;
        unsigned nP = baseT + (unsigned)(schedA + warm), nM = nP + (unsigned)(schedB + warm);
        if (nP == SENT) nP = 0; if (nM == SENT) nM = 0;
        if (lane < NREP) st_mb((v4u){nP, nM, 0u, 0u}, rm, vo_mem(lane, 9216), sbn);
        DVAL(true, 64 + s, now); DVAL(true, 128 + s, fl | (schedA << 8) | (schedB << 20));
      }
    }
    else { __builtin_amdgcn_raw_buffer_store_b128(ub, rs, wave * 1024 + lane * 16, umo, 16); drain(); RAW_BARRIER(); }
    if (s < NSEG - 1) __builtin_amdgcn_raw_buffer_store_b128(ub, rs, wave * 1024 + lane * 16, umo, 16);
    if (wave == 3 && lane == 0 && ((s & 1) || s >= 58)) astore(p.ctrl + C_UM + c * 32, (unsigned)(s + 1 >= NSEG ? NSEG : s));
    sb = sbn;
    if (*(volatile LAS unsigned*)(smem + L_DEAD)) break;
  }
  };
  if (wave == 0) loop(IC<0>{}); else if (wave == 1) loop(IC<1>{}); else if (wave == 2) loop(IC<2>{}); else loop(IC<3>{});
  __syncthreads();
}

DEV void spare_role(const Params& p, char* smem, int j, int wave, int lane, int tid) {
  __amdgpu_buffer_rsrc_t rm = mkrsrc(p.mbox, MBOX_BYTES);
  if (j == 0 && tid == 0) {
    const unsigned grp = (__builtin_amdgcn_s_getreg(20 | (3 << 11)) >> 2) & 1u;
    unsigned m = 0;
    for (int i = 0; i < NPAGES; ++i) { unsigned r = p.ctrl[C_CAL + i]; m |= ((r ^ (r >> 1) ^ grp) & 1u) << i; }
    const unsigned full = (1u << NPAGES) - 1u, alt = 0x555u & full;
    const int agree = __builtin_popcount(~(m ^ alt) & full);
    if (agree >= NPAGES - 3) m = alt; else if (agree <= 3) m = ~alt & full;
    astore(p.ctrl + C_POL, 0x1000u | m);
  }
  wg_wait_ge(p.ctrl + C_POL, 0x1000u, p.ctrl + C_TMO, 9u, wave);
  const unsigned pol = read_pol(p);
  DSTAMP(j == 0 && wave == 0, 4); DVAL(j == 0 && wave == 0, 5, pol);
  {
    const v4u sv = {SENT, SENT, SENT, SENT};
    for (int u = j; u < NSEG * NREP * 5; u += NSPARE) {
      const int st = u / (NREP * 5), cell = u % (NREP * 5), k = cell % 5;
      const unsigned sb = __builtin_amdgcn_readfirstlane(sbase(st, pol));
      if (k != 1 || tid < 128) st_mb(sv, rm, cell * 16384 + tid * 16, sb);
      if (k != 1 && k != 4) st_mb(sv, rm, cell * 16384 + 4096 + tid * 16, sb);
    }
  }
  wg_signal(p.ctrl + C_CLR, tid);
  DSTAMP(j == 0 && wave == 0, 6);
  {
    __amdgpu_buffer_rsrc_t rs = mkrsrc(p.span, SPAN_BYTES);
    const unsigned pr = par_r(pol);
    const int xg = j & 3, rk = j >> 2;
    auto wait_slot = [&](int k) {
      const unsigned sbk = __builtin_amdgcn_readfirstlane(sbase(k, pol));
      unsigned tp = SENT;
      for (unsigned spins = 0; spins < SPIN_CAP; ++spins) {
        asm volatile("" ::: "memory");
        v2u sc2 = ld2_mb(rm, vo_mem(xg, 9216), sbk);
        tp = __builtin_amdgcn_readfirstlane(sc2.x);
        if (tp != SENT) break;
        __builtin_amdgcn_s_sleep(8);
        if ((spins & 63u) == 63u && __builtin_amdgcn_readfirstlane(aload(p.ctrl + C_TMO))) break;
      }
      return tp;
    };
    auto relay = [&]() {
      if (j == 0) {
        unsigned a = aload(p.ctrl + C_UM + lane * 32), b = aload(p.ctrl + (lane < 8 ? C_UM + (64 + lane) * 32 : C_PX + (lane & 31) * 32));
        unsigned c3 = aload(p.ctrl + C_PX + (lane & 7) * 32);
        unsigned m = umin(umin(a, b), c3);
#pragma unroll
        for (int o = 1; o < 64; o <<= 1) m = umin(m, (unsigned)__shfl_xor((int)m, o));
        if (lane < 16) __hip_atomic_fetch_max(p.ctrl + C_PROG + lane * 32, m, __ATOMIC_RELAXED, __HIP_MEMORY_SCOPE_AGENT);
      }
    };
    constexpr int LEAD = PF_LEAD;
    int rdy = 0;
    for (int s = 1; s < NSEG; ++s) {
      if (wave == 0) {
        if (rdy <= s) {
          wait_slow(p.ctrl + C_UX + s, 32u, p.ctrl + C_TMO, 15u);
          wait_flags(p.ctrl + C_KQ + s * 32, 32, 1, 1u, lane, p.ctrl + C_TMO, 18u);
          rdy = s + 1;
          const int s2 = min(s + 4, NSEG - 1);
          unsigned u5 = aload(p.ctrl + C_UX + min(s + 1 + (lane & 3), NSEG - 1));
          unsigned k5a = aload(p.ctrl + C_KQ + min(s + 1 + (lane >> 5), NSEG - 1) * 32 + (lane & 31));
          unsigned k5b = aload(p.ctrl + C_KQ + min(s + 3 + (lane >> 5), NSEG - 1) * 32 + (lane & 31));
          if (__builtin_amdgcn_ballot_w64(u5 < 32u || k5a == 0u || k5b == 0u) == 0) rdy = s2 + 1;
          __builtin_amdgcn_fence(__ATOMIC_ACQUIRE, "wavefront");
        }
        if (s > LEAD) {
          unsigned tp = wait_slot(s - LEAD);
          wait_until(tp - (unsigned)PF_WIN);
        }
      }
      __syncthreads();
      v4u v[12];
#pragma unroll
      for (int i = 0; i < 12; ++i) v[i] = ld_sc1(rs, sp_off(LW_UX + (s * 72 + xg + 4 * (3 * rk + i / 4)) * 16384 + ((i & 3) * 256 + tid) * 16, pr ^ 1u));
      v4u k6[6];
#pragma unroll
      for (int i = 0; i < 6; ++i) {
        int ch = rk * 6 + i;
        unsigned off = (ch < 32) ? sp_off(((ch >> 1) & 1) * SZ_KX + (s * 32 + xg + 4 * (ch >> 2)) * 8192 + (ch & 1) * 4096 + tid * 16, pr) : 0x7ffffff0u;
        k6[i] = ld_sc1(rs, off);
      }
#pragma unroll
      for (int i = 0; i < 6; ++i) asm volatile("" :: "v"(k6[i]));
#pragma unroll
      for (int i = 0; i < 12; ++i) asm volatile("" :: "v"(v[i]));
      if (wave == 0 && s > LEAD && !(s & 1)) relay();
    }
    drain();
    if (j == 0 && wave == 0) {
      for (int k = NSEG - LEAD; k < NSEG; ++k) { unsigned tp = wait_slot(k); wait_until(tp - 80u); relay(); }
      for (unsigned spins = 0; spins < SPIN_CAP && (unsigned)__builtin_amdgcn_readfirstlane(aload(p.ctrl + C_PROG)) < 60u; ++spins) {
        relay();
        __builtin_amdgcn_s_sleep(8);
        if (__builtin_amdgcn_readfirstlane(aload(p.ctrl + C_TMO))) break;
      }
    }
  }
}

DEV int tile_src_off(int wave, int lane) {
  const int row = wave * 8 + (lane >> 3), r = row & 15;
  return row * 576 + (((lane & 7) ^ ((r >> 1) & 7)) * 8);
}
struct XDma { const u16* src; char* dst; bool on; };
DEV XDma x_dma_setup(const u16* x, char* smem, int s, int buf, int wave, int lane, bool on) {
  XDma d;
  d.src = x + (size_t)s * 32 * 576 + tile_src_off(wave, lane);
  d.dst = smem + L_X + buf * XBUF + wave * 1024;
  d.on = on;
  return d;
}
DEV void x_dma_piece(const XDma& d, int wave, int i) {
  if (d.on) __builtin_amdgcn_global_load_lds((const unsigned*)(d.src + i * 64), (LAS unsigned*)(d.dst + i * 4096), 16, 0, 0);
}
template <int DEPTH, class F, class G>
DEV void xloop(const char* tile, int lane, F body, G side) {
  const int r = lane & 15, g = lane >> 4, f = (r >> 1) & 7;
  const char* pe = tile + (r >> 3) * 1024 + (r & 7) * 128 + ((g ^ f) * 16);
  const char* po = tile + (r >> 3) * 1024 + (r & 7) * 128 + (((4 + g) ^ f) * 16);
  auto rd = [&](int ks, int tt) { return *(const v4u*)(((ks & 1) ? po : pe) + (ks >> 1) * 4096 + tt * 2048); };
  v4u f0[DEPTH + 1], f1[DEPTH + 1];
#pragma unroll
  for (int i = 0; i < DEPTH; ++i) { f0[i] = rd(i, 0); f1[i] = rd(i, 1); }
#pragma unroll
  for (int ks = 0; ks < 18; ++ks) {
    if (ks + DEPTH < 18) {
      f0[(ks + DEPTH) % (DEPTH + 1)] = rd(ks + DEPTH, 0);
      f1[(ks + DEPTH) % (DEPTH + 1)] = rd(ks + DEPTH, 1);
    }
    __builtin_amdgcn_sched_barrier(0);
    body(ks, f0[ks % (DEPTH + 1)], f1[ks % (DEPTH + 1)]);
    __builtin_amdgcn_sched_barrier(0);
    side(ks);
  }
}
template <int NST, class FC, class FS>
DEV void stream(const u16* x, char* smem, int s0, int st, int wave, int lane, int tid, FC compute, FS signal) {
  const int n = (NSEG - s0 + st - 1) / st;
  {
    XDma d0 = x_dma_setup(x, smem, s0, 0, wave, lane, true), d1 = x_dma_setup(x, smem, min(s0 + st, NSEG - 1), 1, wave, lane, n > 1);
#pragma unroll
    for (int i = 0; i < 9; ++i) x_dma_piece(d0, wave, i);
#pragma unroll
    for (int i = 0; i < 9; ++i) x_dma_piece(d1, wave, i);
  }
  int buf = 0;
  for (int i = 0; i < n; ++i) {
    const int s = s0 + i * st;
    constexpr int EARLY = 4;
    if (i == 0 && n > 1) wait_vm<9>(); else if (i <= EARLY || i + 1 >= n) wait_vm<0>(); else wait_vm<9 + NST>();
    RAW_BARRIER();
    if (tid == 0) {
      if (i >= 1 && i <= EARLY) signal(s - st);
      else if (i >= EARLY + 2) signal(s - 2 * st);
      if (i > EARLY && i + 1 >= n) signal(s - st);
    }
    XDma d = x_dma_setup(x, smem, min(s + 2 * st, NSEG - 1), buf == 0 ? 2 : buf - 1, wave, lane, i + 2 < n);
    compute(s, smem + L_X + buf * XBUF, d);
    buf = buf == 2 ? 0 : buf + 1;
  }
  wait_vm<0>();
  RAW_BARRIER();
  if (tid == 0) signal(s0 + (n - 1) * st);
}
template <int NTW, class F>
DEV void load_kfrags(char* smem, int wave, int lane, int tid, F srcf, v4u (&w)[NTW][18]) {
  constexpr int RS = NTW * 128;
  const int g = lane >> 4, q4 = (lane & 15) >> 2, p4 = lane & 3;
  staged_load<RS>(smem, wave, lane, srcf, [&](int t, int base) {
#pragma unroll
    for (int tl = 0; tl < NTW; ++tl)
#pragma unroll
      for (int kk = 0; kk < 2; ++kk) {
        int a0 = base + (32 * kk + 8 * g + q4) * RS + ((wave * NTW + tl) * 16 + 4 * p4) * 2;
        v2u x0 = tr_read(smem, a0), x1 = tr_read(smem, a0 + 4 * RS);
        w[tl][2 * t + kk] = (v4u){x0.x, x0.y, x1.x, x1.y};
      }
  });
}

DEV void kq_worker(const Params& p, char* smem, int h, int wave, int lane, int tid) {
  v4u w[4][18];
  load_kfrags<4>(smem, wave, lane, tid, [&](int row, int cc) {
    return (cc < 16 ? p.Wk : p.Wq) + (size_t)row * 4096 + h * 128 + (cc & 15) * 8; }, w);
  DSTAMP(h == 0 && wave == 0, 40);
  __amdgpu_buffer_rsrc_t ro = mkrsrc(p.span, SPAN_BYTES);
  wg_wait_ge(p.ctrl + C_POL, 0x1000u, p.ctrl + C_TMO, 9u, wave);
  const unsigned pr = par_r(read_pol(p));
  DSTAMP(h == 0 && wave == 0, 41);
  stream<4>(p.x, smem, 0, 1, wave, lane, tid,
    [&](int s, const char* xb, const XDma& dm) {
      DSTAMP(h == 0 && wave == 0 && s < 4, 42 + s);
      f32x4 acc[2][4];
#pragma unroll
      for (int a = 0; a < 2; ++a)
#pragma unroll
        for (int b = 0; b < 4; ++b) acc[a][b] = (f32x4){0, 0, 0, 0};
      xloop<2>(xb, lane, [&](int ks, v4u b0, v4u b1) {
#pragma unroll
        for (int tl = 0; tl < 4; ++tl) { acc[0][tl] = mfma16(w[tl][ks], b0, acc[0][tl]); acc[1][tl] = mfma16(w[tl][ks], b1, acc[1][tl]); }
      }, [&](int ks) { if (!(ks & 1)) x_dma_piece(dm, wave, ks >> 1); });
#pragma unroll
      for (int tt = 0; tt < 2; ++tt)
#pragma unroll
        for (int uu = 0; uu < 2; ++uu) {
          f32x4 a = acc[tt][2 * uu], b = acc[tt][2 * uu + 1];
          v4u o = {cvtpk(a[0], a[1]), cvtpk(a[2], a[3]), cvtpk(b[0], b[1]), cvtpk(b[2], b[3])};
          st_sc1(o, ro, sp_off((wave < 2 ? LR_KX : LR_QX) + (s * 32 + h) * 8192, pr) + (tt * 4 + (wave & 1) * 2 + uu) * 1024 + lane * 16);
        }
      DSTAMP(h == 0 && wave == 1, 256 + s); DVAL(h == 0 && wave == 1, 320 + s, (unsigned)clk());
    },
    [&](int s) { astore(p.ctrl + C_KQ + s * 32 + h, 1u); });
}

DEV void vu_worker(const Params& p, char* smem, int h, int par, int wave, int lane, int tid) {
  const int g = lane >> 4;
  v4u wo[9][4];
  ABlk b0, b1;
  wv_block_load(p, h, 6 + par, tid, b0); wv_block_load(p, h, 8, tid, b1);
  load_wo(p, smem, h, par, wave, lane, tid, wo);
  v4u wv[2][18];
  load_kfrags<2>(smem, wave, lane, tid, [&](int row, int cc) {
    return p.Wv + (size_t)row * 4096 + h * 128 + cc * 8; }, wv);
  DSTAMP(h == 0 && par == 0 && wave == 0, 8);
  wg_wait_ge(p.ctrl + C_POL, 0x1000u, p.ctrl + C_TMO, 9u, wave);
  const unsigned pr = par_r(read_pol(p));
  wvo_unit<0, 9>(p, smem, h, 6 + par, 0, wave, lane, tid, b0, wo, pr);
  if (par == 0) wvo_unit<0, 5>(p, smem, h, 8, 1, wave, lane, tid, b1, wo, pr);
  else wvo_unit<5, 9>(p, smem, h, 8, 1, wave, lane, tid, b1, wo, pr);
  wg_signal(p.ctrl + C_WVO, tid);
  DSTAMP(h == 0 && par == 0 && wave == 0, 9);
  __amdgpu_buffer_rsrc_t ro = mkrsrc(p.span, SPAN_BYTES);
  stream<9>(p.x, smem, par, 2, wave, lane, tid,
    [&](int s, const char* xb, const XDma& dm) {
      f32x4 acc[2][2];
#pragma unroll
      for (int a = 0; a < 2; ++a)
#pragma unroll
        for (int b = 0; b < 2; ++b) acc[a][b] = (f32x4){0, 0, 0, 0};
      xloop<3>(xb, lane, [&](int ks, v4u b0, v4u b1) {
#pragma unroll
        for (int tl = 0; tl < 2; ++tl) { acc[0][tl] = mfma16(wv[tl][ks], b0, acc[0][tl]); acc[1][tl] = mfma16(wv[tl][ks], b1, acc[1][tl]); }
      }, [&](int ks) { if (!(ks & 1)) x_dma_piece(dm, wave, ks >> 1); });
#pragma unroll
      for (int tt = 0; tt < 2; ++tt) {
        f32x4 a = acc[tt][0], b = acc[tt][1];
        *(v4u*)(smem + L_V + (tt * 4 + wave) * 1024 + lane * 16) = (v4u){cvtpk(a[0], a[1]), cvtpk(a[2], a[3]), cvtpk(b[0], b[1]), cvtpk(b[2], b[3])};
      }
      RAW_BARRIER();
      v4u a[2][4];
#pragma unroll
      for (int tt = 0; tt < 2; ++tt)
#pragma unroll
        for (int u = 0; u < 4; ++u) a[tt][u] = *(const v4u*)(smem + L_V + (tt * 4 + u) * 1024 + lane * 16);
#pragma unroll
      for (int tl = 0; tl < 9; ++tl) {
        f32x4 d0 = {0, 0, 0, 0}, d1 = {0, 0, 0, 0};
#pragma unroll
        for (int u = 0; u < 4; ++u) { d0 = mfma16(a[0][u], wo[tl][u], d0); d1 = mfma16(a[1][u], wo[tl][u], d1); }
        const int n = (wave * 9 + tl) * 16 + (lane & 15), c = n >> 3;
        v4u o = {cvtpk(d0[0], d0[1]), cvtpk(d0[2], d0[3]), cvtpk(d1[0], d1[1]), cvtpk(d1[2], d1[3])};
        st_sc1(o, ro, sp_off(LW_UX + ((s * 72 + c) * 16 + (h >> 1)) * 1024 + (g * 16 + (h & 1) * 8 + (n & 7)) * 16, pr ^ 1u));
      }
      DSTAMP(h == 0 && wave == 1, 192 + s); DVAL(h == 0 && wave == 1, 384 + s, (unsigned)clk());
    },
    [&](int s) { aadd(p.ctrl + C_UX + s, 1u); });
}

DEV void b1_worker(const Params& p, char* smem, int gi, int wave, int lane, int tid) {
  {
    const unsigned lb = lds_addr(smem) + wave * 1024;
#pragma unroll
    for (int i = 0; i < 9; ++i) dma16(p.x + (size_t)(gi >> 2) * 32 * 576 + (i * 256 + tid) * 8, lb + 65536 + (i & 7) * 4096);
    const u16* src = (((tid & 31) < 16) ? p.Wk : p.Wq) + (size_t)(256 + (tid >> 5)) * 4096 + gi * 128 + (tid & 15) * 8;
#pragma unroll 4
    for (int i = 0; i < 40; ++i) dma16(src + (size_t)i * 8 * 4096, lb + (i & 7) * 4096);
    drain();
  }
  if (gi < 28) {
    const int slab = gi >> 2, sub = gi & 3;
    if (wave == 0) wait_slow(p.ctrl + C_KQ + gi, 1u, p.ctrl + C_TMO, 23u);
    __syncthreads();
    DSTAMP(gi == 0 && wave == 0, 30);
    v4u w[4][18];
    load_kfrags<4>(smem, wave, lane, tid, [&](int row, int cc) {
      int col = slab * 256 + cc * 8; if (col > 1720) col = 1720;
      return p.wqkv + (size_t)row * 1728 + col; }, w);
    DSTAMP(gi == 0 && wave == 0, 31);
    __amdgpu_buffer_rsrc_t rqk = mkrsrc(p.data + OFF_QKB, (unsigned)(2048 * 1152 * 2));
    __amdgpu_buffer_rsrc_t rvt = mkrsrc(p.data + OFF_VTB, (unsigned)(576 * 2048 * 2));
    const int g = lane >> 4;
    const int col0 = slab * 256 + wave * 64;
    const bool isv = col0 >= 1152;
    stream<4>(p.x, smem, sub, 4, wave, lane, tid,
      [&](int s, const char* xb, const XDma& dm) {
        f32x4 acc[2][4];
#pragma unroll
        for (int a = 0; a < 2; ++a)
#pragma unroll
          for (int b = 0; b < 4; ++b) acc[a][b] = (f32x4){0, 0, 0, 0};
        if (!isv) {
          xloop<2>(xb, lane, [&](int ks, v4u b0, v4u b1) {
#pragma unroll
            for (int tl = 0; tl < 4; ++tl) { acc[0][tl] = mfma16(w[tl][ks], b0, acc[0][tl]); acc[1][tl] = mfma16(w[tl][ks], b1, acc[1][tl]); }
          }, [&](int ks) { if (!(ks & 1)) x_dma_piece(dm, wave, ks >> 1); });
#pragma unroll
          for (int tt = 0; tt < 2; ++tt)
#pragma unroll
            for (int tp = 0; tp < 2; ++tp) {
              f32x4 a = acc[tt][2 * tp], b = acc[tt][2 * tp + 1];
              auto r0 = __builtin_amdgcn_permlane16_swap(cvtpk(a[0], a[1]), cvtpk(b[0], b[1]), false, false);
              auto r1 = __builtin_amdgcn_permlane16_swap(cvtpk(a[2], a[3]), cvtpk(b[2], b[3]), false, false);
              st_sc1((v4u){r0[0], r1[0], r0[1], r1[1]}, rqk,
                     ((s * 32 + tt * 16 + (lane & 15)) * 1152 + col0 + 16 * (2 * tp + (g & 1)) + 8 * (g >> 1)) * 2);
            }
        } else {
          xloop<2>(xb, lane, [&](int ks, v4u b0, v4u b1) {
#pragma unroll
            for (int tl = 0; tl < 4; ++tl) { acc[0][tl] = mfma16(b0, w[tl][ks], acc[0][tl]); acc[1][tl] = mfma16(b1, w[tl][ks], acc[1][tl]); }
          }, [&](int ks) { if (!(ks & 1)) x_dma_piece(dm, wave, ks >> 1); });
#pragma unroll
          for (int tl = 0; tl < 4; ++tl) {
            f32x4 a = acc[0][tl], b = acc[1][tl];
            st_sc1((v4u){cvtpk(a[0], a[1]), cvtpk(a[2], a[3]), cvtpk(b[0], b[1]), cvtpk(b[2], b[3])}, rvt,
                   (((col0 - 1152 + tl * 16 + (lane & 15)) * 64 + s) * 4 + g) * 16);
          }
        }
      },
      [&](int) {});
    DSTAMP(gi == 0 && wave == 0, 32);
  }
  wg_signal(p.ctrl + C_B1, tid);
}

DEV void b2_item(const Params& p, char* smem, int hg, int qb, int wave, int lane, int tid) {
  __amdgpu_buffer_rsrc_t rqk = mkrsrc(p.data + OFF_QKB, (unsigned)(2048 * 1152 * 2));
  __amdgpu_buffer_rsrc_t rvt = mkrsrc(p.data + OFF_VTB, (unsigned)(576 * 2048 * 2));
  __amdgpu_buffer_rsrc_t rob = mkrsrc(p.data + OFF_OB, (unsigned)SZ_OB);
  const int g = lane >> 4, li = lane & 15;
  const int hb = hg * 4 + wave;
  const int q0 = qb * 128, k0 = q0 - 256;
  const float SC = 0.23570226039551584f * 1.4426950408889634f;
  const float NEG = -1e30f;
  const unsigned OOB = 0x7ffffff0u;
  auto rowfrag = [&](int tok, int colbase) {
    unsigned off = (tok >= 0 && g < 3) ? (unsigned)((tok * 1152 + colbase + 8 * g) * 2) : OOB;
    v4u v = ld_sc1(rqk, off);
    if (g == 2) { v.y = 0u; v.z = 0u; v.w = 0u; }
    return v;
  };
  v4u kf[24], vf[12][2], qf[8];
#pragma unroll
  for (int qt = 0; qt < 8; ++qt) qf[qt] = rowfrag(q0 + 16 * qt + li, 18 * hb);
#pragma unroll
  for (int t = 0; t < 24; ++t) kf[t] = rowfrag(k0 + 16 * t + li, 576 + 18 * hb);
#pragma unroll
  for (int b = 0; b < 12; ++b)
#pragma unroll
    for (int dt = 0; dt < 2; ++dt) {
      const int blk = 4 * qb - 8 + b, d = 16 * dt + li;
      v4u v = ld_sc1(rvt, (blk >= 0 && d < 18) ? (unsigned)((((18 * hb + d) * 64 + blk) * 4 + g) * 16) : OOB);
      if (dt == 1 && li == 2) v = (v4u){0x3F803F80u, 0x3F803F80u, 0x3F803F80u, 0x3F803F80u};
      vf[b][dt] = v;
    }
#pragma unroll
  for (int qt = 0; qt < 8; ++qt) {
    f32x4 sc[17];
#pragma unroll
    for (int tau = 0; tau < 17; ++tau) {
      const float c = (k0 + 16 * (qt + tau)) >= 0 ? 0.f : NEG;
      f32x4 ci = {c, c, c, c};
      if (tau == 0) {
#pragma unroll
        for (int r = 0; r < 4; ++r) ci[r] = (4 * g + r > li) ? c : NEG;
      }
      if (tau == 16) {
#pragma unroll
        for (int r = 0; r < 4; ++r) ci[r] = (4 * g + r <= li) ? c : NEG;
      }
      sc[tau] = mfma16(kf[qt + tau], qf[qt], ci);
    }
    float m = fmaxf(fmaxf(sc[0][0], sc[0][1]), fmaxf(sc[0][2], sc[0][3]));
#pragma unroll
    for (int tau = 1; tau < 17; ++tau) m = fmaxf(fmaxf(fmaxf(m, sc[tau][0]), sc[tau][1]), fmaxf(sc[tau][2], sc[tau][3]));
    m = xmax(m);
    const float ms = -m * SC;
    unsigned pk[18][2];
#pragma unroll
    for (int t = 0; t < 17; ++t) {
      float e0 = __builtin_amdgcn_exp2f(fmaf(sc[t][0], SC, ms)), e1 = __builtin_amdgcn_exp2f(fmaf(sc[t][1], SC, ms));
      float e2 = __builtin_amdgcn_exp2f(fmaf(sc[t][2], SC, ms)), e3 = __builtin_amdgcn_exp2f(fmaf(sc[t][3], SC, ms));
      pk[t + (qt & 1)][0] = cvtpk(e0, e1); pk[t + (qt & 1)][1] = cvtpk(e2, e3);
    }
    pk[(qt & 1) ? 0 : 17][0] = 0u; pk[(qt & 1) ? 0 : 17][1] = 0u;
    f32x4 o0 = {0, 0, 0, 0}, o1 = {0, 0, 0, 0};
#pragma unroll
    for (int b = 0; b < 9; ++b) {
      v4u pf = {pk[2 * b][0], pk[2 * b][1], pk[2 * b + 1][0], pk[2 * b + 1][1]};
      o0 = mfma16(vf[(qt >> 1) + b][0], pf, o0);
      o1 = mfma16(vf[(qt >> 1) + b][1], pf, o1);
    }
    const float inv = __builtin_amdgcn_rcpf(__shfl(o1[2], li));
    char* orow = smem + (16 * qt + li) * 144 + wave * 36;
    *(unsigned*)(orow + 8 * g) = cvtpk(o0[0] * inv, o0[1] * inv);
    *(unsigned*)(orow + 8 * g + 4) = cvtpk(o0[2] * inv, o0[3] * inv);
    if (g == 0) *(unsigned*)(orow + 32) = cvtpk(o1[0] * inv, o1[1] * inv);
  }
  __syncthreads();
#pragma unroll
  for (int i = 0; i < 5; ++i) {
    int ch = tid + 256 * i;
    if (ch < 1152) { int row = ch / 9, cc = ch % 9; st_sc1(*(const v4u*)(smem + ch * 16), rob, ((q0 + row) * 576 + hg * 72) * 2 + cc * 16); }
  }
  wg_signal(p.ctrl + C_OB + qb * 32, tid);
}

DEV void fa_item(const Params& p, char* smem, int s, int j, int widx, int wave, int lane, int tid) {
  __amdgpu_buffer_rsrc_t rs = mkrsrc(p.span, SPAN_BYTES);
  __amdgpu_buffer_rsrc_t rab = mkrsrc(p.data + OFF_AB, (unsigned)SZ_OB);
  __amdgpu_buffer_rsrc_t rg = mkrsrc(p.data + OFF_GATE, SZ_GATE);
  const int g = lane >> 4, li = lane & 15;
  const int nt = j * 4 + wave;
  if (wave == 0) {
    wait_ge(p.ctrl + C_UX + s, 32u, p.ctrl + C_TMO, 19u);
    wait_ge(p.ctrl + C_FB + (s >> 2), 9u, p.ctrl + C_TMO, 20u);
  }
  __syncthreads();
  const unsigned pw = par_r(read_pol(p)) ^ 1u;
  v4u ux[2][16];
#pragma unroll
  for (int sl = 0; sl < 2; ++sl)
#pragma unroll
    for (int pp = 0; pp < 16; ++pp) ux[sl][pp] = ld_sc1(rs, sp_off(LW_UX + ((s * 72 + nt * 2 + sl) * 16 + (pp & 8)) * 1024, pw) + (pp & 7) * 1024 + lane * 16);
  v2u bp[2] = {{0u, 0u}, {0u, 0u}};
  float gt[2] = {1.f, 1.f};
  {
    v4u b4 = ld_sc1(rab, ((s * 36 + nt) * 64 + lane) * 16);
    bp[0] = (v2u){b4.x, b4.y}; bp[1] = (v2u){b4.z, b4.w};
#pragma unroll
    for (int qt = 0; qt < 2; ++qt) gt[qt] = __uint_as_float(ld1_sc1(rg, (s * 32 + qt * 16 + li) * 4));
  }
  if (wave == 1) {
    wait_slow(p.ctrl + C_PROG + (widx & 15) * 32, umin((unsigned)s + 1u, 60u), p.ctrl + C_TMO, 17u);
    if (s >= 60) {
      wait_flags(p.ctrl + C_PX, 32, 32, (unsigned)s + 1u, lane, p.ctrl + C_TMO, 11u);
      wait_flags(p.ctrl + C_UM, NC, 32, (unsigned)s + 1u, lane, p.ctrl + C_TMO, 12u);
    }
  }
  __syncthreads();
  v4u um[2][4];
  {
    v4u v[20];
#pragma unroll
    for (int i = 0; i < 20; ++i) v[i] = ld_sc1(rs, sp_off(LW_PX + s * 81920 + (i >> 1) * 8192, pw) + ((i & 1) * 256 + tid) * 16);
#pragma unroll
    for (int sl = 0; sl < 2; ++sl)
#pragma unroll
      for (int mm = 0; mm < 4; ++mm) um[sl][mm] = ld_sc1(rs, sp_off(LW_UM + (s * 72 + nt * 2 + sl) * 4096, pw) + mm * 1024 + lane * 16);
#pragma unroll
    for (int i = 0; i < 20; ++i) *(v4u*)(smem + (i * 256 + tid) * 16) = v[i];
  }
  __syncthreads();
#pragma unroll
  for (int qt = 0; qt < 2; ++qt) {
    f32x4 aA[2] = {{0, 0, 0, 0}, {0, 0, 0, 0}}, aB[2] = {{0, 0, 0, 0}, {0, 0, 0, 0}};
    v4u pe[3], po[3];
    auto ldp = [&](int i) {
      const char* b = (i < 16) ? smem + (qt * 32 + 2 * i) * 1024 + lane * 16 : smem + 65536 + (qt * 8 + 2 * (i - 16)) * 1024 + lane * 16;
      pe[i % 3] = *(const v4u*)b; po[i % 3] = *(const v4u*)(b + 1024);
    };
    ldp(0); ldp(1);
#pragma unroll
    for (int i = 0; i < 20; ++i) {
      if (i + 2 < 20) ldp(i + 2);
      __builtin_amdgcn_sched_barrier(0);
#pragma unroll
      for (int sl = 0; sl < 2; ++sl) {
        v4u u = (i < 16) ? ux[sl][i & 15] : um[sl][i & 3];
        aA[sl] = mfma16(u, pe[i % 3], aA[sl]); aB[sl] = mfma16(u, po[i % 3], aB[sl]);
      }
      __builtin_amdgcn_sched_barrier(0);
    }
    const int tok = s * 32 + qt * 16 + li;
    float o[4];
#pragma unroll
    for (int r = 0; r < 4; ++r) {
      auto sw = __builtin_amdgcn_permlane32_swap(__float_as_uint(aB[0][r]), __float_as_uint(aA[1][r]), false, false);
      float own = (lane < 32) ? aA[0][r] : aB[1][r];
      float oth = (lane < 32) ? __uint_as_float(sw[1]) : __uint_as_float(sw[0]);
      unsigned bw = (r < 2) ? bp[qt].x : bp[qt].y;
      o[r] = gt[qt] * (own + oth) + bf2f((r & 1) ? (bw >> 16) : (bw & 0xffffu));
    }
    v2u ov = {cvtpk(o[0], o[1]), cvtpk(o[2], o[3])};
    *(v2u*)(p.out + (size_t)tok * 576 + nt * 16 + 4 * g) = ov;
  }
  __syncthreads();
}

constexpr int SFINE = 61;
DEV void fa_fine(const Params& p, char* smem, int s, int nt, int widx, int wave, int lane, int tid) {
  __amdgpu_buffer_rsrc_t rs = mkrsrc(p.span, SPAN_BYTES);
  __amdgpu_buffer_rsrc_t rab = mkrsrc(p.data + OFF_AB, (unsigned)SZ_OB);
  __amdgpu_buffer_rsrc_t rg = mkrsrc(p.data + OFF_GATE, SZ_GATE);
  const int g = lane >> 4, li = lane & 15;
  if (wave == 0) {
    wait_ge(p.ctrl + C_UX + s, 32u, p.ctrl + C_TMO, 19u);
    wait_ge(p.ctrl + C_FB + (s >> 2), 9u, p.ctrl + C_TMO, 20u);
  }
  __syncthreads();
  const unsigned pw = par_r(read_pol(p)) ^ 1u;
  v4u ux[2][4];
#pragma unroll
  for (int sl = 0; sl < 2; ++sl)
#pragma unroll
    for (int q = 0; q < 4; ++q) ux[sl][q] = ld_sc1(rs, sp_off(LW_UX + ((s * 72 + nt * 2 + sl) * 16 + wave * 4) * 1024, pw) + q * 1024 + lane * 16);
  const v4u b4 = ld_sc1(rab, ((s * 36 + nt) * 64 + lane) * 16);
  const float gt = __uint_as_float(ld1_sc1(rg, (s * 32 + (wave & 1) * 16 + li) * 4));
  if (wave == 1) {
    wait_slow(p.ctrl + C_PROG + (widx & 15) * 32, 60u, p.ctrl + C_TMO, 17u);
    wait_flags(p.ctrl + C_UM, NC, 32, (unsigned)s + 1u, lane, p.ctrl + C_TMO, 12u);
    wait_flags(p.ctrl + C_PX, 32, 32, (unsigned)s + 1u, lane, p.ctrl + C_TMO, 11u);
  }
  __syncthreads();
  v4u px[2][10], um[2];
#pragma unroll
  for (int qt = 0; qt < 2; ++qt) {
#pragma unroll
    for (int i = 0; i < 8; ++i) px[qt][i] = ld_sc1(rs, sp_off(LW_PX + s * 81920 + (qt * 32 + wave * 8) * 1024, pw) + i * 1024 + lane * 16);
#pragma unroll
    for (int i = 0; i < 2; ++i) px[qt][8 + i] = ld_sc1(rs, sp_off(LW_PX + s * 81920 + 65536 + (qt * 8 + wave * 2) * 1024, pw) + i * 1024 + lane * 16);
  }
#pragma unroll
  for (int sl = 0; sl < 2; ++sl) um[sl] = ld_sc1(rs, sp_off(LW_UM + (s * 72 + nt * 2 + sl) * 4096, pw) + wave * 1024 + lane * 16);
#pragma unroll
  for (int qt = 0; qt < 2; ++qt) {
    f32x4 aA[2] = {{0, 0, 0, 0}, {0, 0, 0, 0}}, aB[2] = {{0, 0, 0, 0}, {0, 0, 0, 0}};
#pragma unroll
    for (int i = 0; i < 5; ++i)
#pragma unroll
      for (int sl = 0; sl < 2; ++sl) {
        v4u u = (i < 4) ? ux[sl][i & 3] : um[sl];
        aA[sl] = mfma16(u, px[qt][2 * i], aA[sl]); aB[sl] = mfma16(u, px[qt][2 * i + 1], aB[sl]);
      }
    f32x4 a4;
#pragma unroll
    for (int r = 0; r < 4; ++r) {
      auto sw = __builtin_amdgcn_permlane32_swap(__float_as_uint(aB[0][r]), __float_as_uint(aA[1][r]), false, false);
      a4[r] = ((lane < 32) ? aA[0][r] : aB[1][r]) + ((lane < 32) ? __uint_as_float(sw[1]) : __uint_as_float(sw[0]));
    }
    *(f32x4*)(smem + (qt * 4 + wave) * 1024 + lane * 16) = a4;
  }
  __syncthreads();
  if (wave < 2) {
    const char* q = smem + wave * 4096 + lane * 16;
    f32x4 a = (*(const f32x4*)q + *(const f32x4*)(q + 1024)) + (*(const f32x4*)(q + 2048) + *(const f32x4*)(q + 3072));
    const unsigned b0 = wave ? b4.z : b4.x, b1 = wave ? b4.w : b4.y;
    v2u ov = {cvtpk(gt * a[0] + bf2f(b0 & 0xffffu), gt * a[1] + bf2f(b0 >> 16)), cvtpk(gt * a[2] + bf2f(b1 & 0xffffu), gt * a[3] + bf2f(b1 >> 16))};
    *(v2u*)(p.out + (size_t)(s * 32 + wave * 16 + li) * 576 + nt * 16 + 4 * g) = ov;
  }
}

DEV void fb_item(const Params& p, char* smem, int j, int q, int wave, int lane, int tid) {
  __amdgpu_buffer_rsrc_t rob = mkrsrc(p.data + OFF_OB, (unsigned)SZ_OB);
  __amdgpu_buffer_rsrc_t rab = mkrsrc(p.data + OFF_AB, (unsigned)SZ_OB);
  __amdgpu_buffer_rsrc_t rg = mkrsrc(p.data + OFF_GATE, SZ_GATE);
  const int g = lane >> 4, li = lane & 15;
  const int nt = j * 4 + wave;
  v4u wf[1][18];
  load_kfrags<1>(smem, wave, lane, tid, [&](int row, int cc) { return p.wo + (size_t)row * 576 + j * 64 + cc * 8; }, wf);
  v4u gv[9];
#pragma unroll
  for (int i = 0; i < 9; ++i) gv[i] = *(const v4u*)(p.gw + (i * 8 + (lane & 7)) * 8);
  wg_wait_ge(p.ctrl + C_OB + q * 32, 8u, p.ctrl + C_TMO, 10u, wave);
  v4u v[9], xv[9];
  auto issue = [&](int s) {
#pragma unroll
    for (int i = 0; i < 9; ++i) v[i] = ld_sc1(rob, (s * 32 * 576 + tile_src_off(wave, lane) + i * 64) * 2);
#pragma unroll
    for (int i = 0; i < 9; ++i) xv[i] = *(const v4u*)(p.x + (size_t)(s * 32 + wave * 8 + (lane >> 3)) * 576 + (i * 8 + (lane & 7)) * 8);
  };
  issue(4 * q);
  for (int it = 0; it < 4; ++it) {
    const int s = 4 * q + it;
    char* buf = smem + (it & 1) * 40960;
#pragma unroll
    for (int i = 0; i < 9; ++i) *(v4u*)(buf + (i * 4 + wave) * 1024 + lane * 16) = v[i];
    {
      float a = 0;
#pragma unroll
      for (int i = 0; i < 9; ++i)
#pragma unroll
        for (int e = 0; e < 4; ++e) {
          a += bf2f(xv[i][e] & 0xffffu) * bf2f(gv[i][e] & 0xffffu);
          a += bf2f(xv[i][e] >> 16) * bf2f(gv[i][e] >> 16);
        }
      a += __shfl_xor(a, 1); a += __shfl_xor(a, 2); a += __shfl_xor(a, 4);
      const float gs = 1.f / (1.f + __expf(-a));
      if ((lane & 7) == 0) {
        *(float*)(buf + 36864 + (wave * 8 + (lane >> 3)) * 4) = gs;
      }
    }
    __syncthreads();
    if (it + 1 < 4) issue(s + 1);
    f32x4 bacc[2] = {{0, 0, 0, 0}, {0, 0, 0, 0}};
    xloop<3>(buf, lane, [&](int ks, v4u b0, v4u b1) { bacc[0] = mfma16(wf[0][ks], b0, bacc[0]); bacc[1] = mfma16(wf[0][ks], b1, bacc[1]); }, [&](int) {});
    {
      const float o0 = 1.f - *(const float*)(buf + 36864 + li * 4), o1 = 1.f - *(const float*)(buf + 36864 + (16 + li) * 4);
      st_sc1((v4u){cvtpk(o0 * bacc[0][0], o0 * bacc[0][1]), cvtpk(o0 * bacc[0][2], o0 * bacc[0][3]),
                   cvtpk(o1 * bacc[1][0], o1 * bacc[1][1]), cvtpk(o1 * bacc[1][2], o1 * bacc[1][3])}, rab, ((s * 36 + nt) * 64 + lane) * 16);
      if (j == 0 && wave == 0 && lane < 8) st_sc1(*(const v4u*)(buf + 36864 + lane * 16), rg, s * 128 + lane * 16);
    }
  }
  wg_signal(p.ctrl + C_FB + q, tid);
}

DEV int take(const Params& p, char* smem, int k, int n, int tid) {
  LAS int* cmd = (LAS int*)(smem + 163824);
  __syncthreads();
  if (tid == 0) {
    int c = -1;
    if (aload(p.ctrl + C_FQ + k * 32) < (unsigned)n && !aload(p.ctrl + C_TMO)) {
      unsigned t = __hip_atomic_fetch_add(p.ctrl + C_FQ + k * 32, 1u, __ATOMIC_RELAXED, __HIP_MEMORY_SCOPE_AGENT);
      if (t < (unsigned)n) c = (int)t;
    }
    *cmd = c;
  }
  __syncthreads();
  return __builtin_amdgcn_readfirstlane(*(volatile LAS int*)cmd);
}

DEV void worker_role(const Params& p, char* smem, int widx, int wave, int lane, int tid) {
  const int slab = ((widx >> 3) << 2) | (widx & 3), par = (widx >> 2) & 1;
  if (slab < 32) vu_worker(p, smem, slab, par, wave, lane, tid);
  else if (par == 0) kq_worker(p, smem, slab - 32, wave, lane, tid);
  else b1_worker(p, smem, slab - 32, wave, lane, tid);
  DSTAMP(widx == 0 && wave == 0, 10);
  for (int c; (c = take(p, smem, 0, 128, tid)) >= 0;) {
    wg_wait_ge(p.ctrl + C_B1, 32u, p.ctrl + C_TMO, 13u, wave);
    b2_item(p, smem, c & 7, c >> 3, wave, lane, tid);
    DSTAMP(wave == 0 && (c & 31) == 31, 12 + (c >> 5));
  }
  for (int c; (c = take(p, smem, 1, 144, tid)) >= 0;) {
    fb_item(p, smem, c % 9, c / 9, wave, lane, tid);
    DSTAMP(wave == 0 && (c % 36) == 35, 33 + c / 36);
  }
  for (int c; (c = take(p, smem, 2, SFINE * 9, tid)) >= 0;) {
    fa_item(p, smem, c / 9, c % 9, widx, wave, lane, tid);
    DSTAMP(wave == 0 && (c % 9) == 0, 448 + c / 9);
  }
  for (int c; (c = take(p, smem, 3, (NSEG - SFINE) * 36, tid)) >= 0;) {
    fa_fine(p, smem, SFINE + c / 36, c % 36, widx, wave, lane, tid);
    DSTAMP(wave == 0 && (c % 36) == 0, 448 + SFINE + c / 36);
  }
}

__global__ void __launch_bounds__(256, 1) fused_kernel(Params p) {
  extern __shared__ __attribute__((aligned(16))) char smem[];
  const int tid = threadIdx.x, lane = tid & 63, wave = __builtin_amdgcn_readfirstlane(tid >> 6);
  const int b = blockIdx.x;
  const int idx = (b >> 3) * 4 + (b & 3);
  {
    unsigned long long pc;
    asm volatile("s_getpc_b64 %0" : "=s"(pc));
    const char* cb = (const char*)(pc & ~255ull);
    v4u c0 = *(const v4u*)(cb + ((b >> 3) * 320 + tid) * 16);
    v4u c1 = *(const v4u*)(cb + ((b >> 3) * 320 + 256 + (tid & 63)) * 16);
    asm volatile("" :: "v"(c0), "v"(c1));
  }
  DSTAMP(wave == 0, 1024 + b);
  if ((b & 7) < 4) {
    if (idx < NH) head_role(p, smem, idx, wave, lane, tid);
    else if (idx < NH + NC) col_role(p, smem, idx - NH, wave, lane, tid);
    else spare_role(p, smem, idx - NH - NC, wave, lane, tid);
  } else {
    worker_role(p, smem, idx, wave, lane, tid);
  }
  DSTAMP(wave == 0, 1280 + b);
}

__global__ void __launch_bounds__(512) prep_kernel(Params p) {
  __shared__ unsigned sm[16];
  const int tid = threadIdx.x, lane = tid & 63, wave = __builtin_amdgcn_readfirstlane(tid >> 6), b = blockIdx.x;
  if (b >= NPAGES) {
    ((v4u*)p.ctrl)[(b - NPAGES) * 512 + tid] = (v4u){0u, 0u, 0u, 0u};
    return;
  }
  __amdgpu_buffer_rsrc_t rm = mkrsrc(p.mbox, MBOX_BYTES);
  unsigned acc = 0, mn[2];
  const unsigned off = b * (2u << 20) + (1u << 20) + wave * 16384;
  acc += ld1_sc1(rm, off + 128);
  drain();
  asm volatile("" :: "v"(acc));
#pragma unroll
  for (int q = 0; q < 2; ++q) {
    const int hsel = (q ^ wave) & 1;
    unsigned long long t0 = clk();
    acc += ld1_sc1(rm, off + hsel * 8192 + 512);
    drain();
    asm volatile("" :: "v"(acc));
    mn[q] = (unsigned)(clk() - t0);
  }
  if (lane == 0) { sm[wave * 2 + (wave & 1)] = mn[0]; sm[wave * 2 + ((wave & 1) ^ 1)] = mn[1]; }
  __syncthreads();
  if (tid == 0) {
    unsigned a0 = ~0u, a1 = ~0u;
    for (int i = 0; i < 8; ++i) { a0 = umin(a0, sm[2 * i]); a1 = umin(a1, sm[2 * i + 1]); }
    const unsigned grp = (__builtin_amdgcn_s_getreg(20 | (3 << 11)) >> 2) & 1u;
    p.ctrl[C_CAL + b] = (a1 < a0 ? 1u : 0u) | (grp << 1);
  }
}

}

extern "C" void kernel_launch(void* const* d_in, const int* in_sizes, int n_in, void* d_out, int out_size, void* d_ws,
                              size_t ws_size, hipStream_t stream) {
  (void)in_sizes; (void)n_in; (void)out_size; (void)ws_size;
  Params p;
  p.x = (const u16*)d_in[0]; p.mem0 = (const u16*)d_in[1];
  p.Wq = (const u16*)d_in[2]; p.Wk = (const u16*)d_in[3]; p.Wv = (const u16*)d_in[4]; p.Wo = (const u16*)d_in[5];
  p.wqkv = (const u16*)d_in[8]; p.wo = (const u16*)d_in[9]; p.gw = (const u16*)d_in[10];
  p.out = (u16*)d_out;
  char* ws = (char*)d_ws;
  p.ctrl = (unsigned*)ws;
  size_t a = ((size_t)ws + CTRL_BYTES + (2u << 20) - 1) & ~(size_t)((2u << 20) - 1);
  p.mbox = (char*)a;
  p.span = p.mbox + MBOX_BYTES;
  p.data = p.span + SPAN_BYTES;
  hipFuncSetAttribute((const void*)fused_kernel, hipFuncAttributeMaxDynamicSharedMemorySize, LDS_BYTES);
  prep_kernel<<<NPAGES + 12, 512, 0, stream>>>(p);
  fused_kernel<<<256, 256, LDS_BYTES, stream>>>(p);
}
```

```cpp
#include <hip/hip_runtime.h>
#include <hip/hip_bf16.h>

typedef unsigned short u16;
typedef unsigned v4u __attribute__((ext_vector_type(4)));
typedef unsigned v2u __attribute__((ext_vector_type(2)));
typedef unsigned v8u __attribute__((ext_vector_type(8)));
typedef __attribute__((ext_vector_type(8))) __bf16 bf16x8;
typedef __attribute__((ext_vector_type(16))) __bf16 bf16x16;
typedef __attribute__((ext_vector_type(4))) float f32x4;
typedef __attribute__((ext_vector_type(4))) short s16x4;
#define LAS __attribute__((address_space(3)))
#define DEV __device__ __forceinline__

namespace {

constexpr int NSEG = 64;
constexpr int NH = 32, NC = 72, NSPARE = 24, NWORK = 128;
constexpr unsigned SENT = 0xFFFFFFFFu;
constexpr unsigned SPIN_CAP = 1u << 22;
constexpr int NREP = 4;
constexpr int STEP_SPAN = NREP * 5 * 16384;
constexpr int NPAGES = 11;
constexpr unsigned MBOX_BYTES = NPAGES * (2u << 20);

constexpr size_t CTRL_BYTES = 98304 + 4096;
constexpr unsigned SZ_WVO = 72u * 294912u, SZ_KX = 64u * 32u * 8192u, SZ_UX = 64u * 72u * 16384u, SZ_PX = 64u * 81920u, SZ_UM = 64u * 72u * 4096u;
constexpr unsigned LW_UX = 0, LW_UM = SZ_UX, LW_PX = SZ_UX + SZ_UM;
constexpr unsigned LR_KX = 0, LR_QX = SZ_KX, LR_WVO = 2 * SZ_KX;
constexpr unsigned SPAN_BYTES = 200u << 20;
constexpr size_t OFF_QKB = 0;
constexpr size_t SZ_QKB = (size_t)2048 * 1152 * 2 + 4096;
constexpr size_t OFF_VTB = OFF_QKB + SZ_QKB;
constexpr size_t SZ_VTB = (size_t)608 * 2048 * 2;
constexpr size_t OFF_OB = OFF_VTB + SZ_VTB;
constexpr size_t SZ_OB = (size_t)2048 * 576 * 2;
constexpr size_t OFF_AB = OFF_OB + SZ_OB;
constexpr size_t OFF_A2 = OFF_AB + SZ_OB;
constexpr size_t OFF_GATE = OFF_A2 + SZ_OB;
constexpr size_t SZ_GATE = 9 * 8192;
constexpr size_t DATA_BYTES = OFF_GATE + SZ_GATE;

constexpr int C_TMO = 0;
constexpr int C_POL = 64;
constexpr int C_CLR = 128;
constexpr int C_WVO = 192;
constexpr int C_AUX = 256;
constexpr int C_B1 = 320;
constexpr int C_FQ = 384;
constexpr int C_FA = 1152;
constexpr int C_FB = 1280;
constexpr int C_PROG = 512;
constexpr int C_UX = 1024;
constexpr int C_OB = 2048;
constexpr int C_KQ = 4096;
constexpr int C_PX = 8192;
constexpr int C_ST = 10240;
constexpr int C_DBG = 12288;
constexpr int C_UM = 16384;
constexpr int C_CAL = 24576;

struct Params {
  const u16 *x, *mem0, *Wq, *Wk, *Wv, *Wo, *wqkv, *wo, *gw;
  u16* out;
  unsigned* ctrl;
  char* mbox;
  char* span;
  char* data;
};

template <int N> struct IC { static constexpr int value = N; };
DEV unsigned umin(unsigned a, unsigned b) { return a < b ? a : b; }
DEV unsigned umax(unsigned a, unsigned b) { return a > b ? a : b; }
DEV float bf2f(unsigned v) { return __builtin_bit_cast(float, v << 16); }
typedef __attribute__((ext_vector_type(2))) float f32x2;
typedef __attribute__((ext_vector_type(2))) __bf16 bf16x2;
DEV unsigned cvtpk(float lo, float hi) { return __builtin_bit_cast(unsigned, __builtin_convertvector((f32x2){lo, hi}, bf16x2)); }
DEV unsigned long long clk() { unsigned long long t; asm volatile("s_memtime %0\n\ts_waitcnt lgkmcnt(0)" : "=s"(t) :: "memory"); return t; }
DEV unsigned rt() { unsigned long long t; asm volatile("s_memrealtime %0\n\ts_waitcnt lgkmcnt(0)" : "=s"(t) :: "memory"); return (unsigned)t; }
DEV unsigned vo_mem(int rep, unsigned o) { return (unsigned)(rep * 5 + (o >> 13)) * 16384u + (o & 8191u); }
DEV unsigned vo_p(int rep, unsigned o) { return (unsigned)(rep * 5 + 2 + (o >> 13)) * 16384u + (o & 8191u); }
DEV unsigned sbase(int s, unsigned pol) { int pg = s / 6; return (unsigned)pg * (2u << 20) + (unsigned)(s - 6 * pg) * STEP_SPAN + (((pol >> pg) & 1u) << 13); }
DEV unsigned sp_off(unsigned o, unsigned par) { return ((o >> 13) << 14) | ((((o >> 20) ^ par) & 1u) << 13) | (o & 8191u); }
DEV unsigned par_r(unsigned pol) { return (pol ^ (unsigned)NPAGES) & 1u; }
DEV __amdgpu_buffer_rsrc_t mkrsrc(const void* p, unsigned bytes) { return __builtin_amdgcn_make_buffer_rsrc((void*)p, 0, (int)bytes, 0x00020000); }
DEV v4u ld_sc1(__amdgpu_buffer_rsrc_t r, unsigned off) { return __builtin_amdgcn_raw_buffer_load_b128(r, off, 0, 16); }
DEV v2u ld2_sc1(__amdgpu_buffer_rsrc_t r, unsigned off) { return __builtin_amdgcn_raw_buffer_load_b64(r, off, 0, 16); }
DEV unsigned ld1_sc1(__amdgpu_buffer_rsrc_t r, unsigned off) { return __builtin_amdgcn_raw_buffer_load_b32(r, off, 0, 16); }
DEV void st_sc1(v4u v, __amdgpu_buffer_rsrc_t r, unsigned off) { __builtin_amdgcn_raw_buffer_store_b128(v, r, off, 0, 16); }
DEV void st2_sc1(v2u v, __amdgpu_buffer_rsrc_t r, unsigned off) { __builtin_amdgcn_raw_buffer_store_b64(v, r, off, 0, 16); }
DEV void st1_sc1(unsigned v, __amdgpu_buffer_rsrc_t r, unsigned off) { __builtin_amdgcn_raw_buffer_store_b32(v, r, off, 0, 16); }
DEV v4u ld_mb(__amdgpu_buffer_rsrc_t r, unsigned vo, unsigned so) { return __builtin_amdgcn_raw_buffer_load_b128(r, vo, so, 16); }
DEV v2u ld2_mb(__amdgpu_buffer_rsrc_t r, unsigned vo, unsigned so) { return __builtin_amdgcn_raw_buffer_load_b64(r, vo, so, 16); }
DEV void st_mb(v4u v, __amdgpu_buffer_rsrc_t r, unsigned vo, unsigned so) { __builtin_amdgcn_raw_buffer_store_b128(v, r, vo, so, 16); }
DEV void drain() { asm volatile("s_waitcnt vmcnt(0)" ::: "memory"); }
DEV unsigned max4(v4u v) { return umax(umax(v.x, v.y), umax(v.z, v.w)); }
DEV v2u tr_read(const char* smem, unsigned off) {
  s16x4 v = __builtin_amdgcn_ds_read_tr16_b64_v4i16((LAS s16x4*)(smem + off));
  return __builtin_bit_cast(v2u, v);
}
DEV v4u trfrag(const char* smem, int off) {
  v2u x0 = tr_read(smem, off), x1 = tr_read(smem, off + 512);
  return (v4u){x0.x, x0.y, x1.x, x1.y};
}
DEV f32x4 mfma16(v4u a, v4u b, f32x4 c) {
  return __builtin_amdgcn_mfma_f32_16x16x32_bf16(__builtin_bit_cast(bf16x8, a), __builtin_bit_cast(bf16x8, b), c, 0, 0, 0);
}
DEV f32x4 smfma(v4u a, v8u b, f32x4 c, int idx) {
  return __builtin_amdgcn_smfmac_f32_16x16x64_bf16(__builtin_bit_cast(bf16x8, a), __builtin_bit_cast(bf16x16, b), c, idx, 0, 0);
}
template <int CTRL> DEV float dpp(float x) {
  return __builtin_bit_cast(float, __builtin_amdgcn_mov_dpp(__builtin_bit_cast(int, x), CTRL, 0xf, 0xf, true));
}
constexpr int ROR8 = 0x128;
DEV float amax(float a, float b) { float r; asm("v_max_f32 %0, %1, %2\n\ts_nop 1" : "=v"(r) : "v"(a), "v"(b)); return r; }
DEV float amax3(float a, float b, float c) { return fmaxf(fmaxf(a, b), c); }
DEV float xmax(float x) {
  auto s = __builtin_amdgcn_permlane16_swap(__float_as_uint(x), __float_as_uint(x), false, false);
  x = amax(__uint_as_float(s[0]), __uint_as_float(s[1]));
  auto t = __builtin_amdgcn_permlane32_swap(__float_as_uint(x), __float_as_uint(x), false, false);
  return amax(__uint_as_float(t[0]), __uint_as_float(t[1]));
}
DEV float xmax8(float x) {
  float t;
  asm("s_nop 1\n\tv_max_f32_dpp %0, %0, %0 row_ror:8 row_mask:0xf bank_mask:0xf\n\tv_mov_b32 %1, %0\n\ts_nop 1\n\t"
      "v_permlane16_swap_b32 %0, %1\n\tv_max_f32 %0, %0, %1\n\tv_mov_b32 %1, %0\n\ts_nop 1\n\t"
      "v_permlane32_swap_b32 %0, %1\n\tv_max_f32 %0, %0, %1" : "+v"(x), "=&v"(t));
  return x;
}
DEV float xsum(float x) {
  auto s = __builtin_amdgcn_permlane16_swap(__float_as_uint(x), __float_as_uint(x), false, false);
  x = __uint_as_float(s[0]) + __uint_as_float(s[1]);
  auto t = __builtin_amdgcn_permlane32_swap(__float_as_uint(x), __float_as_uint(x), false, false);
  return __uint_as_float(t[0]) + __uint_as_float(t[1]);
}
DEV unsigned aload(unsigned* p) { return __hip_atomic_load(p, __ATOMIC_RELAXED, __HIP_MEMORY_SCOPE_AGENT); }
DEV void aadd(unsigned* p, unsigned v) { __hip_atomic_fetch_add(p, v, __ATOMIC_RELAXED, __HIP_MEMORY_SCOPE_AGENT); }
DEV void astore(unsigned* p, unsigned v) { __hip_atomic_store(p, v, __ATOMIC_RELAXED, __HIP_MEMORY_SCOPE_AGENT); }

DEV bool wait_ge(unsigned* w, unsigned target, unsigned* tmo, unsigned code) {
  for (unsigned spins = 0;; ++spins) {
    if ((unsigned)__builtin_amdgcn_readfirstlane(aload(w)) >= target) { __builtin_amdgcn_fence(__ATOMIC_ACQUIRE, "wavefront"); return true; }
    __builtin_amdgcn_s_sleep(2);
    if (spins > SPIN_CAP) { astore(tmo, code); return false; }
    if ((spins & 255u) == 255u && __builtin_amdgcn_readfirstlane(aload(tmo))) return false;
  }
}
DEV bool wait_slow(unsigned* w, unsigned target, unsigned* tmo, unsigned code) {
  for (unsigned spins = 0;; ++spins) {
    if ((unsigned)__builtin_amdgcn_readfirstlane(aload(w)) >= target) { __builtin_amdgcn_fence(__ATOMIC_ACQUIRE, "wavefront"); return true; }
    __builtin_amdgcn_s_sleep(12);
    if (spins > SPIN_CAP) { astore(tmo, code); return false; }
    if ((spins & 63u) == 63u && __builtin_amdgcn_readfirstlane(aload(tmo))) return false;
  }
}
DEV bool wait_flags(unsigned* w, int n, int st, unsigned target, int lane, unsigned* tmo, unsigned code) {
  for (unsigned spins = 0;; ++spins) {
    unsigned a = aload(w + min(lane, n - 1) * st), b = aload(w + min(lane + 64, n - 1) * st);
    if (__builtin_amdgcn_ballot_w64(a < target || b < target) == 0) { __builtin_amdgcn_fence(__ATOMIC_ACQUIRE, "wavefront"); return true; }
    __builtin_amdgcn_s_sleep(6);
    if (spins > SPIN_CAP) { astore(tmo, code); return false; }
    if ((spins & 63u) == 63u && __builtin_amdgcn_readfirstlane(aload(tmo))) return false;
  }
}
DEV void wg_wait_ge(unsigned* w, unsigned target, unsigned* tmo, unsigned code, int wave) {
  if (wave == 0) wait_ge(w, target, tmo, code);
  __syncthreads();
}
DEV void wg_signal(unsigned* w, int tid) {
  drain();
  __syncthreads();
  if (tid == 0) aadd(w, 1u);
}
DEV void wait_until(unsigned t) {
  for (int guard = 0; guard < 64; ++guard) {
    int rem = (int)(t - rt());
    if (rem <= 0 || rem > 1000) return;
    if (rem <= 16) { int n = (rem * 24) >> 6; for (int i = 0; i < n; ++i) __builtin_amdgcn_s_sleep(1); return; }
    int n = ((rem - 12) * 24) >> 6; if (n > 16) n = 16;
    for (int i = 0; i < n; ++i) __builtin_amdgcn_s_sleep(1);
  }
}

#define DSTAMP(cond, slot)
#define DVAL(cond, slot, v)
#define DPT(k, ref)
#define DPDUMP(slot)
#define RAW_BARRIER() do { asm volatile("s_waitcnt lgkmcnt(0)" ::: "memory"); __builtin_amdgcn_s_barrier(); asm volatile("" ::: "memory"); } while (0)
constexpr int L_MEM = 0;
constexpr int L_KQ = 10240;
constexpr int L_SP = 43008;
constexpr int L_YP = 90112;
constexpr int L_UIMG = 10240;
constexpr int L_RED = 14336;
constexpr int L_DEAD = 51200;
constexpr int L_PXS = 98304;
constexpr int L_STAGE = 53248;
constexpr int L_X = 0;
constexpr int XBUF = 36864;
constexpr int L_V = 110592;
constexpr int L_WST = 118784;
constexpr int LDS_BYTES = 160 * 1024;

struct MemAddr { unsigned o0, o1, o2, o3; };
DEV MemAddr mem_addr(int rep, int wave, int lane) {
  MemAddr a;
  a.o0 = vo_mem(rep, wave * 3072 + lane * 16);
  a.o1 = vo_mem(rep, wave * 3072 + 1024 + lane * 16);
  a.o2 = vo_mem(rep, wave * 3072 + 2048 + lane * 16);
  a.o3 = vo_mem(rep, 9216);
  return a;
}
template <class H>
DEV unsigned gather_mem(const Params& p, __amdgpu_buffer_rsrc_t rm, char* smem, int wave, int lane, const MemAddr& a, unsigned sb, v4u& v0, v4u& v1, v4u& v2, H behind) {
  v0 = ld_mb(rm, a.o0, sb); v1 = ld_mb(rm, a.o1, sb); v2 = ld_mb(rm, a.o2, sb);
  __builtin_amdgcn_sched_barrier(0);
  behind();
  __builtin_amdgcn_sched_barrier(0);
  unsigned mx = umax(umax(max4(v0), max4(v1)), max4(v2));
  unsigned spins = 0;
  if (__builtin_expect(__builtin_amdgcn_ballot_w64(mx == SENT) != 0, 0)) {
    for (;;) {
      asm volatile("" ::: "memory");
      v0 = ld_mb(rm, a.o0, sb); v1 = ld_mb(rm, a.o1, sb); v2 = ld_mb(rm, a.o2, sb);
      mx = umax(umax(max4(v0), max4(v1)), max4(v2));
      ++spins;
      if (__builtin_amdgcn_ballot_w64(mx == SENT) == 0) break;
      if (spins > SPIN_CAP) { astore(p.ctrl + C_TMO, 1u); *(LAS unsigned*)(smem + L_DEAD) = 1; break; }
      if ((spins & 1023) == 0 && __builtin_amdgcn_readfirstlane(aload(p.ctrl + C_TMO))) { *(LAS unsigned*)(smem + L_DEAD) = 1; break; }
    }
  }
  if (wave < 3) {
    char* d = smem + L_MEM + wave * 3072 + lane * 16;
    *(v4u*)d = v0; *(v4u*)(d + 1024) = v1; *(v4u*)(d + 2048) = v2;
  }
  return spins;
}
DEV v2u sched_fix(__amdgpu_buffer_rsrc_t rm, unsigned off, unsigned sb, v2u v) {
  if (__builtin_expect((unsigned)__builtin_amdgcn_readfirstlane(umax(v.x, v.y)) == SENT, 0)) {
    for (int i = 0; i < 8 && (unsigned)__builtin_amdgcn_readfirstlane(umax(v.x, v.y)) == SENT; ++i) {
      asm volatile("" ::: "memory");
      v = ld2_mb(rm, off, sb);
    }
    if ((unsigned)__builtin_amdgcn_readfirstlane(umax(v.x, v.y)) == SENT) { unsigned n = rt(); v = (v2u){n, n}; }
  }
  return v;
}
DEV void zero_acc(f32x4 (&acc)[4]) {
#pragma unroll
  for (int i = 0; i < 4; ++i) acc[i] = (f32x4){0, 0, 0, 0};
  asm volatile("" : "+v"(acc[0]), "+v"(acc[1]), "+v"(acc[2]), "+v"(acc[3]));
}
DEV void proj36(const char* smem, const v8u (&w)[4][9], int lane, f32x4 (&acc)[4]) {
  const int idx = (lane & 8) ? 0xEEEEEEEE : 0x44444444;
  v4u a[9];
  __builtin_amdgcn_sched_barrier(0);
#pragma unroll
  for (int t = 0; t < 9; ++t) a[t] = *(const v4u*)(smem + L_MEM + t * 1024 + lane * 16);
  __builtin_amdgcn_sched_barrier(0);
#pragma unroll
  for (int t = 0; t < 9; ++t) {
#pragma unroll
    for (int i = 0; i < 4; ++i) acc[i] = smfma(a[t], w[i][t], acc[i], idx);
  }
  __builtin_amdgcn_sched_barrier(0);
}
DEV v2u pairsum(f32x4 a0, f32x4 a1) {
  float r[4];
#pragma unroll
  for (int i = 0; i < 4; ++i) {
    auto sw = __builtin_amdgcn_permlane32_swap(__float_as_uint(a0[i]), __float_as_uint(a1[i]), false, false);
    r[i] = __uint_as_float(sw[0]) + __uint_as_float(sw[1]);
  }
  return (v2u){cvtpk(r[0], r[1]), cvtpk(r[2], r[3])};
}
DEV unsigned read_pol(const Params& p) {
  return __builtin_amdgcn_readfirstlane(aload(p.ctrl + C_POL)) & 0xfffu;
}

DEV void dma16(const void* g, unsigned lds) {
  unsigned keep;
  asm volatile("s_mov_b32 %0, m0\n\ts_mov_b32 m0, %2\n\ts_nop 0\n\tglobal_load_lds_dwordx4 %1, off\n\ts_mov_b32 m0, %0"
               : "=&s"(keep) : "v"(g), "s"(lds) : "memory");
}
DEV void dma16_sc1(const void* g, unsigned lds) {
  unsigned keep;
  asm volatile("s_mov_b32 %0, m0\n\ts_mov_b32 m0, %2\n\ts_nop 0\n\tglobal_load_lds_dwordx4 %1, off sc1\n\ts_mov_b32 m0, %0"
               : "=&s"(keep) : "v"(g), "s"(lds) : "memory");
}
DEV unsigned lds_addr(const char* p) { return __builtin_amdgcn_readfirstlane((unsigned)(size_t)(LAS const char*)p); }
template <int N> DEV void wait_vm() { asm volatile("s_waitcnt vmcnt(%0)" :: "n"(N) : "memory"); }
template <int RB, class FS, class FU>
DEV void staged_load(char* smem, int wave, int lane, FS src, FU use) {
  constexpr int CH = RB / 16, BLK = 64 * RB, PW = BLK / 4096;
  constexpr int NB = (147456 / BLK) < 9 ? (147456 / BLK) : 9;
  const unsigned lb = lds_addr(smem) + wave * PW * 1024;
  auto issue = [&](int t) {
#pragma unroll
    for (int i = 0; i < PW; ++i) {
      const int ch = (wave * PW + i) * 64 + lane;
      dma16(src(64 * t + ch / CH, ch % CH), lb + (t % NB) * BLK + i * 1024);
    }
  };
  RAW_BARRIER();
#pragma unroll
  for (int t = 0; t < NB; ++t) issue(t);
#pragma unroll
  for (int t = 0; t < 9; ++t) {
    constexpr int dummy = 0; (void)dummy;
    const int younger = (t + NB <= 9 ? NB - 1 : 8 - t) * PW;
    if (younger >= 24) wait_vm<24>(); else if (younger >= 16) wait_vm<16>(); else if (younger >= 14) wait_vm<14>(); else if (younger >= 12) wait_vm<12>();
    else if (younger >= 10) wait_vm<10>(); else if (younger >= 8) wait_vm<8>(); else if (younger >= 6) wait_vm<6>(); else if (younger >= 4) wait_vm<4>();
    else if (younger >= 2) wait_vm<2>(); else wait_vm<0>();
    RAW_BARRIER();
    use(t, (t % NB) * BLK);
    if (t + NB < 9) { RAW_BARRIER(); issue(t + NB); }
  }
  RAW_BARRIER();
}

DEV f32x4 mfma16k16(v2u a, v2u b, f32x4 c) {
  return __builtin_amdgcn_mfma_f32_16x16x16bf16_1k(__builtin_bit_cast(s16x4, a), __builtin_bit_cast(s16x4, b), c, 0, 0, 0);
}
template <int WV>
DEV void head_loop(const Params& p, char* smem, int h, int lane, const v8u (&w)[4][9], unsigned pol) {
  constexpr int wave = WV;
  __amdgpu_buffer_rsrc_t rm = mkrsrc(p.mbox, MBOX_BYTES);
  __amdgpu_buffer_rsrc_t rs = mkrsrc(p.span, SPAN_BYTES);
  const unsigned pr = par_r(pol), pw = pr ^ 1u;
  const float SC = 0.08838834764831845f * 1.4426950408889634f;
  const int g = lane >> 4, qi = lane & 15, hi = (lane >> 3) & 1;
  const float NEG = -1e30f;
  const float c0 = (wave == 0 && g >= 2) ? NEG : 0.f;
  const int rep = h & 3;
  v2u selq[2], selk[2];
#pragma unroll
  for (int tl = 0; tl < 2; ++tl) {
    unsigned e[4];
#pragma unroll
    for (int jj = 0; jj < 4; ++jj) e[jj] = (4 * g + jj == 8 * tl + (qi & 7)) ? 0x3F80u : 0u;
    selq[tl] = (v2u){e[0] | (e[1] << 16), e[2] | (e[3] << 16)};
    selk[tl] = hi ? (v2u){0u, 0u} : selq[tl];
  }
  const f32x4 zero4 = {0, 0, 0, 0}, c04 = {c0, c0, c0, c0};
  const MemAddr ma = mem_addr(rep, wave == 3 ? 0 : wave, lane);
  unsigned adx = hi ? vo_p(0, 16384 + (h >> 3) * 1024 + (h & 3) * 256 + ((h >> 2) & 1) * 128 + (qi & 7) * 16)
                    : vo_p(0, (h >> 1) * 1024 + g * 256 + (h & 1) * 128 + (qi & 7) * 16);
  if (hi && g != 0) adx = 0x70000000u;
  auto px_out = [&](int sp) {
    const char* q = smem + L_PXS + (sp & 1) * 2560;
    v4u a0 = *(const v4u*)(q + lane * 16), a1 = *(const v4u*)(q + 1024 + lane * 16), a2 = *(const v4u*)(q + 2048 + (lane & 31) * 16);
    st_sc1(a0, rs, sp_off(LW_PX + sp * 81920 + h * 1024 + lane * 16, pw));
    st_sc1(a1, rs, sp_off(LW_PX + sp * 81920 + (32 + h) * 1024 + lane * 16, pw));
    if (lane < 32) st_sc1(a2, rs, sp_off(LW_PX + sp * 81920 + 65536 + (((lane >> 4) * 8 + (h >> 2)) * 64 + (h & 3) * 16 + (lane & 15)) * 16, pw));
  };
  unsigned tM = rt();
  int kq_rdy = 2;
  for (int s = 0; s < NSEG; ++s) {
    const char* kq = smem + L_KQ + (s & 1) * 16384 + lane * 16;
    v4u kxw[2];
    kxw[0] = *(const v4u*)(kq + wave * 1024); kxw[1] = *(const v4u*)(kq + (4 + wave) * 1024);
    const unsigned sb = __builtin_amdgcn_readfirstlane(sbase(s, pol));
    f32x4 acc[4]; zero_acc(acc);
    f32x4 t0, t1, t2, dq0, dq1, dk0, dk1;
    asm volatile("" : "+v"(kxw[0]), "+v"(kxw[1]));
    const unsigned tM0 = tM; (void)tM0;
    DPT(10, tM0);
    constexpr int T0 = (wave == 3) ? 0 : 3 * wave;
    const int sidx = (lane & 8) ? 0xEEEEEEEE : 0x44444444;
    {
      v4u g3[3];
      wait_until(tM);
      gather_mem(p, rm, smem, wave, lane, ma, sb, g3[0], g3[1], g3[2], [] {});
      __builtin_amdgcn_sched_barrier(0);
#pragma unroll
      for (int i = 0; i < 4; ++i) acc[i] = smfma(g3[0], w[i][T0], acc[i], sidx);
      __builtin_amdgcn_sched_barrier(0);
      DPT(11, tM0);
      RAW_BARRIER();
      v4u a[6];
#pragma unroll
      for (int j = 0; j < 6; ++j) a[j] = *(const v4u*)(smem + L_MEM + ((T0 + 3 + j) % 9) * 1024 + lane * 16);
      __builtin_amdgcn_sched_barrier(0);
#pragma unroll
      for (int j = 1; j < 3; ++j)
#pragma unroll
        for (int i = 0; i < 4; ++i) acc[i] = smfma(g3[j], w[i][T0 + j], acc[i], sidx);
#pragma unroll
      for (int j = 0; j < 6; ++j)
#pragma unroll
        for (int i = 0; i < 4; ++i) acc[i] = smfma(a[j], w[i][(T0 + 3 + j) % 9], acc[i], sidx);
      __builtin_amdgcn_sched_barrier(0);
    }
    DPT(12, tM0);
    v2u sch = ld2_mb(rm, ma.o3, sb);
    v2u pkq = pairsum(acc[0], acc[1]), pkk = pairsum(acc[2], acc[3]);
    asm volatile("" : "+v"(pkq), "+v"(pkk));
    DPT(13, tM0);
    asm("s_nop 1\n\tv_mfma_f32_16x16x16_bf16 %0, %4, %6, 0\n\tv_mfma_f32_16x16x16_bf16 %1, %4, %7, 0\n\t"
        "v_mfma_f32_16x16x16_bf16 %2, %5, %8, 0\n\tv_mfma_f32_16x16x16_bf16 %3, %5, %9, 0\n\ts_nop 7"
        : "=&v"(dq0), "=&v"(dq1), "=&v"(dk0), "=&v"(dk1) : "v"(pkq), "v"(pkk), "v"(selq[0]), "v"(selq[1]), "v"(selk[0]), "v"(selk[1]));
    v4u qf = {cvtpk(dq0[0], dq0[1]), cvtpk(dq0[2], dq0[3]), cvtpk(dq1[0], dq1[1]), cvtpk(dq1[2], dq1[3])};
    v4u kf = {cvtpk(dk0[0], dk0[1]), cvtpk(dk0[2], dk0[3]), cvtpk(dk1[0], dk1[1]), cvtpk(dk1[2], dk1[3])};
    asm("s_nop 1\n\tv_mfma_f32_16x16x32_bf16 %0, %3, %6, 0\n\tv_mfma_f32_16x16x32_bf16 %1, %4, %6, 0\n\t"
        "v_mfma_f32_16x16x32_bf16 %2, %5, %6, %7\n\ts_nop 7"
        : "=&v"(t1), "=&v"(t2), "=&v"(t0) : "v"(kxw[0]), "v"(kxw[1]), "v"(kf), "v"(qf), "v"(c04));
    f32x4 va = {hi ? t2[0] : t1[0], hi ? t2[1] : t1[1], hi ? t2[2] : t1[2], hi ? t2[3] : t1[3]};
    f32x2 vb = {hi ? t0[2] : t0[0], hi ? t0[3] : t0[1]};
    asm volatile("" : "+v"(va), "+v"(vb));
    DPT(14, tM0);
    *(v4u*)(smem + L_YP + wave * 1024 + lane * 16) = kf;
    if (wave > 0) {
      *(f32x4*)(smem + L_SP + (wave - 1) * 1536 + lane * 16) = va;
      *(f32x2*)(smem + L_SP + (wave - 1) * 1536 + 1024 + lane * 8) = vb;
    }
    RAW_BARRIER();
    DPT(15, tM0);
    if (wave == 0) {
      f32x4 pa[3]; f32x2 pb[3];
#pragma unroll
      for (int i = 0; i < 3; ++i) {
        pa[i] = *(const f32x4*)(smem + L_SP + i * 1536 + lane * 16);
        pb[i] = *(const f32x2*)(smem + L_SP + i * 1536 + 1024 + lane * 8);
      }
      __builtin_amdgcn_sched_barrier(0);
      va = (va + pa[0]) + (pa[1] + pa[2]);
      vb = (vb + pb[0]) + (pb[1] + pb[2]);
      float m = fmaxf(fmaxf(fmaxf(va[0], va[1]), va[2]), fmaxf(fmaxf(va[3], vb[0]), vb[1]));
      m = xmax8(m);
      const float ms = -m * SC;
      float v0 = __builtin_amdgcn_exp2f(fmaf(va[0], SC, ms)), v1 = __builtin_amdgcn_exp2f(fmaf(va[1], SC, ms)), v2 = __builtin_amdgcn_exp2f(fmaf(va[2], SC, ms));
      float v3 = __builtin_amdgcn_exp2f(fmaf(va[3], SC, ms)), v4 = __builtin_amdgcn_exp2f(fmaf(vb[0], SC, ms)), v5 = __builtin_amdgcn_exp2f(fmaf(vb[1], SC, ms));
      float l = ((v0 + v1) + (v2 + v3)) + (v4 + v5);
      l += dpp<ROR8>(l);
      l = xsum(l);
      float inv = __builtin_amdgcn_rcpf(l);
      v2u px = {umin(cvtpk(v0 * inv, v1 * inv), 0xFFFFFFFEu), umin(cvtpk(v2 * inv, v3 * inv), 0xFFFFFFFEu)};
      unsigned pm = umin(cvtpk(v4 * inv, v5 * inv), 0xFFFFFFFEu);
      unsigned ox = __builtin_amdgcn_mov_dpp(px.x, ROR8, 0xf, 0xf, true), oy = __builtin_amdgcn_mov_dpp(px.y, ROR8, 0xf, 0xf, true);
      unsigned pmo = __builtin_amdgcn_mov_dpp(pm, ROR8, 0xf, 0xf, true);
      auto s1 = __builtin_amdgcn_permlane16_swap(pm, pm, false, false);
      auto s2 = __builtin_amdgcn_permlane16_swap(pmo, pmo, false, false);
      v4u o;
      o.x = hi ? pmo : px.x; o.y = hi ? pm : px.y; o.z = hi ? s2[1] : ox; o.w = hi ? s1[1] : oy;
#pragma unroll
      for (int rp = 0; rp < NREP; ++rp) st_mb(o, rm, adx, sb + rp * 81920);
      *(volatile LAS int*)(smem + L_DEAD + 64) = s + 1;
    }
    DPT(16, tM0);
    if (wave == 3) {
      for (int i = 0; i < 4096 && *(volatile LAS int*)(smem + L_DEAD + 64) != s + 1; ++i) __builtin_amdgcn_s_sleep(1);
      if (s > 1 && lane == 0 && (!(s & 1) || s >= 58)) astore(p.ctrl + C_PX + h * 32, (unsigned)(s - 1));
      if (s > 0) px_out(s - 1);
    }
    if (wave == 3 || wave == 0) {
      const int sn = min(s + 1, NSEG - 1);
      const char* src = p.span + sp_off((wave == 0 ? LR_KX : LR_QX) + (sn * 32 + h) * 8192, pr) + lane * 16;
      const unsigned ldst = lds_addr(smem) + L_KQ + ((s + 1) & 1) * 16384 + (wave == 0 ? 0 : 8192);
      unsigned kf8 = 1u;
      const bool chk = (wave == 0) && kq_rdy < s + 3 && kq_rdy < NSEG;
      if (chk) kf8 = aload(p.ctrl + C_KQ + min(s + 2 + (lane & 7), NSEG - 1) * 32 + h);
#pragma unroll
      for (int i = 0; i < 8; ++i) dma16_sc1(src + i * 1024, ldst + i * 1024);
      if (chk) {
        const unsigned long long nb = __builtin_amdgcn_ballot_w64(kf8 == 0u);
        kq_rdy = s + 2 + (nb ? (int)__builtin_ctzll(nb) : 8);
        __builtin_amdgcn_fence(__ATOMIC_ACQUIRE, "wavefront");
        if (kq_rdy < s + 3) {
          if (!wait_ge(p.ctrl + C_KQ + min(s + 2, NSEG - 1) * 32 + h, 1u, p.ctrl + C_TMO, 2u)) *(LAS unsigned*)(smem + L_DEAD) = 1;
          kq_rdy = s + 3;
        }
      }
      drain();
    }
    if (wave == 1 || wave == 2) {
      constexpr int qt = (wave == 2) ? 1 : 0;
      f32x4 x1 = zero4, x2 = zero4, y = zero4;
#pragma unroll
      for (int u = 0; u < 4; ++u) {
        v4u q = *(const v4u*)(kq + 8192 + (qt * 4 + u) * 1024);
        x1 = mfma16(*(const v4u*)(kq + u * 1024), q, x1); x2 = mfma16(*(const v4u*)(kq + (4 + u) * 1024), q, x2);
        y = mfma16(*(const v4u*)(smem + L_YP + u * 1024 + lane * 16), q, y);
      }
      float v[12];
      const int tq = 16 * qt + qi;
#pragma unroll
      for (int r = 0; r < 4; ++r) {
        v[r] = (g < 2) ? y[r] * SC : NEG;
        v[4 + r] = (4 * g + r <= tq) ? x1[r] * SC : NEG;
        v[8 + r] = (16 + 4 * g + r <= tq) ? x2[r] * SC : NEG;
      }
      float m = amax3(amax3(v[0], v[1], v[2]), amax3(v[3], v[4], v[5]), fmaxf(amax3(v[6], v[7], v[8]), amax3(v[9], v[10], v[11])));
      m = xmax(m);
      float l = 0;
#pragma unroll
      for (int i = 0; i < 12; ++i) { v[i] = __builtin_amdgcn_exp2f(v[i] - m); l += v[i]; }
      l = xsum(l);
      float inv = __builtin_amdgcn_rcpf(l);
#pragma unroll
      for (int i = 0; i < 12; ++i) v[i] *= inv;
      v4u px = {cvtpk(v[4], v[5]), cvtpk(v[6], v[7]), cvtpk(v[8], v[9]), cvtpk(v[10], v[11])};
      v2u pm = {cvtpk(v[0], v[1]), cvtpk(v[2], v[3])};
      *(v4u*)(smem + L_PXS + (s & 1) * 2560 + qt * 1024 + lane * 16) = px;
      if (g < 2) *(v2u*)(smem + L_PXS + (s & 1) * 2560 + 2048 + qt * 256 + qi * 16 + g * 8) = pm;
    }
    sch = sched_fix(rm, ma.o3, sb, sch);
    tM = __builtin_amdgcn_readfirstlane(sch.y);
    RAW_BARRIER();
    if (*(volatile LAS unsigned*)(smem + L_DEAD)) break;
  }
  if (wave == 3) { px_out(NSEG - 1); drain(); if (lane == 0) astore(p.ctrl + C_PX + h * 32, (unsigned)NSEG); }
}

DEV void head_role(const Params& p, char* smem, int h, int wave, int lane, int tid) {
  v8u w[4][9];
  {
    const int gB = lane >> 4, q4 = (lane & 15) >> 2, p4 = lane & 3;
    staged_load<512>(smem, wave, lane,
      [&](int row, int cc) { return (cc < 16 ? p.Wq : p.Wk) + (size_t)row * 4096 + h * 128 + (cc & 15) * 8; },
      [&](int t, int base) {
#pragma unroll
        for (int tl = 0; tl < 4; ++tl) {
          v8u ww;
#pragma unroll
          for (int X = 0; X < 2; ++X)
#pragma unroll
            for (int half = 0; half < 2; ++half) {
              int row0 = 8 * (4 * half + 2 * X + (gB >> 1)) + 4 * (gB & 1);
              v2u v = tr_read(smem, base + (row0 + q4) * 512 + ((tl >> 1) * 128 + 32 * wave + 16 * (tl & 1) + 4 * p4) * 2);
              ww[4 * X + half] = v.x; ww[4 * X + 2 + half] = v.y;
            }
          if (tl >= 2 || (tl == 1 && t < 3)) asm volatile("" : "+a"(ww));
          w[tl][t] = ww;
        }
      });
  }
  DSTAMP(h == 0 && wave == 0, 0);
  if (tid == 0) { *(unsigned*)(smem + L_DEAD) = 0; *(unsigned*)(smem + L_DEAD + 64) = 0; }
  if (wave == 0) { wait_ge(p.ctrl + C_CLR, NSPARE, p.ctrl + C_TMO, 3u); wait_ge(p.ctrl + C_KQ + h, 1u, p.ctrl + C_TMO, 4u); wait_ge(p.ctrl + C_KQ + 32 + h, 1u, p.ctrl + C_TMO, 4u); }
  __syncthreads();
  const unsigned pol = read_pol(p);
  {
    __amdgpu_buffer_rsrc_t rs = mkrsrc(p.span, SPAN_BYTES);
    v4u v[4];
#pragma unroll
    for (int i = 0; i < 4; ++i) { int f = wave * 4 + i; v[i] = ld_sc1(rs, sp_off((wave < 2 ? LR_KX : LR_QX) + h * 8192 + (f & 7) * 1024 + lane * 16, par_r(pol))); }
#pragma unroll
    for (int i = 0; i < 4; ++i) *(v4u*)(smem + L_KQ + (wave * 4 + i) * 1024 + lane * 16) = v[i];
  }
  __syncthreads();
  DSTAMP(h == 0 && wave == 0, 1);
  if (wave == 0) head_loop<0>(p, smem, h, lane, w, pol);
  else if (wave == 1) head_loop<1>(p, smem, h, lane, w, pol);
  else if (wave == 2) head_loop<2>(p, smem, h, lane, w, pol);
  else head_loop<3>(p, smem, h, lane, w, pol);
}

constexpr int L_WOST = 0;
constexpr int L_AST = 118784;
DEV void load_wo(const Params& p, char* smem, int h, int par, int wave, int lane, int tid, v4u (&wo)[9][4]) {
  constexpr int RS = 1152;
  const int g = lane >> 4, q4 = (lane & 15) >> 2, p4 = lane & 3;
  const char* src = (const char*)(p.Wo + (size_t)h * 128 * 576) + lane * 16;
  RAW_BARRIER();
#pragma unroll
  for (int hf = 0; hf < 2; ++hf)
#pragma unroll
    for (int i = 0; i < 18; ++i) {
      const int pc = (hf ^ par) * 72 + wave * 18 + i;
      dma16(src + pc * 1024, lds_addr(smem) + pc * 1024);
    }
#pragma unroll
  for (int hf = 0; hf < 2; ++hf) {
    if (hf == 0) { wait_vm<0>(); RAW_BARRIER(); }
#pragma unroll
    for (int tl = 0; tl < 9; ++tl)
#pragma unroll
      for (int uu = 0; uu < 2; ++uu) {
        int a0 = (64 * hf + 32 * uu + 4 * g + q4) * RS + ((wave * 9 + tl) * 16 + 4 * p4) * 2;
        v2u x0 = tr_read(smem, a0), x1 = tr_read(smem, a0 + 16 * RS);
        wo[tl][2 * hf + uu] = (v4u){x0.x, x0.y, x1.x, x1.y};
      }
  }
  RAW_BARRIER();
}
struct ABlk { v4u v[4]; };
DEV void wv_block_load(const Params& p, int h, int t, int tid, ABlk& b) {
#pragma unroll
  for (int i = 0; i < 4; ++i) { int ch = tid + 256 * i; b.v[i] = *(const v4u*)(p.Wv + (size_t)(64 * t + (ch >> 4)) * 4096 + h * 128 + (ch & 15) * 8); }
}
template <int TL0, int TL1>
DEV void wvo_unit(const Params& p, char* smem, int h, int t, int slot, int wave, int lane, int tid, const ABlk& blk, const v4u (&wo)[9][4], unsigned pr) {
  const int g = lane >> 4;
  __amdgpu_buffer_rsrc_t rw = mkrsrc(p.span, SPAN_BYTES);
  char* ab = smem + L_AST + slot * 17408;
#pragma unroll
  for (int i = 0; i < 4; ++i) { int ch = tid + 256 * i; *(v4u*)(ab + (ch >> 4) * 272 + (ch & 15) * 16) = blk.v[i]; }
  RAW_BARRIER();
  v4u a[4][4];
#pragma unroll
  for (int tt = 0; tt < 4; ++tt)
#pragma unroll
    for (int u = 0; u < 4; ++u) {
      const char* src = ab + (16 * tt + (lane & 15)) * 272 + (32 * u + 4 * g) * 2;
      v2u x0 = *(const v2u*)src, x1 = *(const v2u*)(src + 32);
      a[tt][u] = (v4u){x0.x, x0.y, x1.x, x1.y};
    }
#pragma unroll
  for (int tl = TL0; tl < TL1; ++tl) {
    f32x4 d[4];
#pragma unroll
    for (int tt = 0; tt < 4; ++tt) {
      d[tt] = (f32x4){0, 0, 0, 0};
#pragma unroll
      for (int u = 0; u < 4; ++u) d[tt] = mfma16(a[tt][u], wo[tl][u], d[tt]);
    }
    const int n = (wave * 9 + tl) * 16 + (lane & 15), c = n >> 3;
    unsigned off = sp_off(LR_WVO + ((c * 16 + (h >> 1)) * 9 + t) * 2048 + (g * 16 + (h & 1) * 8 + (n & 7)) * 16, pr);
    st_sc1((v4u){cvtpk(d[0][0], d[0][1]), cvtpk(d[2][0], d[2][1]), cvtpk(d[0][2], d[0][3]), cvtpk(d[2][2], d[2][3])}, rw, off);
    st_sc1((v4u){cvtpk(d[1][0], d[1][1]), cvtpk(d[3][0], d[3][1]), cvtpk(d[1][2], d[1][3]), cvtpk(d[3][2], d[3][3])}, rw, off + 1024);
  }
}

#define SCHED_A0 124
#define SCHED_B0 67
#define LATE0 78
#define WARM_N 0
#define WARM_K 0
#define TU 0
#define PF_KQ 1
#define PF_LEAD 2
#define PF_WIN 88
#define ADAPT0 2
#define DEC_EVERY 5
DEV void col_role(const Params& p, char* smem, int c, int wave, int lane, int tid) {
  __amdgpu_buffer_rsrc_t rm = mkrsrc(p.mbox, MBOX_BYTES);
  __amdgpu_buffer_rsrc_t rs = mkrsrc(p.span, SPAN_BYTES);
  if (c < 64) {
    const int hh = ((c >> 3) << 2) | (c & 3), par = (c >> 2) & 1;
    ABlk b0, b1, b2;
    v4u wo[9][4];
    wv_block_load(p, hh, par, tid, b0); wv_block_load(p, hh, par + 2, tid, b1); wv_block_load(p, hh, par + 4, tid, b2);
    load_wo(p, smem, hh, par, wave, lane, tid, wo);
    wg_wait_ge(p.ctrl + C_POL, 0x1000u, p.ctrl + C_TMO, 9u, wave);
    const unsigned pr0 = par_r(read_pol(p));
    wvo_unit<0, 9>(p, smem, hh, par, 0, wave, lane, tid, b0, wo, pr0);
    wvo_unit<0, 9>(p, smem, hh, par + 2, 1, wave, lane, tid, b1, wo, pr0);
    wvo_unit<0, 9>(p, smem, hh, par + 4, 0, wave, lane, tid, b2, wo, pr0);
    wg_signal(p.ctrl + C_WVO + 32, tid);
  }
  if (tid == 0) *(unsigned*)(smem + L_DEAD) = 0;
  if (wave == 0) wait_ge(p.ctrl + C_CLR, NSPARE, p.ctrl + C_TMO, 5u);
  __syncthreads();
  DSTAMP(c == 0 && wave == 0, 2);
  const unsigned pol = read_pol(p);
  const int rep = c & 3;
  const unsigned slice = (c >> 3) * 1024 + ((c & 3) * 16 + ((c >> 2) & 1) * 8) * 16;
  if (wave == 0) {
    const unsigned sb0 = __builtin_amdgcn_readfirstlane(sbase(0, pol));
    if (lane < 8 * NREP) {
      int r = lane & 7, rp = lane >> 3;
      v4u v = *(const v4u*)(p.mem0 + r * 576 + 8 * c);
      v.x = umin(v.x, 0xFFFFFFFEu); v.y = umin(v.y, 0xFFFFFFFEu); v.z = umin(v.z, 0xFFFFFFFEu); v.w = umin(v.w, 0xFFFFFFFEu);
      st_mb(v, rm, vo_mem(rp, slice + r * 16), sb0);
    }
    if (c == 0) {
      unsigned now = rt(); if (now == SENT) now = 0;
      if (lane < NREP) st_mb((v4u){now, now, 0u, 0u}, rm, vo_mem(lane, 9216), sb0);
    }
    wait_ge(p.ctrl + C_WVO, 64u, p.ctrl + C_TMO, 6u);
  }
  __syncthreads();
  v8u w[4][9];
#pragma unroll
  for (int t = 8; t >= 0; --t) {
    if (t == 5) { wg_wait_ge(p.ctrl + C_WVO + 32, 64u, p.ctrl + C_TMO, 6u, wave); DSTAMP(c == 0 && wave == 0, 3); }
#pragma unroll
    for (int tl = 0; tl < 4; ++tl) {
      unsigned off = sp_off(LR_WVO + ((c * 16 + wave * 4 + tl) * 9 + t) * 2048, par_r(pol)) + lane * 16;
      v4u x0 = ld_sc1(rs, off), x1 = ld_sc1(rs, off + 1024);
      w[tl][t] = __builtin_shufflevector(x0, x1, 0, 1, 2, 3, 4, 5, 6, 7);
    }
  }
#pragma unroll
  for (int tl = 1; tl < 4; ++tl)
#pragma unroll
    for (int t = 0; t < 9; ++t)
      if (tl >= 2 || t < 3) asm volatile("" : "+a"(w[tl][t]));
  auto loop = [&](auto WV) {
  constexpr int wave = decltype(WV)::value;
  const int g = lane >> 4, half = (lane >> 3) & 1, n7 = lane & 7;
  const int uw_off = L_UIMG + (((2 * (4 * wave + (lane >> 5)) + ((lane & 15) >> 3)) * 8 + (lane & 7)) * 8 + 4 * ((lane >> 4) & 1)) * 2;
  const int red_w = ((g >> 1) == half) ? L_RED + (((wave * 2 + half) * 8 + n7) * 8 + 4 * (g & 1)) * 4 : L_RED + 2048 + lane * 16;
  const int red_r = L_RED + (((lane >> 1) & 7) * 8 + 4 * (lane & 1)) * 4;
  const MemAddr ma = mem_addr(rep, wave, lane);
  const unsigned po = vo_p(rep, wave * 4096 + lane * 16), pm_o = vo_p(rep, 16384 + wave * 1024 + lane * 16);
  const unsigned ado = (lane & 1) ? 0x70000000u : vo_mem(lane >> 4, slice + ((lane >> 1) & 7) * 16);
  const unsigned pw = par_r(pol) ^ 1u;
  DSTAMP(c == 0 && wave == 0, 20);
  wait_ge(p.ctrl + C_UX, 32u, p.ctrl + C_TMO, 7u);
  DSTAMP(c == 0 && wave == 0, 21);
  int rdy = 1;
  unsigned tM = rt();
  int schedA = SCHED_A0, schedB = SCHED_B0, streak = 0, flip = 0; bool prevLate = true; int lateM = LATE0;
  unsigned sb = __builtin_amdgcn_readfirstlane(sbase(0, pol));
  for (int s = 0; s < NSEG; ++s) {
    f32x4 acc[4]; zero_acc(acc);
    unsigned mfail = 0;
    const unsigned tM0 = tM; (void)tM0;
    DPT(0, tM0);
    v4u ux[4];
    const unsigned uso = __builtin_amdgcn_readfirstlane(sp_off(LW_UX + (s * 72 + c) * 16384 + wave * 4096, pw));
    auto ldux = [&] {
#pragma unroll
      for (int q = 0; q < 4; ++q) ux[q] = __builtin_amdgcn_raw_buffer_load_b128(rs, q * 1024 + lane * 16, uso, 16);
    };
    if (wave < 3) { wait_until(tM); v4u g0, g1, g2; mfail = gather_mem(p, rm, smem, wave, lane, ma, sb, g0, g1, g2, [] {}); }
    DPT(1, tM0);
    RAW_BARRIER();
    DPT(2, tM0);
    ldux();
    v2u sch = ld2_mb(rm, ma.o3, sb);
    unsigned ofail = SENT;
    if (wave == 3 && c == 0) ofail = __builtin_amdgcn_raw_buffer_load_b32(rm, vo_mem(0, 9216 + 128 * (1 + (lane % 3))), sb, 16);
    unsigned fl2 = 32u;
    const bool chk = rdy < s + 3 && rdy < NSEG;
    if (__builtin_expect(chk, 0)) fl2 = aload(p.ctrl + C_UX + lane);
    if (TU > 0 && s < NSEG - 1) {
      sch = sched_fix(rm, ma.o3, sb, sch);
      wait_until(__builtin_amdgcn_readfirstlane(sch.x) - (unsigned)TU);
    }
    proj36(smem, w, lane, acc);
    v2u pk0 = pairsum(acc[0], acc[1]), pk1 = pairsum(acc[2], acc[3]);
    asm volatile("" : "+v"(pk0), "+v"(pk1));
    DPT(3, tM0);
    *(v2u*)(smem + uw_off) = pk0;
    *(v2u*)(smem + uw_off + 512) = pk1;
    v4u ub = *(const v4u*)(smem + L_UIMG + ((8 * wave + 4 * half + g) * 8 + n7) * 16);
    const unsigned sbn = __builtin_amdgcn_readfirstlane(sbase(s + 1, pol));
    const unsigned umo = __builtin_amdgcn_readfirstlane(sp_off(LW_UM + (s * 72 + c) * 4096, pw));
    if (s < NSEG - 1) {
      asm volatile("s_waitcnt vmcnt(0)" : "+v"(ux[0]), "+v"(ux[1]), "+v"(ux[2]), "+v"(ux[3]), "+v"(ub), "+v"(fl2), "+v"(sch), "+v"(ofail) :: "memory");
      if (__builtin_expect(chk, 0)) {
        unsigned long long nb = __builtin_amdgcn_ballot_w64(fl2 < 32u);
        rdy = nb ? (int)__builtin_ctzll(nb) : NSEG;
        __builtin_amdgcn_fence(__ATOMIC_ACQUIRE, "wavefront");
        if (rdy < s + 2) {
          if (!wait_ge(p.ctrl + C_UX + s + 1, 32u, p.ctrl + C_TMO, 7u)) *(LAS unsigned*)(smem + L_DEAD) = 1;
          rdy = s + 2;
        }
      }
      DPT(4, tM0);
      sch = sched_fix(rm, ma.o3, sb, sch);
      const unsigned tP = __builtin_amdgcn_readfirstlane(sch.x);
      tM = __builtin_amdgcn_readfirstlane(sch.y);
      f32x4 d = {0, 0, 0, 0};
      DPT(5, tM0);
      wait_until(tP);
      DPT(6, tP);
      unsigned pfail = 0;
      {
        v4u v[5];
#pragma unroll
        for (int q = 0; q < 4; ++q) { v[q] = ld_mb(rm, po + q * 1024, sb); __builtin_amdgcn_sched_barrier(0); }
        v[4] = ld_mb(rm, pm_o, sb);
        unsigned mx = 0;
        __builtin_amdgcn_sched_barrier(0);
#pragma unroll
        for (int q = 0; q < 4; ++q) { d = mfma16(ux[q], v[q], d); mx = umax(mx, max4(v[q])); __builtin_amdgcn_sched_barrier(0); }
        d = mfma16(ub, v[4], d); mx = umax(mx, max4(v[4]));
        if (__builtin_expect(__builtin_amdgcn_ballot_w64(mx == SENT) != 0, 0)) {
          for (;;) {
            asm volatile("" ::: "memory");
#pragma unroll
            for (int q = 0; q < 4; ++q) v[q] = ld_mb(rm, po + q * 1024, sb);
            v[4] = ld_mb(rm, pm_o, sb);
            mx = 0;
#pragma unroll
            for (int q = 0; q < 5; ++q) mx = umax(mx, max4(v[q]));
            ++pfail;
            if (__builtin_amdgcn_ballot_w64(mx == SENT) == 0) break;
            if (pfail > SPIN_CAP) { astore(p.ctrl + C_TMO, 8u); *(LAS unsigned*)(smem + L_DEAD) = 1; break; }
            if ((pfail & 1023) == 0 && __builtin_amdgcn_readfirstlane(aload(p.ctrl + C_TMO))) { *(LAS unsigned*)(smem + L_DEAD) = 1; break; }
          }
          d = (f32x4){0, 0, 0, 0};
#pragma unroll
          for (int q = 0; q < 4; ++q) d = mfma16(ux[q], v[q], d);
          d = mfma16(ub, v[4], d);
        }
      }
      DPT(7, tP);
      *(f32x4*)(smem + red_w) = d;
      if (c < 4) *(LAS unsigned*)(smem + L_DEAD + 16 + wave * 4) = (mfail ? 1u : 0u) | (pfail ? 2u : 0u);
      RAW_BARRIER();
      if (wave == 0) {
        f32x4 xb[8];
#pragma unroll
        for (int b8 = 0; b8 < 8; ++b8) xb[b8] = *(const f32x4*)(smem + red_r + b8 * 256);
        __builtin_amdgcn_sched_barrier(0);
        f32x4 x = ((xb[0] + xb[1]) + (xb[2] + xb[3])) + ((xb[4] + xb[5]) + (xb[6] + xb[7]));
        float r0 = x[0], r1 = x[1], r2 = x[2], r3 = x[3];
        v2u o2 = {umin(cvtpk(r0, r1), 0xFFFFFFFEu), umin(cvtpk(r2, r3), 0xFFFFFFFEu)};
        v4u o4 = {o2.x, o2.y, (unsigned)__builtin_amdgcn_mov_dpp(o2.x, 0xB1, 0xf, 0xf, true), (unsigned)__builtin_amdgcn_mov_dpp(o2.y, 0xB1, 0xf, 0xf, true)};
        st_mb(o4, rm, ado, sbn);
        DPT(8, tP);
      } else if (wave == 3 && c >= 1 && c <= 3) {
        v4u f = *(const v4u*)(smem + L_DEAD + 16);
        if (((f.x | f.y | f.z | f.w) & 2u) && lane == 0) __builtin_amdgcn_raw_buffer_store_b32(0u, rm, vo_mem(0, 9216 + 128 * c), sbn, 16);
      } else if (wave == 3 && c == 0) {
        v4u f = *(const v4u*)(smem + L_DEAD + 16);
        unsigned fl = __builtin_amdgcn_readfirstlane(f.x | f.y | f.z | f.w);
        unsigned now = rt();
        unsigned baseT = tM;
        const bool late = (int)(now - tM) > 2 || (int)(tM - now) > 600;
        if (late) baseT = now + (unsigned)lateM;
        if (prevLate && (fl & 2u)) { if (lateM < 80) lateM += 6; } else if (!late && !prevLate && lateM > LATE0) lateM -= 2;
        if (!prevLate && s >= ADAPT0) {
          const bool oth = __builtin_amdgcn_ballot_w64(ofail == 0u) != 0;
          if (fl & 1u) { if (oth) schedA += 2; else schedB += 2; }
          else if (fl & 2u) schedA += 2;
          else if (late) schedB += 2;
          else if (++streak >= DEC_EVERY) { streak = 0; if (++flip & 1) schedA -= 1; else schedB -= 1; }
        }
        prevLate = late || (fl & 2u);
        const int warm = s < WARM_N ? (WARM_N - s) * WARM_K : 0;
        unsigned nP = baseT + (unsigned)(schedA + warm), nM = nP + (unsigned)(schedB + warm);
        if (nP == SENT) nP = 0; if (nM == SENT) nM = 0;
        if (lane < NREP) st_mb((v4u){nP, nM, 0u, 0u}, rm, vo_mem(lane, 9216), sbn);
        DVAL(true, 512 + s, (unsigned)clk());
        DVAL(true, 64 + s, now); DVAL(true, 128 + s, fl | (schedA << 8) | (schedB << 20));
      }
    }
    else { __builtin_amdgcn_raw_buffer_store_b128(ub, rs, wave * 1024 + lane * 16, umo, 16); drain(); RAW_BARRIER(); }
    if (s < NSEG - 1) __builtin_amdgcn_raw_buffer_store_b128(ub, rs, wave * 1024 + lane * 16, umo, 16);
    if (wave == 3 && lane == 0 && ((s & 1) || s >= 58)) astore(p.ctrl + C_UM + c * 32, (unsigned)(s + 1 >= NSEG ? NSEG : s));
    sb = sbn;
    if (*(volatile LAS unsigned*)(smem + L_DEAD)) break;
  }
  };
  if (wave == 0) loop(IC<0>{}); else if (wave == 1) loop(IC<1>{}); else if (wave == 2) loop(IC<2>{}); else loop(IC<3>{});
  __syncthreads();
}

DEV void spare_role(const Params& p, char* smem, int j, int wave, int lane, int tid) {
  __amdgpu_buffer_rsrc_t rm = mkrsrc(p.mbox, MBOX_BYTES);
  if (j == 0 && tid == 0) {
    const unsigned grp = (__builtin_amdgcn_s_getreg(20 | (3 << 11)) >> 2) & 1u;
    unsigned m = 0;
    for (int i = 0; i < NPAGES; ++i) { unsigned r = p.ctrl[C_CAL + i]; m |= ((r ^ (r >> 1) ^ grp) & 1u) << i; }
    const unsigned full = (1u << NPAGES) - 1u, alt = 0x555u & full;
    const int agree = __builtin_popcount(~(m ^ alt) & full);
    if (agree >= NPAGES - 3) m = alt; else if (agree <= 3) m = ~alt & full;
    astore(p.ctrl + C_POL, 0x1000u | m);
  }
  wg_wait_ge(p.ctrl + C_POL, 0x1000u, p.ctrl + C_TMO, 9u, wave);
  const unsigned pol = read_pol(p);
  DSTAMP(j == 0 && wave == 0, 4); DVAL(j == 0 && wave == 0, 5, pol);
  {
    const v4u sv = {SENT, SENT, SENT, SENT};
    for (int u = j; u < NSEG * NREP * 5; u += NSPARE) {
      const int st = u / (NREP * 5), cell = u % (NREP * 5), k = cell % 5;
      const unsigned sb = __builtin_amdgcn_readfirstlane(sbase(st, pol));
      if (k != 1 || tid < 128) st_mb(sv, rm, cell * 16384 + tid * 16, sb);
      if (k != 1 && k != 4) st_mb(sv, rm, cell * 16384 + 4096 + tid * 16, sb);
    }
  }
  wg_signal(p.ctrl + C_CLR, tid);
  DSTAMP(j == 0 && wave == 0, 6);
  {
    __amdgpu_buffer_rsrc_t rs = mkrsrc(p.span, SPAN_BYTES);
    const unsigned pr = par_r(pol);
    const int xg = j & 3, rk = j >> 2;
    auto wait_slot = [&](int k) {
      const unsigned sbk = __builtin_amdgcn_readfirstlane(sbase(k, pol));
      unsigned tp = SENT;
      for (unsigned spins = 0; spins < SPIN_CAP; ++spins) {
        asm volatile("" ::: "memory");
        v2u sc2 = ld2_mb(rm, vo_mem(xg, 9216), sbk);
        tp = __builtin_amdgcn_readfirstlane(sc2.x);
        if (tp != SENT) break;
        __builtin_amdgcn_s_sleep(8);
        if ((spins & 63u) == 63u && __builtin_amdgcn_readfirstlane(aload(p.ctrl + C_TMO))) break;
      }
      return tp;
    };
    auto relay = [&]() {
      if (j == 0) {
        unsigned a = aload(p.ctrl + C_UM + lane * 32), b = aload(p.ctrl + (lane < 8 ? C_UM + (64 + lane) * 32 : C_PX + (lane & 31) * 32));
        unsigned c3 = aload(p.ctrl + C_PX + (lane & 7) * 32);
        unsigned m = umin(umin(a, b), c3);
#pragma unroll
        for (int o = 1; o < 64; o <<= 1) m = umin(m, (unsigned)__shfl_xor((int)m, o));
        if (lane < 16) __hip_atomic_fetch_max(p.ctrl + C_PROG + lane * 32, m, __ATOMIC_RELAXED, __HIP_MEMORY_SCOPE_AGENT);
      }
    };
    constexpr int LEAD = PF_LEAD;
    int rdy = 0;
    for (int s = 1; s < NSEG; ++s) {
      if (wave == 0) {
        if (rdy <= s) {
          wait_slow(p.ctrl + C_UX + s, 32u, p.ctrl + C_TMO, 15u);
          wait_flags(p.ctrl + C_KQ + s * 32, 32, 1, 1u, lane, p.ctrl + C_TMO, 18u);
          rdy = s + 1;
          const int s2 = min(s + 4, NSEG - 1);
          unsigned u5 = aload(p.ctrl + C_UX + min(s + 1 + (lane & 3), NSEG - 1));
          unsigned k5a = aload(p.ctrl + C_KQ + min(s + 1 + (lane >> 5), NSEG - 1) * 32 + (lane & 31));
          unsigned k5b = aload(p.ctrl + C_KQ + min(s + 3 + (lane >> 5), NSEG - 1) * 32 + (lane & 31));
          if (__builtin_amdgcn_ballot_w64(u5 < 32u || k5a == 0u || k5b == 0u) == 0) rdy = s2 + 1;
          __builtin_amdgcn_fence(__ATOMIC_ACQUIRE, "wavefront");
        }
        if (s > LEAD) {
          unsigned tp = wait_slot(s - LEAD);
          wait_until(tp - (unsigned)PF_WIN);
        }
      }
      __syncthreads();
      v4u v[12];
#pragma unroll
      for (int i = 0; i < 12; ++i) v[i] = ld_sc1(rs, sp_off(LW_UX + (s * 72 + xg + 4 * (3 * rk + i / 4)) * 16384 + ((i & 3) * 256 + tid) * 16, pr ^ 1u));
      v4u k6[6];
#pragma unroll
      for (int i = 0; i < 6; ++i) {
        int ch = rk * 6 + i;
        unsigned off = (ch < 32) ? sp_off(((ch >> 1) & 1) * SZ_KX + (s * 32 + xg + 4 * (ch >> 2)) * 8192 + (ch & 1) * 4096 + tid * 16, pr) : 0x7ffffff0u;
        k6[i] = ld_sc1(rs, off);
      }
#pragma unroll
      for (int i = 0; i < 6; ++i) asm volatile("" :: "v"(k6[i]));
#pragma unroll
      for (int i = 0; i < 12; ++i) asm volatile("" :: "v"(v[i]));
      if (wave == 0 && s > LEAD && !(s & 1)) relay();
    }
    drain();
    if (j == 0 && wave == 0) {
      for (int k = NSEG - LEAD; k < NSEG; ++k) { unsigned tp = wait_slot(k); wait_until(tp - 80u); relay(); }
      for (unsigned spins = 0; spins < SPIN_CAP && (unsigned)__builtin_amdgcn_readfirstlane(aload(p.ctrl + C_PROG)) < 60u; ++spins) {
        relay();
        __builtin_amdgcn_s_sleep(8);
        if (__builtin_amdgcn_readfirstlane(aload(p.ctrl + C_TMO))) break;
      }
    }
  }
}

DEV int tile_src_off(int wave, int lane) {
  const int row = wave * 8 + (lane >> 3), r = row & 15;
  return row * 576 + (((lane & 7) ^ ((r >> 1) & 7)) * 8);
}
struct XDma { const u16* src; char* dst; bool on; };
DEV XDma x_dma_setup(const u16* x, char* smem, int s, int buf, int wave, int lane, bool on) {
  XDma d;
  d.src = x + (size_t)s * 32 * 576 + tile_src_off(wave, lane);
  d.dst = smem + L_X + buf * XBUF + wave * 1024;
  d.on = on;
  return d;
}
DEV void x_dma_piece(const XDma& d, int wave, int i) {
  if (d.on) __builtin_amdgcn_global_load_lds((const unsigned*)(d.src + i * 64), (LAS unsigned*)(d.dst + i * 4096), 16, 0, 0);
}
template <int DEPTH, class F, class G>
DEV void xloop(const char* tile, int lane, F body, G side) {
  const int r = lane & 15, g = lane >> 4, f = (r >> 1) & 7;
  const char* pe = tile + (r >> 3) * 1024 + (r & 7) * 128 + ((g ^ f) * 16);
  const char* po = tile + (r >> 3) * 1024 + (r & 7) * 128 + (((4 + g) ^ f) * 16);
  auto rd = [&](int ks, int tt) { return *(const v4u*)(((ks & 1) ? po : pe) + (ks >> 1) * 4096 + tt * 2048); };
  v4u f0[DEPTH + 1], f1[DEPTH + 1];
#pragma unroll
  for (int i = 0; i < DEPTH; ++i) { f0[i] = rd(i, 0); f1[i] = rd(i, 1); }
#pragma unroll
  for (int ks = 0; ks < 18; ++ks) {
    if (ks + DEPTH < 18) {
      f0[(ks + DEPTH) % (DEPTH + 1)] = rd(ks + DEPTH, 0);
      f1[(ks + DEPTH) % (DEPTH + 1)] = rd(ks + DEPTH, 1);
    }
    __builtin_amdgcn_sched_barrier(0);
    body(ks, f0[ks % (DEPTH + 1)], f1[ks % (DEPTH + 1)]);
    __builtin_amdgcn_sched_barrier(0);
    side(ks);
  }
}
template <int NST, int EARLY, class FC, class FS>
DEV void stream(const u16* x, char* smem, int s0, int st, int wave, int lane, int tid, FC compute, FS signal) {
  const int n = (NSEG - s0 + st - 1) / st;
  {
    XDma d0 = x_dma_setup(x, smem, s0, 0, wave, lane, true), d1 = x_dma_setup(x, smem, min(s0 + st, NSEG - 1), 1, wave, lane, n > 1);
#pragma unroll
    for (int i = 0; i < 9; ++i) x_dma_piece(d0, wave, i);
#pragma unroll
    for (int i = 0; i < 9; ++i) x_dma_piece(d1, wave, i);
  }
  int buf = 0;
  for (int i = 0; i < n; ++i) {
    const int s = s0 + i * st;
    if (i == 0 && n > 1) wait_vm<9>(); else if (i <= EARLY || i + 1 >= n) wait_vm<0>(); else wait_vm<9 + NST>();
    RAW_BARRIER();
    if (tid == 0) {
      if (i >= 1 && i <= EARLY) signal(s - st);
      else if (i >= EARLY + 2) signal(s - 2 * st);
      if (i > EARLY && i + 1 >= n) signal(s - st);
    }
    XDma d = x_dma_setup(x, smem, min(s + 2 * st, NSEG - 1), buf == 0 ? 2 : buf - 1, wave, lane, i + 2 < n);
    compute(s, smem + L_X + buf * XBUF, d);
    buf = buf == 2 ? 0 : buf + 1;
  }
  wait_vm<0>();
  RAW_BARRIER();
  if (tid == 0) signal(s0 + (n - 1) * st);
}
template <int NTW, class F>
DEV void load_kfrags(char* smem, int wave, int lane, int tid, F srcf, v4u (&w)[NTW][18]) {
  constexpr int RS = NTW * 128;
  const int g = lane >> 4, q4 = (lane & 15) >> 2, p4 = lane & 3;
  staged_load<RS>(smem, wave, lane, srcf, [&](int t, int base) {
#pragma unroll
    for (int tl = 0; tl < NTW; ++tl)
#pragma unroll
      for (int kk = 0; kk < 2; ++kk) {
        int a0 = base + (32 * kk + 8 * g + q4) * RS + ((wave * NTW + tl) * 16 + 4 * p4) * 2;
        v2u x0 = tr_read(smem, a0), x1 = tr_read(smem, a0 + 4 * RS);
        w[tl][2 * t + kk] = (v4u){x0.x, x0.y, x1.x, x1.y};
      }
  });
}

DEV void kq_worker(const Params& p, char* smem, int h, int wave, int lane, int tid) {
  v4u w[4][18];
  load_kfrags<4>(smem, wave, lane, tid, [&](int row, int cc) {
    return (cc < 16 ? p.Wk : p.Wq) + (size_t)row * 4096 + h * 128 + (cc & 15) * 8; }, w);
  DSTAMP(h == 0 && wave == 0, 40);
  __amdgpu_buffer_rsrc_t ro = mkrsrc(p.span, SPAN_BYTES);
  wg_wait_ge(p.ctrl + C_POL, 0x1000u, p.ctrl + C_TMO, 9u, wave);
  const unsigned pr = par_r(read_pol(p));
  DSTAMP(h == 0 && wave == 0, 41);
  stream<4, 4>(p.x, smem, 0, 1, wave, lane, tid,
    [&](int s, const char* xb, const XDma& dm) {
      DSTAMP(h == 0 && wave == 0 && s < 4, 42 + s);
      f32x4 acc[2][4];
#pragma unroll
      for (int a = 0; a < 2; ++a)
#pragma unroll
        for (int b = 0; b < 4; ++b) acc[a][b] = (f32x4){0, 0, 0, 0};
      xloop<2>(xb, lane, [&](int ks, v4u b0, v4u b1) {
#pragma unroll
        for (int tl = 0; tl < 4; ++tl) { acc[0][tl] = mfma16(w[tl][ks], b0, acc[0][tl]); acc[1][tl] = mfma16(w[tl][ks], b1, acc[1][tl]); }
      }, [&](int ks) { if (!(ks & 1)) x_dma_piece(dm, wave, ks >> 1); });
#pragma unroll
      for (int tt = 0; tt < 2; ++tt)
#pragma unroll
        for (int uu = 0; uu < 2; ++uu) {
          f32x4 a = acc[tt][2 * uu], b = acc[tt][2 * uu + 1];
          v4u o = {cvtpk(a[0], a[1]), cvtpk(a[2], a[3]), cvtpk(b[0], b[1]), cvtpk(b[2], b[3])};
          st_sc1(o, ro, sp_off((wave < 2 ? LR_KX : LR_QX) + (s * 32 + h) * 8192, pr) + (tt * 4 + (wave & 1) * 2 + uu) * 1024 + lane * 16);
        }
      DSTAMP(h == 0 && wave == 1, 256 + s); DVAL(h == 0 && wave == 1, 320 + s, (unsigned)clk());
    },
    [&](int s) { astore(p.ctrl + C_KQ + s * 32 + h, 1u); });
}

DEV void vu_worker(const Params& p, char* smem, int h, int par, int wave, int lane, int tid) {
  const int g = lane >> 4;
  v4u wo[9][4];
  ABlk b0, b1;
  wv_block_load(p, h, 6 + par, tid, b0); wv_block_load(p, h, 8, tid, b1);
  load_wo(p, smem, h, par, wave, lane, tid, wo);
  v4u wv[2][18];
  load_kfrags<2>(smem, wave, lane, tid, [&](int row, int cc) {
    return p.Wv + (size_t)row * 4096 + h * 128 + cc * 8; }, wv);
  DSTAMP(h == 0 && par == 0 && wave == 0, 8);
  wg_wait_ge(p.ctrl + C_POL, 0x1000u, p.ctrl + C_TMO, 9u, wave);
  const unsigned pr = par_r(read_pol(p));
  wvo_unit<0, 9>(p, smem, h, 6 + par, 0, wave, lane, tid, b0, wo, pr);
  if (par == 0) wvo_unit<0, 5>(p, smem, h, 8, 1, wave, lane, tid, b1, wo, pr);
  else wvo_unit<5, 9>(p, smem, h, 8, 1, wave, lane, tid, b1, wo, pr);
  wg_signal(p.ctrl + C_WVO, tid);
  DSTAMP(h == 0 && par == 0 && wave == 0, 9);
  __amdgpu_buffer_rsrc_t ro = mkrsrc(p.span, SPAN_BYTES);
  stream<9, 4>(p.x, smem, par, 2, wave, lane, tid,
    [&](int s, const char* xb, const XDma& dm) {
      f32x4 acc[2][2];
#pragma unroll
      for (int a = 0; a < 2; ++a)
#pragma unroll
        for (int b = 0; b < 2; ++b) acc[a][b] = (f32x4){0, 0, 0, 0};
      xloop<3>(xb, lane, [&](int ks, v4u b0, v4u b1) {
#pragma unroll
        for (int tl = 0; tl < 2; ++tl) { acc[0][tl] = mfma16(wv[tl][ks], b0, acc[0][tl]); acc[1][tl] = mfma16(wv[tl][ks], b1, acc[1][tl]); }
      }, [&](int ks) { if (!(ks & 1)) x_dma_piece(dm, wave, ks >> 1); });
#pragma unroll
      for (int tt = 0; tt < 2; ++tt) {
        f32x4 a = acc[tt][0], b = acc[tt][1];
        *(v4u*)(smem + L_V + (tt * 4 + wave) * 1024 + lane * 16) = (v4u){cvtpk(a[0], a[1]), cvtpk(a[2], a[3]), cvtpk(b[0], b[1]), cvtpk(b[2], b[3])};
      }
      RAW_BARRIER();
      v4u a[2][4];
#pragma unroll
      for (int tt = 0; tt < 2; ++tt)
#pragma unroll
        for (int u = 0; u < 4; ++u) a[tt][u] = *(const v4u*)(smem + L_V + (tt * 4 + u) * 1024 + lane * 16);
#pragma unroll
      for (int tl = 0; tl < 9; ++tl) {
        f32x4 d0 = {0, 0, 0, 0}, d1 = {0, 0, 0, 0};
#pragma unroll
        for (int u = 0; u < 4; ++u) { d0 = mfma16(a[0][u], wo[tl][u], d0); d1 = mfma16(a[1][u], wo[tl][u], d1); }
        const int n = (wave * 9 + tl) * 16 + (lane & 15), c = n >> 3;
        v4u o = {cvtpk(d0[0], d0[1]), cvtpk(d0[2], d0[3]), cvtpk(d1[0], d1[1]), cvtpk(d1[2], d1[3])};
        st_sc1(o, ro, sp_off(LW_UX + ((s * 72 + c) * 16 + (h >> 1)) * 1024 + (g * 16 + (h & 1) * 8 + (n & 7)) * 16, pr ^ 1u));
      }
      DSTAMP(h == 0 && wave == 1, 192 + s); DVAL(h == 0 && wave == 1, 384 + s, (unsigned)clk());
    },
    [&](int s) { aadd(p.ctrl + C_UX + s, 1u); });
}

DEV void b1_worker(const Params& p, char* smem, int gi, int wave, int lane, int tid) {
  {
    const unsigned lb = lds_addr(smem) + wave * 1024;
#pragma unroll
    for (int i = 0; i < 9; ++i) dma16(p.x + (size_t)(gi >> 2) * 32 * 576 + (i * 256 + tid) * 8, lb + 65536 + (i & 7) * 4096);
#pragma unroll
    for (int i = 0; i < 18; ++i) dma16(p.x + (size_t)gi * 64 * 576 + (i * 256 + tid) * 8, lb + 65536 + (i & 7) * 4096);
    const u16* src = (((tid & 31) < 16) ? p.Wk : p.Wq) + (size_t)(256 + (tid >> 5)) * 4096 + gi * 128 + (tid & 15) * 8;
#pragma unroll 4
    for (int i = 0; i < 40; ++i) dma16(src + (size_t)i * 8 * 4096, lb + (i & 7) * 4096);
    drain();
  }
  if (gi < 28) {
    const int slab = gi >> 2, sub = gi & 3;
    DSTAMP(gi == 0 && wave == 0, 30);
    v4u w[4][18];
    load_kfrags<4>(smem, wave, lane, tid, [&](int row, int cc) {
      int col = slab * 256 + cc * 8; if (col > 1720) col = 1720;
      return p.wqkv + (size_t)row * 1728 + col; }, w);
    DSTAMP(gi == 0 && wave == 0, 31);
    __amdgpu_buffer_rsrc_t rqk = mkrsrc(p.data + OFF_QKB, (unsigned)(2048 * 1152 * 2));
    __amdgpu_buffer_rsrc_t rvt = mkrsrc(p.data + OFF_VTB, (unsigned)(576 * 2048 * 2));
    const int g = lane >> 4;
    const int col0 = slab * 256 + wave * 64;
    const bool isv = col0 >= 1152;
    stream<4, 1>(p.x, smem, sub, 4, wave, lane, tid,
      [&](int s, const char* xb, const XDma& dm) {
        f32x4 acc[2][4];
#pragma unroll
        for (int a = 0; a < 2; ++a)
#pragma unroll
          for (int b = 0; b < 4; ++b) acc[a][b] = (f32x4){0, 0, 0, 0};
        if (!isv) {
          xloop<2>(xb, lane, [&](int ks, v4u b0, v4u b1) {
#pragma unroll
            for (int tl = 0; tl < 4; ++tl) { acc[0][tl] = mfma16(w[tl][ks], b0, acc[0][tl]); acc[1][tl] = mfma16(w[tl][ks], b1, acc[1][tl]); }
          }, [&](int ks) { if (!(ks & 1)) x_dma_piece(dm, wave, ks >> 1); });
#pragma unroll
          for (int tt = 0; tt < 2; ++tt)
#pragma unroll
            for (int tp = 0; tp < 2; ++tp) {
              f32x4 a = acc[tt][2 * tp], b = acc[tt][2 * tp + 1];
              auto r0 = __builtin_amdgcn_permlane16_swap(cvtpk(a[0], a[1]), cvtpk(b[0], b[1]), false, false);
              auto r1 = __builtin_amdgcn_permlane16_swap(cvtpk(a[2], a[3]), cvtpk(b[2], b[3]), false, false);
              st_sc1((v4u){r0[0], r1[0], r0[1], r1[1]}, rqk,
                     ((s * 32 + tt * 16 + (lane & 15)) * 1152 + col0 + 16 * (2 * tp + (g & 1)) + 8 * (g >> 1)) * 2);
            }
        } else {
          xloop<2>(xb, lane, [&](int ks, v4u b0, v4u b1) {
#pragma unroll
            for (int tl = 0; tl < 4; ++tl) { acc[0][tl] = mfma16(b0, w[tl][ks], acc[0][tl]); acc[1][tl] = mfma16(b1, w[tl][ks], acc[1][tl]); }
          }, [&](int ks) { if (!(ks & 1)) x_dma_piece(dm, wave, ks >> 1); });
#pragma unroll
          for (int tl = 0; tl < 4; ++tl) {
            f32x4 a = acc[0][tl], b = acc[1][tl];
            st_sc1((v4u){cvtpk(a[0], a[1]), cvtpk(a[2], a[3]), cvtpk(b[0], b[1]), cvtpk(b[2], b[3])}, rvt,
                   (((col0 - 1152 + tl * 16 + (lane & 15)) * 64 + s) * 4 + g) * 16);
          }
        }
      },
      [&](int) {});
    DSTAMP(gi == 0 && wave == 0, 32);
  }
  wg_signal(p.ctrl + C_B1, tid);
}

DEV void b2_item(const Params& p, char* smem, int hg, int qb, int wave, int lane, int tid) {
  __amdgpu_buffer_rsrc_t rqk = mkrsrc(p.data + OFF_QKB, (unsigned)(2048 * 1152 * 2));
  __amdgpu_buffer_rsrc_t rvt = mkrsrc(p.data + OFF_VTB, (unsigned)(576 * 2048 * 2));
  __amdgpu_buffer_rsrc_t rob = mkrsrc(p.data + OFF_OB, (unsigned)SZ_OB);
  const int g = lane >> 4, li = lane & 15;
  const int hb = hg * 4 + wave;
  const int q0 = qb * 128, k0 = q0 - 256;
  const float SC = 0.23570226039551584f * 1.4426950408889634f;
  const float NEG = -1e30f;
  const unsigned OOB = 0x7ffffff0u;
  constexpr int LK = 0, LQ = 384 * 144, LV = 512 * 144;
#pragma unroll
  for (int part = 0; part < 2; ++part) {
    v4u t[9];
#pragma unroll
    for (int i = 0; i < 9; ++i) {
      const int ch = (part * 9 + i) * 256 + tid, row = ch / 9, cc = ch - row * 9;
      const int tok = row < 384 ? k0 + row : q0 + row - 384;
      t[i] = ld_sc1(rqk, tok >= 0 ? (unsigned)((tok * 1152 + (row < 384 ? 576 : 0) + hg * 72) * 2 + cc * 16) : OOB);
    }
#pragma unroll
    for (int i = 0; i < 9; ++i) *(v4u*)(smem + ((part * 9 + i) * 256 + tid) * 16) = t[i];
  }
  {
    v4u t[14];
#pragma unroll
    for (int i = 0; i < 14; ++i) {
      const int ch = i * 256 + tid, row = ch / 48, cc = ch - row * 48;
      const int blk = 4 * qb - 8 + (cc >> 2);
      t[i] = ld_sc1(rvt, (row < 72 && blk >= 0) ? (unsigned)((((hg * 72 + row) * 64 + 4 * qb - 8) * 4 + cc) * 16) : OOB);
    }
#pragma unroll
    for (int i = 0; i < 14; ++i) { const int ch = i * 256 + tid; if (ch < 72 * 48) *(v4u*)(smem + LV + (ch / 48) * 784 + (ch % 48) * 16) = t[i]; }
  }
  __syncthreads();
  auto rowfrag = [&](int base, unsigned extra) {
    const unsigned* q = (const unsigned*)(smem + base + li * 144 + wave * 36 + (g < 3 ? g : 0) * 16);
    v4u v = {q[0], q[g < 2 ? 1 : 0], q[g < 2 ? 2 : 0], q[g < 2 ? 3 : 0]};
    if (g >= 2) { v.y = (g == 2) ? extra : 0u; v.z = 0u; v.w = 0u; }
    if (g == 3) v.x = 0u;
    return v;
  };
  v4u kf[24], vf[12][2];
#pragma unroll
  for (int t = 0; t < 24; ++t) kf[t] = rowfrag(LK + t * 16 * 144, (k0 + 16 * t) >= 0 ? 0u : 0xFF00u);
#pragma unroll
  for (int b = 0; b < 12; ++b) {
    vf[b][0] = *(const v4u*)(smem + LV + (wave * 18 + li) * 784 + (b * 4 + g) * 16);
    v4u v = *(const v4u*)(smem + LV + (wave * 18 + 16 + (li & 1)) * 784 + (b * 4 + g) * 16);
    if (li >= 2) v = (v4u){0u, 0u, 0u, 0u};
    if (li == 2) v = (v4u){0x3F803F80u, 0x3F803F80u, 0x3F803F80u, 0x3F803F80u};
    vf[b][1] = v;
  }
  f32x4 cfirst, clast;
#pragma unroll
  for (int r = 0; r < 4; ++r) { cfirst[r] = (4 * g + r > li) ? 0.f : NEG; clast[r] = (4 * g + r <= li) ? 0.f : NEG; }
#define B2_MF "v_mfma_f32_16x16x32_bf16 "
#pragma unroll
  for (int qt = 0; qt < 8; ++qt) {
    const v4u qfr = rowfrag(LQ + qt * 16 * 144, 0x3F80u);
    f32x4 sc[17];
    asm("s_nop 1\n\t" B2_MF "%0, %6, %12, %13\n\t" B2_MF "%1, %7, %12, 0\n\t" B2_MF "%2, %8, %12, 0\n\t" B2_MF "%3, %9, %12, 0\n\t"
        B2_MF "%4, %10, %12, 0\n\t" B2_MF "%5, %11, %12, 0"
        : "=&v"(sc[0]), "=&v"(sc[1]), "=&v"(sc[2]), "=&v"(sc[3]), "=&v"(sc[4]), "=&v"(sc[5])
        : "v"(kf[qt]), "v"(kf[qt + 1]), "v"(kf[qt + 2]), "v"(kf[qt + 3]), "v"(kf[qt + 4]), "v"(kf[qt + 5]), "v"(qfr), "v"(cfirst));
    asm("s_nop 1\n\t" B2_MF "%0, %6, %12, 0\n\t" B2_MF "%1, %7, %12, 0\n\t" B2_MF "%2, %8, %12, 0\n\t" B2_MF "%3, %9, %12, 0\n\t"
        B2_MF "%4, %10, %12, 0\n\t" B2_MF "%5, %11, %12, 0"
        : "=&v"(sc[6]), "=&v"(sc[7]), "=&v"(sc[8]), "=&v"(sc[9]), "=&v"(sc[10]), "=&v"(sc[11])
        : "v"(kf[qt + 6]), "v"(kf[qt + 7]), "v"(kf[qt + 8]), "v"(kf[qt + 9]), "v"(kf[qt + 10]), "v"(kf[qt + 11]), "v"(qfr));
    asm("s_nop 1\n\t" B2_MF "%0, %17, %22, 0\n\t" B2_MF "%1, %18, %22, 0\n\t" B2_MF "%2, %19, %22, 0\n\t" B2_MF "%3, %20, %22, 0\n\t"
        B2_MF "%4, %21, %22, %23\n\ts_nop 7"
        : "=&v"(sc[12]), "=&v"(sc[13]), "=&v"(sc[14]), "=&v"(sc[15]), "=&v"(sc[16]),
          "+v"(sc[0]), "+v"(sc[1]), "+v"(sc[2]), "+v"(sc[3]), "+v"(sc[4]), "+v"(sc[5]), "+v"(sc[6]), "+v"(sc[7]), "+v"(sc[8]), "+v"(sc[9]), "+v"(sc[10]), "+v"(sc[11])
        : "v"(kf[qt + 12]), "v"(kf[qt + 13]), "v"(kf[qt + 14]), "v"(kf[qt + 15]), "v"(kf[qt + 16]), "v"(qfr), "v"(clast));
    float m0 = NEG, m1 = NEG;
#pragma unroll
    for (int tau = 0; tau < 17; ++tau) {
      asm("v_max3_f32 %0, %0, %1, %2" : "+v"(m0) : "v"(sc[tau][0]), "v"(sc[tau][1]));
      asm("v_max3_f32 %0, %0, %1, %2" : "+v"(m1) : "v"(sc[tau][2]), "v"(sc[tau][3]));
    }
    const float m = xmax(amax(m0, m1));
    const float ms = -m * SC;
    unsigned pk[18][2];
#pragma unroll
    for (int t = 0; t < 17; ++t) {
      float e0 = __builtin_amdgcn_exp2f(fmaf(sc[t][0], SC, ms)), e1 = __builtin_amdgcn_exp2f(fmaf(sc[t][1], SC, ms));
      float e2 = __builtin_amdgcn_exp2f(fmaf(sc[t][2], SC, ms)), e3 = __builtin_amdgcn_exp2f(fmaf(sc[t][3], SC, ms));
      pk[t + (qt & 1)][0] = cvtpk(e0, e1); pk[t + (qt & 1)][1] = cvtpk(e2, e3);
    }
    pk[(qt & 1) ? 0 : 17][0] = 0u; pk[(qt & 1) ? 0 : 17][1] = 0u;
    f32x4 o0 = {0, 0, 0, 0}, o1 = {0, 0, 0, 0};
#pragma unroll
    for (int b = 0; b < 9; ++b) {
      v4u pf = {pk[2 * b][0], pk[2 * b][1], pk[2 * b + 1][0], pk[2 * b + 1][1]};
      o0 = mfma16(vf[(qt >> 1) + b][0], pf, o0);
      o1 = mfma16(vf[(qt >> 1) + b][1], pf, o1);
    }
    const float inv = __builtin_amdgcn_rcpf(__shfl(o1[2], li));
    char* orow = smem + 131072 + (16 * qt + li) * 144 + wave * 36;
    *(unsigned*)(orow + 8 * g) = cvtpk(o0[0] * inv, o0[1] * inv);
    *(unsigned*)(orow + 8 * g + 4) = cvtpk(o0[2] * inv, o0[3] * inv);
    if (g == 0) *(unsigned*)(orow + 32) = cvtpk(o1[0] * inv, o1[1] * inv);
  }
  __syncthreads();
#pragma unroll
  for (int i = 0; i < 5; ++i) {
    int ch = tid + 256 * i;
    if (ch < 1152) { int row = ch / 9, cc = ch % 9; st_sc1(*(const v4u*)(smem + 131072 + ch * 16), rob, ((q0 + row) * 576 + hg * 72) * 2 + cc * 16); }
  }
  wg_signal(p.ctrl + C_OB + qb * 32, tid);
}

DEV unsigned mix2(float g, unsigned a, unsigned b) { return cvtpk(fmaf(g, bf2f(a & 0xffffu), bf2f(b & 0xffffu)), fmaf(g, bf2f(a >> 16), bf2f(b >> 16))); }
DEV void fa_item(const Params& p, char* smem, int s, int j, int widx, int wave, int lane, int tid) {
  __amdgpu_buffer_rsrc_t rs = mkrsrc(p.span, SPAN_BYTES);
  __amdgpu_buffer_rsrc_t rab = mkrsrc(p.data + OFF_AB, (unsigned)SZ_OB);
  __amdgpu_buffer_rsrc_t rg = mkrsrc(p.data + OFF_GATE, SZ_GATE);
  const int g = lane >> 4, li = lane & 15;
  const int nt = j * 4 + wave;
  if (wave == 0) {
    const unsigned w4 = aload(p.ctrl + (lane == 0 ? C_UX + s : lane == 1 ? C_ST + s * 9 + j : lane == 2 ? C_PROG + (widx & 15) * 32 : C_POL));
    const unsigned ux = __builtin_amdgcn_readlane(w4, 0), st0 = __builtin_amdgcn_readlane(w4, 1), pg = __builtin_amdgcn_readlane(w4, 2);
    unsigned pl = __builtin_amdgcn_readlane(w4, 3);
    if (ux < 32u || !(pl & 0x1000u)) {
      wait_ge(p.ctrl + C_UX + s, 32u, p.ctrl + C_TMO, 19u);
      wait_ge(p.ctrl + C_POL, 0x1000u, p.ctrl + C_TMO, 9u);
      pl = __builtin_amdgcn_readfirstlane(aload(p.ctrl + C_POL));
    }
    __builtin_amdgcn_fence(__ATOMIC_ACQUIRE, "wavefront");
    if (lane == 0) *(LAS unsigned*)(smem + 163808) = (st0 ? 1u : 0u) | ((s < 60 && pg >= (unsigned)s + 1u) ? 2u : 0u) | ((par_r(pl & 0xfffu) ^ 1u) << 2);
  }
  __syncthreads();
  const unsigned stw = __builtin_amdgcn_readfirstlane(*(volatile LAS unsigned*)(smem + 163808));
  const bool bready = stw & 1u;
  const unsigned pw = (stw >> 2) & 1u;
  v4u ux[2][16], um[2][4];
  v2u bp[2] = {{0u, 0u}, {0u, 0u}};
  float gt[2] = {1.f, 1.f};
  auto ldux = [&] {
#pragma unroll
    for (int sl = 0; sl < 2; ++sl)
#pragma unroll
      for (int pp = 0; pp < 16; ++pp) ux[sl][pp] = ld_sc1(rs, sp_off(LW_UX + ((s * 72 + nt * 2 + sl) * 16 + (pp & 8)) * 1024, pw) + (pp & 7) * 1024 + lane * 16);
  };
  auto ldb = [&] {
    v4u b4 = ld_sc1(rab, ((s * 36 + nt) * 64 + lane) * 16);
    bp[0] = (v2u){b4.x, b4.y}; bp[1] = (v2u){b4.z, b4.w};
#pragma unroll
    for (int qt = 0; qt < 2; ++qt) gt[qt] = __uint_as_float(ld1_sc1(rg, j * 8192 + (s * 32 + qt * 16 + li) * 4));
  };
  if (bready) ldb();
  if (!(stw & 2u)) {
    ldux();
    if (wave == 1) {
      wait_slow(p.ctrl + C_PROG + (widx & 15) * 32, umin((unsigned)s + 1u, 60u), p.ctrl + C_TMO, 17u);
      if (s >= 60) {
        wait_flags(p.ctrl + C_PX, 32, 32, (unsigned)s + 1u, lane, p.ctrl + C_TMO, 11u);
        wait_flags(p.ctrl + C_UM, NC, 32, (unsigned)s + 1u, lane, p.ctrl + C_TMO, 12u);
      }
    }
    __syncthreads();
  }
  {
    const unsigned lb = lds_addr(smem) + wave * 1024;
#pragma unroll
    for (int i = 0; i < 20; ++i) dma16_sc1(p.span + sp_off(LW_PX + s * 81920 + (i >> 1) * 8192, pw) + ((i & 1) * 256 + tid) * 16, lb + i * 4096);
#pragma unroll
    for (int sl = 0; sl < 2; ++sl)
#pragma unroll
      for (int mm = 0; mm < 4; ++mm) um[sl][mm] = ld_sc1(rs, sp_off(LW_UM + (s * 72 + nt * 2 + sl) * 4096, pw) + mm * 1024 + lane * 16);
    if (stw & 2u) ldux();
    drain();
  }
  __syncthreads();
  float av[2][4];
#pragma unroll
  for (int qt = 0; qt < 2; ++qt) {
    f32x4 aA[2] = {{0, 0, 0, 0}, {0, 0, 0, 0}}, aB[2] = {{0, 0, 0, 0}, {0, 0, 0, 0}};
    v4u pe[3], po[3];
    auto ldp = [&](int i) {
      const char* b = (i < 16) ? smem + (qt * 32 + 2 * i) * 1024 + lane * 16 : smem + 65536 + (qt * 8 + 2 * (i - 16)) * 1024 + lane * 16;
      pe[i % 3] = *(const v4u*)b; po[i % 3] = *(const v4u*)(b + 1024);
    };
    ldp(0); ldp(1);
#pragma unroll
    for (int i = 0; i < 20; ++i) {
      if (i + 2 < 20) ldp(i + 2);
      __builtin_amdgcn_sched_barrier(0);
#pragma unroll
      for (int sl = 0; sl < 2; ++sl) {
        v4u u = (i < 16) ? ux[sl][i & 15] : um[sl][i & 3];
        aA[sl] = mfma16(u, pe[i % 3], aA[sl]); aB[sl] = mfma16(u, po[i % 3], aB[sl]);
      }
      __builtin_amdgcn_sched_barrier(0);
    }
#pragma unroll
    for (int r = 0; r < 4; ++r) {
      auto sw = __builtin_amdgcn_permlane32_swap(__float_as_uint(aB[0][r]), __float_as_uint(aA[1][r]), false, false);
      av[qt][r] = ((lane < 32) ? aA[0][r] : aB[1][r]) + ((lane < 32) ? __uint_as_float(sw[1]) : __uint_as_float(sw[0]));
    }
  }
  bool mix = bready;
  if (!bready) {
    __amdgpu_buffer_rsrc_t ra2 = mkrsrc(p.data + OFF_A2, (unsigned)SZ_OB);
    st_sc1((v4u){cvtpk(av[0][0], av[0][1]), cvtpk(av[0][2], av[0][3]), cvtpk(av[1][0], av[1][1]), cvtpk(av[1][2], av[1][3])}, ra2, ((s * 36 + nt) * 64 + lane) * 16);
    drain();
    __syncthreads();
    if (tid == 0) *(LAS unsigned*)(smem + 163808) = __hip_atomic_fetch_add(p.ctrl + C_ST + s * 9 + j, 1u, __ATOMIC_RELAXED, __HIP_MEMORY_SCOPE_AGENT);
    __syncthreads();
    mix = __builtin_amdgcn_readfirstlane(*(volatile LAS unsigned*)(smem + 163808)) != 0;
    __builtin_amdgcn_fence(__ATOMIC_ACQUIRE, "wavefront");
    if (mix) ldb();
  }
  if (mix) {
#pragma unroll
    for (int qt = 0; qt < 2; ++qt) {
      const unsigned a01 = cvtpk(av[qt][0], av[qt][1]), a23 = cvtpk(av[qt][2], av[qt][3]);
      v2u ov = {mix2(gt[qt], a01, bp[qt].x), mix2(gt[qt], a23, bp[qt].y)};
      *(v2u*)(p.out + (size_t)(s * 32 + qt * 16 + li) * 576 + nt * 16 + 4 * g) = ov;
    }
  }
  __syncthreads();
}

constexpr int SFINE = 61;
DEV void fa_fine(const Params& p, char* smem, int s, int nt, int widx, int wave, int lane, int tid) {
  __amdgpu_buffer_rsrc_t rs = mkrsrc(p.span, SPAN_BYTES);
  __amdgpu_buffer_rsrc_t rab = mkrsrc(p.data + OFF_AB, (unsigned)SZ_OB);
  __amdgpu_buffer_rsrc_t rg = mkrsrc(p.data + OFF_GATE, SZ_GATE);
  const int g = lane >> 4, li = lane & 15;
  if (wave == 0) {
    wait_ge(p.ctrl + C_UX + s, 32u, p.ctrl + C_TMO, 19u);
    wait_ge(p.ctrl + C_FB + (s >> 2), 9u, p.ctrl + C_TMO, 20u);
  }
  __syncthreads();
  const unsigned pw = par_r(read_pol(p)) ^ 1u;
  v4u ux[2][4];
#pragma unroll
  for (int sl = 0; sl < 2; ++sl)
#pragma unroll
    for (int q = 0; q < 4; ++q) ux[sl][q] = ld_sc1(rs, sp_off(LW_UX + ((s * 72 + nt * 2 + sl) * 16 + wave * 4) * 1024, pw) + q * 1024 + lane * 16);
  const v4u b4 = ld_sc1(rab, ((s * 36 + nt) * 64 + lane) * 16);
  const float gt = __uint_as_float(ld1_sc1(rg, (nt >> 2) * 8192 + (s * 32 + (wave & 1) * 16 + li) * 4));
  if (wave == 1) {
    wait_slow(p.ctrl + C_PROG + (widx & 15) * 32, 60u, p.ctrl + C_TMO, 17u);
    wait_flags(p.ctrl + C_UM, NC, 32, (unsigned)s + 1u, lane, p.ctrl + C_TMO, 12u);
    wait_flags(p.ctrl + C_PX, 32, 32, (unsigned)s + 1u, lane, p.ctrl + C_TMO, 11u);
  }
  __syncthreads();
  v4u px[2][10], um[2];
#pragma unroll
  for (int qt = 0; qt < 2; ++qt) {
#pragma unroll
    for (int i = 0; i < 8; ++i) px[qt][i] = ld_sc1(rs, sp_off(LW_PX + s * 81920 + (qt * 32 + wave * 8) * 1024, pw) + i * 1024 + lane * 16);
#pragma unroll
    for (int i = 0; i < 2; ++i) px[qt][8 + i] = ld_sc1(rs, sp_off(LW_PX + s * 81920 + 65536 + (qt * 8 + wave * 2) * 1024, pw) + i * 1024 + lane * 16);
  }
#pragma unroll
  for (int sl = 0; sl < 2; ++sl) um[sl] = ld_sc1(rs, sp_off(LW_UM + (s * 72 + nt * 2 + sl) * 4096, pw) + wave * 1024 + lane * 16);
#pragma unroll
  for (int qt = 0; qt < 2; ++qt) {
    f32x4 aA[2] = {{0, 0, 0, 0}, {0, 0, 0, 0}}, aB[2] = {{0, 0, 0, 0}, {0, 0, 0, 0}};
#pragma unroll
    for (int i = 0; i < 5; ++i)
#pragma unroll
      for (int sl = 0; sl < 2; ++sl) {
        v4u u = (i < 4) ? ux[sl][i & 3] : um[sl];
        aA[sl] = mfma16(u, px[qt][2 * i], aA[sl]); aB[sl] = mfma16(u, px[qt][2 * i + 1], aB[sl]);
      }
    f32x4 a4;
#pragma unroll
    for (int r = 0; r < 4; ++r) {
      auto sw = __builtin_amdgcn_permlane32_swap(__float_as_uint(aB[0][r]), __float_as_uint(aA[1][r]), false, false);
      a4[r] = ((lane < 32) ? aA[0][r] : aB[1][r]) + ((lane < 32) ? __uint_as_float(sw[1]) : __uint_as_float(sw[0]));
    }
    *(f32x4*)(smem + (qt * 4 + wave) * 1024 + lane * 16) = a4;
  }
  __syncthreads();
  if (wave < 2) {
    const char* q = smem + wave * 4096 + lane * 16;
    f32x4 a = (*(const f32x4*)q + *(const f32x4*)(q + 1024)) + (*(const f32x4*)(q + 2048) + *(const f32x4*)(q + 3072));
    const unsigned b0 = wave ? b4.z : b4.x, b1 = wave ? b4.w : b4.y;
    v2u ov = {cvtpk(gt * a[0] + bf2f(b0 & 0xffffu), gt * a[1] + bf2f(b0 >> 16)), cvtpk(gt * a[2] + bf2f(b1 & 0xffffu), gt * a[3] + bf2f(b1 >> 16))};
    *(v2u*)(p.out + (size_t)(s * 32 + wave * 16 + li) * 576 + nt * 16 + 4 * g) = ov;
  }
}

DEV void fb_item(const Params& p, char* smem, int j, int q, int wave, int lane, int tid) {
  __amdgpu_buffer_rsrc_t rob = mkrsrc(p.data + OFF_OB, (unsigned)SZ_OB);
  __amdgpu_buffer_rsrc_t rab = mkrsrc(p.data + OFF_AB, (unsigned)SZ_OB);
  __amdgpu_buffer_rsrc_t rg = mkrsrc(p.data + OFF_GATE, SZ_GATE);
  const int g = lane >> 4, li = lane & 15;
  const int nt = j * 4 + wave;
  v4u wf[1][18];
  load_kfrags<1>(smem, wave, lane, tid, [&](int row, int cc) { return p.wo + (size_t)row * 576 + j * 64 + cc * 8; }, wf);
  v4u gv[9];
#pragma unroll
  for (int i = 0; i < 9; ++i) gv[i] = *(const v4u*)(p.gw + (i * 8 + (lane & 7)) * 8);
  wg_wait_ge(p.ctrl + C_OB + q * 32, 8u, p.ctrl + C_TMO, 10u, wave);
  v4u v[9], xv[9];
  auto issue = [&](int s) {
#pragma unroll
    for (int i = 0; i < 9; ++i) v[i] = ld_sc1(rob, (s * 32 * 576 + tile_src_off(wave, lane) + i * 64) * 2);
#pragma unroll
    for (int i = 0; i < 9; ++i) xv[i] = *(const v4u*)(p.x + (size_t)(s * 32 + wave * 8 + (lane >> 3)) * 576 + (i * 8 + (lane & 7)) * 8);
  };
  issue(4 * q);
  for (int it = 0; it < 4; ++it) {
    const int s = 4 * q + it;
    char* buf = smem + (it & 1) * 40960;
#pragma unroll
    for (int i = 0; i < 9; ++i) *(v4u*)(buf + (i * 4 + wave) * 1024 + lane * 16) = v[i];
    {
      float a = 0;
#pragma unroll
      for (int i = 0; i < 9; ++i)
#pragma unroll
        for (int e = 0; e < 4; ++e) {
          a += bf2f(xv[i][e] & 0xffffu) * bf2f(gv[i][e] & 0xffffu);
          a += bf2f(xv[i][e] >> 16) * bf2f(gv[i][e] >> 16);
        }
      a += __shfl_xor(a, 1); a += __shfl_xor(a, 2); a += __shfl_xor(a, 4);
      const float gs = 1.f / (1.f + __expf(-a));
      if ((lane & 7) == 0) {
        *(float*)(buf + 36864 + (wave * 8 + (lane >> 3)) * 4) = gs;
      }
    }
    __syncthreads();
    if (it + 1 < 4) issue(s + 1);
    f32x4 bacc[2] = {{0, 0, 0, 0}, {0, 0, 0, 0}};
    xloop<3>(buf, lane, [&](int ks, v4u b0, v4u b1) { bacc[0] = mfma16(wf[0][ks], b0, bacc[0]); bacc[1] = mfma16(wf[0][ks], b1, bacc[1]); }, [&](int) {});
    {
      const float g0 = *(const float*)(buf + 36864 + li * 4), g1 = *(const float*)(buf + 36864 + (16 + li) * 4);
      const float o0 = 1.f - g0, o1 = 1.f - g1;
      const v4u bb = {cvtpk(o0 * bacc[0][0], o0 * bacc[0][1]), cvtpk(o0 * bacc[0][2], o0 * bacc[0][3]),
                      cvtpk(o1 * bacc[1][0], o1 * bacc[1][1]), cvtpk(o1 * bacc[1][2], o1 * bacc[1][3])};
      st_sc1(bb, rab, ((s * 36 + nt) * 64 + lane) * 16);
      if (wave == 0 && lane < 8) st_sc1(*(const v4u*)(buf + 36864 + lane * 16), rg, j * 8192 + s * 128 + lane * 16);
      *(v4u*)(smem + 98304 + (it * 4 + wave) * 1024 + lane * 16) = bb;
      *(f32x2*)(smem + 114688 + ((it * 4 + wave) * 64 + lane) * 8) = (f32x2){g0, g1};
    }
  }
  drain();
  __syncthreads();
  if (tid == 0) aadd(p.ctrl + C_FB + q, 1u);
  if (tid < 4 && 4 * q + tid < SFINE)
    *(LAS unsigned*)(smem + 163792 + tid * 4) = __hip_atomic_fetch_add(p.ctrl + C_ST + (4 * q + tid) * 9 + j, 1u, __ATOMIC_RELAXED, __HIP_MEMORY_SCOPE_AGENT);
  else if (tid < 4) *(LAS unsigned*)(smem + 163792 + tid * 4) = 0u;
  __syncthreads();
  __amdgpu_buffer_rsrc_t ra2 = mkrsrc(p.data + OFF_A2, (unsigned)SZ_OB);
#pragma unroll 1
  for (int it = 0; it < 4; ++it) {
    if (__builtin_amdgcn_readfirstlane(*(volatile LAS unsigned*)(smem + 163792 + it * 4)) == 0) continue;
    __builtin_amdgcn_fence(__ATOMIC_ACQUIRE, "wavefront");
    const int s = 4 * q + it;
    const v4u a4 = ld_sc1(ra2, ((s * 36 + nt) * 64 + lane) * 16);
    const v4u bb = *(const v4u*)(smem + 98304 + (it * 4 + wave) * 1024 + lane * 16);
    const f32x2 gg = *(const f32x2*)(smem + 114688 + ((it * 4 + wave) * 64 + lane) * 8);
#pragma unroll
    for (int qt = 0; qt < 2; ++qt) {
      const unsigned a01 = a4[2 * qt], a23 = a4[2 * qt + 1], b01 = bb[2 * qt], b23 = bb[2 * qt + 1];
      v2u ov = {mix2(gg[qt], a01, b01), mix2(gg[qt], a23, b23)};
      *(v2u*)(p.out + (size_t)(s * 32 + qt * 16 + li) * 576 + nt * 16 + 4 * g) = ov;
    }
  }
}

DEV int take(const Params& p, char* smem, int k, int n, int tid) {
  LAS int* cmd = (LAS int*)(smem + 163824);
  __syncthreads();
  if (tid == 0) {
    int c = -1;
    if (aload(p.ctrl + C_FQ + k * 32) < (unsigned)n && !aload(p.ctrl + C_TMO)) {
      unsigned t = __hip_atomic_fetch_add(p.ctrl + C_FQ + k * 32, 1u, __ATOMIC_RELAXED, __HIP_MEMORY_SCOPE_AGENT);
      if (t < (unsigned)n) c = (int)t;
    }
    *cmd = c;
  }
  __syncthreads();
  return __builtin_amdgcn_readfirstlane(*(volatile LAS int*)cmd);
}

DEV void worker_role(const Params& p, char* smem, int widx, int wave, int lane, int tid) {
  const int slab = ((widx >> 3) << 2) | (widx & 3), par = (widx >> 2) & 1;
  if (slab < 32) vu_worker(p, smem, slab, par, wave, lane, tid);
  else if (par == 0) kq_worker(p, smem, slab - 32, wave, lane, tid);
  else b1_worker(p, smem, slab - 32, wave, lane, tid);
  DSTAMP(widx == 0 && wave == 0, 10);
  const bool faonly = slab < 32 && par == 0;
  for (int c; !faonly && (c = take(p, smem, 0, 128, tid)) >= 0;) {
    wg_wait_ge(p.ctrl + C_B1, 32u, p.ctrl + C_TMO, 13u, wave);
    b2_item(p, smem, c & 7, c >> 3, wave, lane, tid);
    DSTAMP(wave == 0 && (c & 31) == 31, 12 + (c >> 5));
  }
  for (int c; !faonly && (c = take(p, smem, 1, 144, tid)) >= 0;) {
    fb_item(p, smem, c % 9, c / 9, wave, lane, tid);
    DSTAMP(wave == 0 && (c % 36) == 35, 33 + c / 36);
  }
  for (int c; (c = take(p, smem, 2, SFINE * 9, tid)) >= 0;) {
    fa_item(p, smem, c / 9, c % 9, widx, wave, lane, tid);
    DSTAMP(wave == 0 && (c % 9) == 0, 448 + c / 9);
  }
  for (int c; (c = take(p, smem, 3, (NSEG - SFINE) * 36, tid)) >= 0;) {
    fa_fine(p, smem, SFINE + c / 36, c % 36, widx, wave, lane, tid);
    DSTAMP(wave == 0 && (c % 36) == 0, 448 + SFINE + c / 36);
  }
}

__global__ void __launch_bounds__(256, 1) fused_kernel(Params p) {
  extern __shared__ __attribute__((aligned(16))) char smem[];
  const int tid = threadIdx.x, lane = tid & 63, wave = __builtin_amdgcn_readfirstlane(tid >> 6);
  const int b = blockIdx.x;
  const int idx = (b >> 3) * 4 + (b & 3);
  {
    unsigned long long pc;
    asm volatile("s_getpc_b64 %0" : "=s"(pc));
    const char* cb = (const char*)(pc & ~255ull);
    v4u c0 = *(const v4u*)(cb + ((b >> 3) * 320 + tid) * 16);
    v4u c1 = *(const v4u*)(cb + ((b >> 3) * 320 + 256 + (tid & 63)) * 16);
    asm volatile("" :: "v"(c0), "v"(c1));
  }
  DSTAMP(wave == 0, 1024 + b);
  if ((b & 7) < 4) {
    if (idx < NH) head_role(p, smem, idx, wave, lane, tid);
    else if (idx < NH + NC) col_role(p, smem, idx - NH, wave, lane, tid);
    else spare_role(p, smem, idx - NH - NC, wave, lane, tid);
  } else {
    worker_role(p, smem, idx, wave, lane, tid);
  }
  DSTAMP(wave == 0, 1280 + b);
}

__global__ void __launch_bounds__(512) prep_kernel(Params p) {
  __shared__ unsigned sm[16];
  const int tid = threadIdx.x, lane = tid & 63, wave = __builtin_amdgcn_readfirstlane(tid >> 6), b = blockIdx.x;
  if (b >= NPAGES) {
    ((v4u*)p.ctrl)[(b - NPAGES) * 512 + tid] = (v4u){0u, 0u, 0u, 0u};
    return;
  }
  __amdgpu_buffer_rsrc_t rm = mkrsrc(p.mbox, MBOX_BYTES);
  unsigned acc = 0, mn[2];
  const unsigned off = b * (2u << 20) + (1u << 20) + wave * 16384;
  acc += ld1_sc1(rm, off + 128);
  drain();
  asm volatile("" :: "v"(acc));
#pragma unroll
  for (int q = 0; q < 2; ++q) {
    const int hsel = (q ^ wave) & 1;
    unsigned long long t0 = clk();
    acc += ld1_sc1(rm, off + hsel * 8192 + 512);
    drain();
    asm volatile("" :: "v"(acc));
    mn[q] = (unsigned)(clk() - t0);
  }
  if (lane == 0) { sm[wave * 2 + (wave & 1)] = mn[0]; sm[wave * 2 + ((wave & 1) ^ 1)] = mn[1]; }
  __syncthreads();
  if (tid == 0) {
    unsigned a0 = ~0u, a1 = ~0u;
    for (int i = 0; i < 8; ++i) { a0 = umin(a0, sm[2 * i]); a1 = umin(a1, sm[2 * i + 1]); }
    const unsigned grp = (__builtin_amdgcn_s_getreg(20 | (3 << 11)) >> 2) & 1u;
    p.ctrl[C_CAL + b] = (a1 < a0 ? 1u : 0u) | (grp << 1);
  }
}

}

extern "C" void kernel_launch(void* const* d_in, const int* in_sizes, int n_in, void* d_out, int out_size, void* d_ws,
                              size_t ws_size, hipStream_t stream) {
  (void)in_sizes; (void)n_in; (void)out_size; (void)ws_size;
  Params p;
  p.x = (const u16*)d_in[0]; p.mem0 = (const u16*)d_in[1];
  p.Wq = (const u16*)d_in[2]; p.Wk = (const u16*)d_in[3]; p.Wv = (const u16*)d_in[4]; p.Wo = (const u16*)d_in[5];
  p.wqkv = (const u16*)d_in[8]; p.wo = (const u16*)d_in[9]; p.gw = (const u16*)d_in[10];
  p.out = (u16*)d_out;
  char* ws = (char*)d_ws;
  p.ctrl = (unsigned*)ws;
  size_t a = ((size_t)ws + CTRL_BYTES + (2u << 20) - 1) & ~(size_t)((2u << 20) - 1);
  p.mbox = (char*)a;
  p.span = p.mbox + MBOX_BYTES;
  p.data = p.span + SPAN_BYTES;
  hipFuncSetAttribute((const void*)fused_kernel, hipFuncAttributeMaxDynamicSharedMemorySize, LDS_BYTES);
  prep_kernel<<<NPAGES + 12, 512, 0, stream>>>(p);
  fused_kernel<<<256, 256, LDS_BYTES, stream>>>(p);
}
```
